```python
import torch

def _softmax(x, axis=-1):
    return torch.softmax(x, dim=axis)
def _gelu(x):
    return torch.nn.functional.gelu(x, approximate="tanh")
def _silu(x):
    return torch.nn.functional.silu(x)
def _sigmoid(x):
    return torch.sigmoid(x)
def _layernorm(x, axis=-1, eps=1e-5):
    m = x.mean(dim=axis, keepdim=True)
    v = x.var(dim=axis, keepdim=True, unbiased=False)
    return (x - m) / torch.sqrt(v + eps)
def _scatter_add(tgt, idx, src, axis=0):
    return tgt.clone().index_add_(axis, idx, src)
def _segment_sum(src, seg, n):
    out = torch.zeros((n,) + src.shape[1:], dtype=src.dtype)
    return out.index_add_(0, seg, src)
def _pad(x, pw):
    flat = tuple(v for pair in reversed(pw) for v in pair)
    return torch.nn.functional.pad(x, flat)

kernel_name = 'interleave_layers__linear_kernel_hedgehog__geglu_ffn__prompt'

def setup_inputs():

    _w = (lambda a: (lambda t: t.to(torch.bfloat16) if t.is_floating_point() else t)(torch.as_tensor(a)))
    def _A_setup():
        B, S, D, H = 1, 2048, 512, 32
        Dh = D // H
        return {
            "x": _w((lambda *s: torch.randn(*s, dtype=torch.bfloat16) * 0.05)(B, S, D)),
            "wqkv": _w((lambda *s: torch.randn(*s, dtype=torch.bfloat16) * 0.2)(D, 3 * D)),
            "w_phi": _w((lambda *s: torch.randn(*s, dtype=torch.bfloat16) * 0.2)(H, Dh, Dh) * (Dh ** -0.5)),
            "wo": _w((lambda *s: torch.randn(*s, dtype=torch.bfloat16) * 0.05)(D, D)),
        }
    _A_in = _A_setup()

    def _B_setup():
        B, S, D, F = 1, 2048, 512, 1376
        return {
            "x": _w((lambda *s: torch.randn(*s, dtype=torch.bfloat16) * 0.05)(B, S, D)),
            "w_gate": _w((lambda *s: torch.randn(*s, dtype=torch.bfloat16) * 0.05)(D, F)),
            "w_up": _w((lambda *s: torch.randn(*s, dtype=torch.bfloat16) * 0.05)(D, F)),
            "w_down": _w((lambda *s: torch.randn(*s, dtype=torch.bfloat16) * 0.05)(F, D)),
        }
    _B_in = _B_setup()

    return {
        "A_x": _A_in["x"],
        "A_wqkv": _A_in["wqkv"],
        "A_w_phi": _A_in["w_phi"],
        "A_wo": _A_in["wo"],
        "B_x": _B_in["x"],
        "B_w_gate": _B_in["w_gate"],
        "B_w_up": _B_in["w_up"],
        "B_w_down": _B_in["w_down"],
    }

def reference(A_x, A_wqkv, A_w_phi, A_wo, B_x, B_w_gate, B_w_up, B_w_down):
    def _A_ref(x, wqkv, w_phi, wo):
        B, S, D = x.shape
        H = 32; Dh = D // H
        qkv = (lambda x, *d: x.permute(*d))((x @ wqkv).reshape(B, S, 3, H, Dh), 2, 0, 3, 1, 4)
        q, k, v = qkv[0], qkv[1], qkv[2]
        pq = torch.einsum("bhsd,hdf->bhsf", q, w_phi)
        pk = torch.einsum("bhsd,hdf->bhsf", k, w_phi)
        phi_q = torch.concatenate([_softmax(pq, axis=-1), _softmax(-pq, axis=-1)], axis=-1)
        phi_k = torch.concatenate([_softmax(pk, axis=-1), _softmax(-pk, axis=-1)], axis=-1)
        kv = torch.einsum("bhsf,bhsd->bhfd", phi_k, v)
        z  = torch.einsum("bhsf->bhf", phi_k)
        num = torch.einsum("bhqf,bhfd->bhqd", phi_q, kv)
        den = torch.einsum("bhqf,bhf->bhq", phi_q, z)[..., None] + 1e-6
        o = num / den
        return (lambda x, *d: x.permute(*d))(o, 0, 2, 1, 3).reshape(B, S, D) @ wo

    def _B_ref(x, w_gate, w_up, w_down):
        return (_gelu(x @ w_gate) * (x @ w_up)) @ w_down

    x = A_x
    x = x + _A_ref(_layernorm(x, axis=-1), A_wqkv, A_w_phi, A_wo)
    x = x + _B_ref(_layernorm(x, axis=-1), B_w_gate, B_w_up, B_w_down)
    x = x + _A_ref(_layernorm(x, axis=-1), A_wqkv, A_w_phi, A_wo)
    x = x + _B_ref(_layernorm(x, axis=-1), B_w_gate, B_w_up, B_w_down)
    return x
```

```cpp
#include <hip/hip_runtime.h>
#include <hip/hip_bf16.h>
#include <stdint.h>
typedef __attribute__((ext_vector_type(8))) short bf16x8;
typedef __attribute__((ext_vector_type(4))) float f32x4;
typedef unsigned short u16;
constexpr int S = 2048, D = 512, H = 32, DH = 16, F = 1376;
__device__ __forceinline__ float bf2f(u16 b) { return __uint_as_float((unsigned)b << 16); }
__device__ __forceinline__ u16 f2bf(float f) { __hip_bfloat16 h = __float2bfloat16(f); return *(u16*)&h; }

__global__ void k_ln(const u16* x, u16* xn) {
  int row = blockIdx.x * 4 + (threadIdx.x >> 6), lane = threadIdx.x & 63;
  bf16x8 v = *(const bf16x8*)(x + row * D + lane * 8);
  float f[8], s = 0;
  for (int j = 0; j < 8; ++j) { f[j] = bf2f((u16)v[j]); s += f[j]; }
  for (int o = 32; o; o >>= 1) s += __shfl_xor(s, o);
  float m = s / D, q = 0;
  for (int j = 0; j < 8; ++j) { f[j] -= m; q += f[j] * f[j]; }
  for (int o = 32; o; o >>= 1) q += __shfl_xor(q, o);
  float r = rsqrtf(q / D + 1e-5f);
  bf16x8 o8;
  for (int j = 0; j < 8; ++j) o8[j] = (short)f2bf(f[j] * r);
  *(bf16x8*)(xn + row * D + lane * 8) = o8;
}

template <int MODE>
__global__ __launch_bounds__(256) void k_gemm(const u16* A, const u16* W, const u16* W2, const u16* res, u16* C, int N, int K) {
  __shared__ u16 As[64][40];
  __shared__ u16 Ws[2][64][40];
  int tid = threadIdx.x, lane = tid & 63, w = tid >> 6;
  int m0 = blockIdx.y * 64, n0 = blockIdx.x * 64;
  int wm = (w >> 1) * 32, wn = (w & 1) * 32;
  f32x4 acc[2][2][2] = {};
  for (int k0 = 0; k0 < K; k0 += 32) {
    { int r = tid >> 2, c = (tid & 3) * 8;
      *(bf16x8*)&As[r][c] = *(const bf16x8*)(A + (size_t)(m0 + r) * K + k0 + c); }
    { int kk = tid >> 3, c = (tid & 7) * 8;
      bf16x8 v = {};
      if (n0 + c < N) v = *(const bf16x8*)(W + (size_t)(k0 + kk) * N + n0 + c);
      for (int j = 0; j < 8; ++j) Ws[0][c + j][kk] = (u16)v[j];
      if (MODE == 2) {
        bf16x8 v2 = {};
        if (n0 + c < N) v2 = *(const bf16x8*)(W2 + (size_t)(k0 + kk) * N + n0 + c);
        for (int j = 0; j < 8; ++j) Ws[1][c + j][kk] = (u16)v2[j];
      } }
    __syncthreads();
    bf16x8 a[2], b[2][2];
    for (int i = 0; i < 2; ++i) a[i] = *(const bf16x8*)&As[wm + i * 16 + (lane & 15)][(lane >> 4) * 8];
    for (int i = 0; i < 2; ++i) b[0][i] = *(const bf16x8*)&Ws[0][wn + i * 16 + (lane & 15)][(lane >> 4) * 8];
    if (MODE == 2) for (int i = 0; i < 2; ++i) b[1][i] = *(const bf16x8*)&Ws[1][wn + i * 16 + (lane & 15)][(lane >> 4) * 8];
    for (int i = 0; i < 2; ++i) for (int j = 0; j < 2; ++j) {
      acc[0][i][j] = __builtin_amdgcn_mfma_f32_16x16x32_bf16(a[i], b[0][j], acc[0][i][j], 0, 0, 0);
      if (MODE == 2) acc[1][i][j] = __builtin_amdgcn_mfma_f32_16x16x32_bf16(a[i], b[1][j], acc[1][i][j], 0, 0, 0);
    }
    __syncthreads();
  }
  for (int i = 0; i < 2; ++i) for (int j = 0; j < 2; ++j) for (int r = 0; r < 4; ++r) {
    int m = m0 + wm + i * 16 + (lane >> 4) * 4 + r, n = n0 + wn + j * 16 + (lane & 15);
    if (n >= N) continue;
    float v = acc[0][i][j][r];
    if (MODE == 1) v += bf2f(res[(size_t)m * N + n]);
    if (MODE == 2) { float g = v, u = acc[1][i][j][r];
      float t = tanhf(0.7978845608028654f * (g + 0.044715f * g * g * g)); v = 0.5f * g * (1.f + t) * u; }
    C[(size_t)m * N + n] = f2bf(v);
  }
}

__device__ __forceinline__ void phi(const float* q, const float* wp, float* o) {
  float p[16], mx = -1e30f, mn = 1e30f;
  for (int f = 0; f < 16; ++f) { float s = 0; for (int d = 0; d < 16; ++d) s += q[d] * wp[d * 16 + f]; p[f] = s; mx = fmaxf(mx, s); mn = fminf(mn, s); }
  float s1 = 0, s2 = 0;
  for (int f = 0; f < 16; ++f) { o[f] = __expf(p[f] - mx); s1 += o[f]; o[16 + f] = __expf(mn - p[f]); s2 += o[16 + f]; }
  for (int f = 0; f < 16; ++f) { o[f] /= s1; o[16 + f] /= s2; }
}
__global__ __launch_bounds__(256) void k_kv(const u16* qkv, const u16* wphi, float* part) {
  __shared__ float wp[256]; __shared__ float ph[256][33]; __shared__ float vv[256][17];
  int h = blockIdx.x, t = blockIdx.y * 256 + threadIdx.x;
  wp[threadIdx.x] = bf2f(wphi[h * 256 + threadIdx.x]);
  __syncthreads();
  float k[16], o[32];
  for (int d = 0; d < 16; ++d) { k[d] = bf2f(qkv[(size_t)t * 1536 + 512 + h * 16 + d]); vv[threadIdx.x][d] = bf2f(qkv[(size_t)t * 1536 + 1024 + h * 16 + d]); }
  vv[threadIdx.x][16] = 1.f;
  phi(k, wp, o);
  for (int f = 0; f < 32; ++f) ph[threadIdx.x][f] = o[f];
  __syncthreads();
  for (int i = threadIdx.x; i < 32 * 17; i += 256) { int f = i / 17, d = i % 17; float s = 0;
    for (int tt = 0; tt < 256; ++tt) s += ph[tt][f] * vv[tt][d];
    part[(blockIdx.y * H + h) * 544 + i] = s; }
}
__global__ __launch_bounds__(256) void k_o(const u16* qkv, const u16* wphi, const float* part, u16* o_out) {
  __shared__ float wp[256]; __shared__ float kv[544];
  int h = blockIdx.x, t = blockIdx.y * 256 + threadIdx.x;
  wp[threadIdx.x] = bf2f(wphi[h * 256 + threadIdx.x]);
  for (int i = threadIdx.x; i < 544; i += 256) { float s = 0; for (int p = 0; p < 8; ++p) s += part[(p * H + h) * 544 + i]; kv[i] = s; }
  __syncthreads();
  float q[16], o[32];
  for (int d = 0; d < 16; ++d) q[d] = bf2f(qkv[(size_t)t * 1536 + h * 16 + d]);
  phi(q, wp, o);
  float den = 1e-6f; for (int f = 0; f < 32; ++f) den += o[f] * kv[f * 17 + 16];
  for (int d = 0; d < 16; ++d) { float s = 0; for (int f = 0; f < 32; ++f) s += o[f] * kv[f * 17 + d]; o_out[(size_t)t * D + h * 16 + d] = f2bf(s / den); }
}
__global__ void k_empty(int* p) { if (p == nullptr) *p = 0; }

extern "C" void kernel_launch(void* const* d_in, const int* in_sizes, int n_in,
                              void* d_out, int out_size, void* d_ws, size_t ws_size,
                              hipStream_t stream) {
  const u16* x = (const u16*)d_in[0]; const u16* wqkv = (const u16*)d_in[1]; const u16* wphi = (const u16*)d_in[2];
  const u16* wo = (const u16*)d_in[3]; const u16* wg = (const u16*)d_in[5]; const u16* wu = (const u16*)d_in[6]; const u16* wd = (const u16*)d_in[7];
  char* ws = (char*)d_ws + (1 << 20);
  u16* xn = (u16*)ws; u16* qkv = (u16*)(ws + (4 << 20)); u16* o = (u16*)(ws + (12 << 20)); u16* x1 = (u16*)(ws + (16 << 20));
  u16* hh = (u16*)(ws + (20 << 20)); u16* x2 = (u16*)(ws + (28 << 20)); float* part = (float*)(ws + (32 << 20));
  for (int l = 0; l < 2; ++l) {
    const u16* xin = l == 0 ? x : x2; u16* xout = l == 0 ? x2 : (u16*)d_out;
    k_ln<<<S / 4, 256, 0, stream>>>(xin, xn);
    k_gemm<0><<<dim3(24, 32), 256, 0, stream>>>(xn, wqkv, nullptr, nullptr, qkv, 1536, 512);
    k_kv<<<dim3(H, 8), 256, 0, stream>>>(qkv, wphi, part);
    k_o<<<dim3(H, 8), 256, 0, stream>>>(qkv, wphi, part, o);
    k_gemm<1><<<dim3(8, 32), 256, 0, stream>>>(o, wo, nullptr, xin, x1, 512, 512);
    k_ln<<<S / 4, 256, 0, stream>>>(x1, xn);
    k_gemm<2><<<dim3(22, 32), 256, 0, stream>>>(xn, wg, wu, nullptr, hh, F, 512);
    k_gemm<1><<<dim3(8, 32), 256, 0, stream>>>(hh, wd, nullptr, x1, xout, 512, F);
  }
}
```

```cpp
#include <hip/hip_runtime.h>
#include <stdint.h>

typedef unsigned short u16;
typedef unsigned char u8;
typedef unsigned long long u64;
typedef unsigned v4u __attribute__((ext_vector_type(4)));
typedef unsigned v2u __attribute__((ext_vector_type(2)));
typedef float f32x2 __attribute__((ext_vector_type(2)));
typedef float f32x4 __attribute__((ext_vector_type(4)));
typedef float f32x16 __attribute__((ext_vector_type(16)));
typedef short s16x4 __attribute__((ext_vector_type(4)));
typedef __bf16 bf16x2 __attribute__((ext_vector_type(2)));
typedef __bf16 bf16x8 __attribute__((ext_vector_type(8)));
#define LAS __attribute__((address_space(3)))
#define DI __device__ __forceinline__
typedef __amdgpu_buffer_rsrc_t rsrc_t;

constexpr int KB = 1024, MB = 1024 * 1024;
constexpr int FLAG_OFF = 4096, X_OFF = 1 * MB, ST_OFF = 9 * MB, O_OFF = 10 * MB, H_OFF = 14 * MB, H_SZ = 64 * 88 * KB,
              KV_OFF = 25 * MB, Z_OFF = 27 * MB, NT_OFF = 28 * MB, CS_OFF = 33 * MB, WO_OFF = 34 * MB, WD_OFF = 35 * MB,
              DBG_OFF = 40 * MB, WS_BYTES = 48 * MB;
constexpr int SLOT = 33280, SCR = 3 * SLOT;
constexpr int PV_ROW = 208, KVI = SCR + 28 * KB, KVI_ROW = 80, KVI_ZERO = KVI + 2 * 17 * KVI_ROW;
constexpr int SC1 = 16;
constexpr float LOG2E = 1.4426950408889634f;

DI f32x16 mfma32(v4u a, v4u b, f32x16 c) {
  return __builtin_amdgcn_mfma_f32_32x32x16_bf16(__builtin_bit_cast(bf16x8, a), __builtin_bit_cast(bf16x8, b), c, 0, 0, 0);
}
DI f32x4 mfma16(v4u a, v4u b, f32x4 c) {
  return __builtin_amdgcn_mfma_f32_16x16x32_bf16(__builtin_bit_cast(bf16x8, a), __builtin_bit_cast(bf16x8, b), c, 0, 0, 0);
}
DI unsigned pk(float a, float b) { f32x2 v = {a, b}; return __builtin_bit_cast(unsigned, __builtin_convertvector(v, bf16x2)); }
DI float lo16(unsigned u) { return __uint_as_float(u << 16); }
DI float hi16(unsigned u) { return __uint_as_float(u & 0xffff0000u); }
DI float dot2(unsigned a, unsigned b, float c) {
  return __builtin_amdgcn_fdot2_f32_bf16(__builtin_bit_cast(bf16x2, a), __builtin_bit_cast(bf16x2, b), c, false);
}
DI v4u ld4(rsrc_t r, int voff, int soff) { return __builtin_amdgcn_raw_buffer_load_b128(r, voff, soff, SC1); }
DI void st4(v4u v, rsrc_t r, int voff, int soff) { __builtin_amdgcn_raw_buffer_store_b128(v, r, voff, soff, SC1); }
DI v4u lds4(LAS u8* p) { return *(LAS v4u*)p; }
DI v4u tr2(LAS u8* p0, LAS u8* p1) {
  s16x4 a = __builtin_amdgcn_ds_read_tr16_b64_v4i16((LAS s16x4*)p0), b = __builtin_amdgcn_ds_read_tr16_b64_v4i16((LAS s16x4*)p1);
  v2u x = __builtin_bit_cast(v2u, a), y = __builtin_bit_cast(v2u, b);
  return v4u{x[0], x[1], y[0], y[1]};
}
DI void dma16(v4u srd, unsigned lds, int voff, int soff) {
  asm volatile("s_mov_b32 m0, %0\n\ts_nop 4\n\tbuffer_load_dwordx4 %1, %2, %3 offen sc1 lds" ::"s"(lds), "v"(voff), "s"(srd), "s"(soff) : "memory");
}
DI void dma4(v4u srd, unsigned lds, int voff, int soff) {
  asm volatile("s_mov_b32 m0, %0\n\ts_nop 4\n\tbuffer_load_dword %1, %2, %3 offen sc1 lds" ::"s"(lds), "v"(voff), "s"(srd), "s"(soff) : "memory");
}
DI v4u mk_srd(const void* p, unsigned bytes) {
  u64 a = (u64)p;
  return v4u{(unsigned)a, (unsigned)(a >> 32) & 0xffffu, bytes, 0x00020000u};
}
#define DRAIN() asm volatile("s_waitcnt vmcnt(0)" ::: "memory")
#define BAR() do { asm volatile("s_waitcnt lgkmcnt(0)" ::: "memory"); __builtin_amdgcn_s_barrier(); asm volatile("" ::: "memory"); } while (0)
#define STAMP(i)

DI void publish(rsrc_t rw, int ph, int id, v2u tag, int w, int lane) {
  DRAIN();
  BAR();
  if (w == 0) {
    if (lane == 0) __builtin_amdgcn_raw_buffer_store_b64(tag, rw, FLAG_OFF + (ph * 256 + id) * 128, 0, SC1);
    DRAIN();
  }
}
DI void wait_flags(rsrc_t rw, int ph, int id_lane, v2u tag, bool poller) {
  if (poller) {
    for (int sp = 0; sp < (1 << 22); ++sp) {
      asm volatile("" ::: "memory");
      v2u f = __builtin_amdgcn_raw_buffer_load_b64(rw, FLAG_OFF + (ph * 256 + id_lane) * 128, 0, SC1);
      if (__all(f[0] == tag[0] && f[1] == tag[1])) break;
      __builtin_amdgcn_s_sleep(1);
    }
  }
  BAR();
}
DI void dma_tile(v4u srd, unsigned lds_base, int slot, int nt, int w, int lane) {
  int ntv = nt, wv = w;
  asm volatile("" : "+s"(ntv), "+s"(wv));
  unsigned l0 = lds_base + slot * SLOT + wv * 8 * KB;
  int s0 = NT_OFF + ntv * 32 * KB + wv * 8 * KB;
  asm volatile("s_mov_b32 m0, %0\n\ts_nop 4\n\t"
               "buffer_load_dwordx4 %2, %3, %1 offen sc1 lds\n\t"
               "buffer_load_dwordx4 %2, %3, %1 offen offset:1024 sc1 lds\n\t"
               "buffer_load_dwordx4 %2, %3, %1 offen offset:2048 sc1 lds\n\t"
               "buffer_load_dwordx4 %2, %3, %1 offen offset:3072 sc1 lds\n\t"
               "s_add_u32 m0, %0, 4096\n\ts_add_u32 %1, %1, 4096\n\t"
               "buffer_load_dwordx4 %2, %3, %1 offen sc1 lds\n\t"
               "buffer_load_dwordx4 %2, %3, %1 offen offset:1024 sc1 lds\n\t"
               "buffer_load_dwordx4 %2, %3, %1 offen offset:2048 sc1 lds\n\t"
               "buffer_load_dwordx4 %2, %3, %1 offen offset:3072 sc1 lds"
               : "+s"(l0), "+s"(s0) : "v"(lane * 16), "s"(srd) : "memory", "scc");
  dma4(srd, lds_base + slot * SLOT + 32 * KB + (wv & 1) * 256, (lane & 3) * 128 + (lane >> 2) * 4, CS_OFF + ntv * 512 + (wv & 1) * 64);
}
DI void softmax2(const f32x16& p, unsigned (&o)[16]) {
  float mx = p[0], mn = p[0];
#pragma unroll
  for (int i = 1; i < 16; ++i) { mx = fmaxf(mx, p[i]); mn = fminf(mn, p[i]); }
  const float a = -mx * LOG2E, b = mn * LOG2E;
  float e1[16], e2[16], s1 = 0.f, s2 = 0.f;
#pragma unroll
  for (int i = 0; i < 16; ++i) {
    e1[i] = __builtin_amdgcn_exp2f(fmaf(p[i], LOG2E, a)); s1 += e1[i];
    e2[i] = __builtin_amdgcn_exp2f(fmaf(p[i], -LOG2E, b)); s2 += e2[i];
  }
  const float r1 = __builtin_amdgcn_rcpf(s1), r2 = __builtin_amdgcn_rcpf(s2);
#pragma unroll
  for (int i = 0; i < 8; ++i) { o[i] = pk(e1[2 * i] * r1, e1[2 * i + 1] * r1); o[8 + i] = pk(e2[2 * i] * r2, e2[2 * i + 1] * r2); }
}
DI void pack16(const f32x16& a, float s, v4u& f0, v4u& f1) {
#pragma unroll
  for (int i = 0; i < 4; ++i) { f0[i] = pk(a[2 * i] * s, a[2 * i + 1] * s); f1[i] = pk(a[8 + 2 * i] * s, a[9 + 2 * i] * s); }
}
DI float xhalf_sum(float v) {
  auto r = __builtin_amdgcn_permlane32_swap(__float_as_uint(v), __float_as_uint(v), false, false);
  return __uint_as_float(r[0]) + __uint_as_float(r[1]);
}
DI void load_x(rsrc_t rw, int xi, int tt, int lane, v4u (&xf)[32], float& rstd, v4u& mfix) {
  v2u st[8];
#pragma unroll
  for (int i = 0; i < 8; ++i)
    st[i] = __builtin_amdgcn_raw_buffer_load_b64(rw, (lane >> 5) * 2048 + (lane & 31) * 8 + i * 256, ST_OFF + xi * 256 * KB + tt * 4096, SC1);
#pragma unroll
  for (int i = 0; i < 32; ++i) xf[i] = ld4(rw, lane * 16 + (i & 3) * KB, X_OFF + xi * 2 * MB + tt * 32 * KB + (i >> 2) * 4 * KB);
  __builtin_amdgcn_sched_barrier(0);
  float s = 0.f, q = 0.f;
#pragma unroll
  for (int i = 0; i < 8; ++i) { s += __uint_as_float(st[i][0]); q += __uint_as_float(st[i][1]); }
  s = xhalf_sum(s); q = xhalf_sum(q);
  const float mean = s * (1.f / 512.f);
  const float var = fmaxf(q * (1.f / 512.f) - mean * mean, 0.f);
  rstd = __builtin_amdgcn_rsqf(var + 1e-5f);
  const unsigned mh = pk(-mean, -mean);
  const float ml = -mean - lo16(mh);
  const unsigned m = lane < 32 ? mh : pk(ml, ml);
  mfix = v4u{m, m, m, m};
}
DI v4u geglu(const f32x16& a, float rstd) {
  const float ir = __builtin_amdgcn_rcpf(rstd);
  constexpr float K1 = -2.f * 0.7978845608028654f * LOG2E, K2 = K1 * 0.044715f;
  float o[8];
#pragma unroll
  for (int i = 0; i < 8; ++i) {
    const float g = a[i] * rstd;
    const float e = __builtin_amdgcn_exp2f(g * fmaf(g * g, K2, K1));
    o[i] = g * a[8 + i] * __builtin_amdgcn_rcpf(fmaf(e, ir, ir));
  }
  return v4u{pk(o[0], o[1]), pk(o[2], o[3]), pk(o[4], o[5]), pk(o[6], o[7])};
}
DI void ksplit_reduce(LAS u8* L, f32x16 (&acc)[2][2], int w, int lane) {
#pragma unroll
  for (int t = 1; t < 4; ++t)
#pragma unroll
    for (int c = 0; c < 4; ++c) {
      const f32x16& a = acc[t >> 1][t & 1];
      *(LAS f32x4*)(L + SCR + (((w ^ t) * 3 + t - 1) * 4 + c) * KB + lane * 16) = f32x4{a[4 * c], a[4 * c + 1], a[4 * c + 2], a[4 * c + 3]};
    }
  BAR();
#pragma unroll
  for (int t = 0; t < 3; ++t)
#pragma unroll
    for (int c = 0; c < 4; ++c) {
      const f32x4 v = *(LAS f32x4*)(L + SCR + ((w * 3 + t) * 4 + c) * KB + lane * 16);
#pragma unroll
      for (int e = 0; e < 4; ++e) acc[0][0][4 * c + e] += v[e];
    }
}
DI void store_x(rsrc_t rw, int xi, int tt, int nt, int lane, v4u f0, v4u f1) {
  st4(f0, rw, lane * 16, X_OFF + xi * 2 * MB + tt * 32 * KB + nt * 2 * KB);
  st4(f1, rw, lane * 16 + KB, X_OFF + xi * 2 * MB + tt * 32 * KB + nt * 2 * KB);
  float s = 0.f, q = 0.f;
#pragma unroll
  for (int i = 0; i < 4; ++i) {
    s = dot2(f0[i], 0x3f803f80u, s); s = dot2(f1[i], 0x3f803f80u, s);
    q = dot2(f0[i], f0[i], q); q = dot2(f1[i], f1[i], q);
  }
  s = xhalf_sum(s); q = xhalf_sum(q);
  if (lane < 32)
    __builtin_amdgcn_raw_buffer_store_b64(v2u{__float_as_uint(s), __float_as_uint(q)}, rw, lane * 8, ST_OFF + xi * 256 * KB + tt * 4096 + nt * 256, SC1);
}
DI f32x16 unpack16(v4u f0, v4u f1) {
  f32x16 r;
#pragma unroll
  for (int i = 0; i < 4; ++i) { r[2 * i] = lo16(f0[i]); r[2 * i + 1] = hi16(f0[i]); r[8 + 2 * i] = lo16(f1[i]); r[9 + 2 * i] = hi16(f1[i]); }
  return r;
}

__global__ __launch_bounds__(256, 1) void fused_block(const u16* __restrict__ x, const u16* __restrict__ wqkv, const u16* __restrict__ wphi,
                                                      const u16* __restrict__ wo, const u16* __restrict__ wg, const u16* __restrict__ wu,
                                                      const u16* __restrict__ wd, u16* __restrict__ out, u8* __restrict__ ws) {
  __shared__ __attribute__((aligned(16))) u8 smem[163840];
  LAS u8* L = (LAS u8*)smem;
  const unsigned Lb = (unsigned)(size_t)L;
  const int b = blockIdx.x, lane = threadIdx.x & 63, w = __builtin_amdgcn_readfirstlane(threadIdx.x >> 6);
  const int r = lane & 31, h = lane >> 5;
  const int xcd = b & 7, m = b >> 3;
  const int g = (xcd >> 1) * 4 + (m & 3), j = (xcd & 1) * 8 + (m >> 2);
  const int tb = xcd * 4 + (m & 3), cb = m >> 2;
  const int idA = g * 16 + j, idB = tb * 8 + cb;
  const rsrc_t rw = __builtin_amdgcn_make_buffer_rsrc((void*)ws, 0, WS_BYTES, 0x00020000);
  const v4u sw = mk_srd(ws, WS_BYTES);
  const f32x16 zero16 = {};
  const v4u ones = {0x3f803f80u, 0x3f803f80u, 0x3f803f80u, 0x3f803f80u};
  STAMP(0);
  {
    u64 pc; asm volatile("s_getpc_b64 %0" : "=s"(pc));
    if (w == 3 && lane < 8) { unsigned pf = *(const volatile unsigned*)((pc & ~127ull) + (m * 8 + lane) * 128); asm volatile("" ::"v"(pf)); }
  }
  const v2u ep = __builtin_amdgcn_raw_buffer_load_b64(rw, 0, 0, SC1);

  const int hp = (r >> 2) & 1, ip = (r & 3) + 4 * (r >> 3);
  v4u res0, res1;
  {
    const u16* src; int ld, row0, col0, nrows = 512, kind;
    if (b < 48) { kind = 0; src = wqkv; ld = 1536; row0 = (b & 3) * 128; col0 = (b >> 2) * 128; }
    else if (b < 64) { kind = 1; src = wo; ld = 512; row0 = (b & 3) * 128; col0 = ((b - 48) >> 2) * 128; }
    else if (b < 152) { kind = 2; src = wg; ld = 1376; row0 = (b & 3) * 128; col0 = ((b - 64) >> 2) * 64; }
    else { kind = 3; src = wd; ld = 512; nrows = 1376; row0 = ((b - 152) >> 2) * 128; col0 = (b & 3) * 128; }
    if (b < 196) {
      if (kind != 2) {
        const v4u sr = mk_srd(src, nrows * ld * 2);
#pragma unroll
        for (int i = 0; i < 8; ++i)
          dma16(sr, Lb + (w * 8 + i) * KB, ((lane >> 4) * ld + (lane & 15) * 8) * 2, ((row0 + (w * 8 + i) * 4) * ld + col0) * 2);
      } else {
        const v4u sr = mk_srd(w < 2 ? wg : wu, 512 * 1376 * 2);
#pragma unroll
        for (int i = 0; i < 8; ++i)
          dma16(sr, Lb + (w * 8 + i) * KB, ((lane >> 3) * ld + (lane & 7) * 8) * 2, ((row0 + ((w & 1) * 8 + i) * 8) * ld + col0) * 2);
      }
    }
    {
      const int t8 = lane & 7, c = lane >> 3, tok = g * 128 + j * 8 + t8, tt = tok >> 5, rr = tok & 31;
#pragma unroll
      for (int i = 0; i < 2; ++i) {
        const int pc = 16 * w + 8 * i + c;
        const v4u v = *(const v4u*)(x + tok * 512 + pc * 8);
        const int ks = 2 * (pc >> 2) + (pc & 1), hh = (pc >> 1) & 1;
        st4(v, rw, tt * 32 * KB + ks * KB + (hh * 32 + rr) * 16, X_OFF);
        float s = 0.f, q = 0.f;
#pragma unroll
        for (int e = 0; e < 4; ++e) { s = dot2(v[e], 0x3f803f80u, s); q = dot2(v[e], v[e], q); }
        s += __shfl_xor(s, 8); q += __shfl_xor(q, 8);
        s += __shfl_xor(s, 16); q += __shfl_xor(q, 16);
        if ((c & 3) == 0)
          __builtin_amdgcn_raw_buffer_store_b64(v2u{__float_as_uint(s), __float_as_uint(q)}, rw, tt * 4096 + (pc >> 2) * 256 + rr * 8, ST_OFF, SC1);
      }
    }
    {
      const u16* p = x + ((2 * tb + (w >> 1)) * 32 + r) * 512 + cb * 64 + (w & 1) * 32 + h * 16;
      res0 = *(const v4u*)p; res1 = *(const v4u*)(p + 8);
    }
    DRAIN();
    BAR();
    if (b < 196) {
      const int G = (lane >> 4) & 1, q4 = (lane & 15) >> 2, p4 = lane & 3;
      int colb, rstride, dst, nks = 8;
      if (kind != 2) { colb = (w * 32 + 16 * (p4 & 1) + 4 * (2 * G + (p4 >> 1))) * 2; rstride = 256; }
      else { colb = G * 16 * KB + (w * 16 + 8 * (p4 & 1) + 4 * (p4 >> 1)) * 2; rstride = 128; }
      int nt;
      bool valid = true;
      if (kind == 0) { nt = (col0 >> 5) + w; dst = NT_OFF + nt * 32 * KB + (b & 3) * 8 * KB; }
      else if (kind == 1) { nt = (col0 >> 5) + w; dst = WO_OFF + nt * 32 * KB + (b & 3) * 8 * KB; }
      else if (kind == 2) { nt = 48 + (col0 >> 4) + w; dst = NT_OFF + nt * 32 * KB + (b & 3) * 8 * KB; valid = nt < 134; }
      else { nt = (col0 >> 5) + w; dst = WD_OFF + (nt * 86 + (row0 >> 4)) * KB; nks = row0 == 1280 ? 6 : 8; }
      f32x16 cs = zero16;
#pragma unroll
      for (int ks = 0; ks < 8; ++ks) {
        const int row = (kind == 3 ? 16 * ks + 8 * h : 32 * (ks >> 1) + 16 * h + 8 * (ks & 1)) + q4;
        const v4u f = tr2(L + row * rstride + colb, L + (row + 4) * rstride + colb);
        if (valid && ks < nks) st4(f, rw, lane * 16, dst + ks * KB);
        cs = mfma32(ones, f, cs);
      }
      if (valid && (kind == 0 || kind == 2) && lane < 32) {
        const unsigned hi = pk(cs[0], cs[0]);
        const unsigned v = pk(cs[0], cs[0] - lo16(hi));
        __builtin_amdgcn_raw_buffer_store_b32(v, rw, lane * 4, CS_OFF + nt * 512 + (b & 3) * 128, SC1);
      }
    }
  }
  v4u wph[2];
#pragma unroll
  for (int s = 0; s < 2; ++s)
#pragma unroll
    for (int e = 0; e < 4; ++e) {
      const u16* p = wphi + (2 * j + hp) * 256 + (8 * s + 2 * e) * 16 + ip;
      wph[s][e] = h == hp ? ((unsigned)p[0] | ((unsigned)p[16] << 16)) : 0u;
    }
  v2u tag;
  {
    const u64 e = (((u64)__builtin_amdgcn_readfirstlane(ep[1]) << 32) | (unsigned)__builtin_amdgcn_readfirstlane(ep[0])) * 6364136223846793005ull + 1442695040888963407ull;
    tag = v2u{(unsigned)e, (unsigned)(e >> 32)};
  }
  STAMP(1);
  publish(rw, 0, b, tag, w, lane);
  wait_flags(rw, 0, w * 64 + lane, tag, true);
  STAMP(2);

  const int q = j >> 2, qa = j & 3;
  const int ubase = 48 + (q == 0 ? 0 : q == 1 ? 22 : q == 2 ? 44 : 65) + qa;
  const bool six = qa < (q < 2 ? 2 : 1);
  const int wbase = w == 0 ? 0 : w == 1 ? 22 : w == 2 ? 44 : 65;
  dma_tile(sw, Lb, 2, 16 + j, w, lane);
  dma_tile(sw, Lb, 0, 32 + j, w, lane);
  dma_tile(sw, Lb, 1, j, w, lane);
#pragma unroll 1
  for (int l = 0; l < 2; ++l) {
    const int ph = 1 + 5 * l, sb = 3 + 20 * l;
    const int tt = 4 * g + w;
    v4u xf[32], mfix; float rstd;
    DRAIN();
    BAR();
    STAMP(sb);
    load_x(rw, 2 * l, tt, lane, xf, rstd, mfix);
    f32x16 ak = zero16, av = zero16;
#pragma unroll
    for (int ks = 0; ks < 32; ++ks) {
      ak = mfma32(lds4(L + 2 * SLOT + ks * KB + lane * 16), xf[ks], ak);
      av = mfma32(lds4(L + 0 * SLOT + ks * KB + lane * 16), xf[ks], av);
    }
    ak = mfma32(lds4(L + 2 * SLOT + 32 * KB + r * 16), mfix, ak);
    av = mfma32(lds4(L + 0 * SLOT + 32 * KB + r * 16), mfix, av);
    STAMP(sb + 1);
    {
      v4u k0, k1, v0, v1;
      pack16(ak, rstd, k0, k1);
      pack16(av, rstd, v0, v1);
      f32x16 p = mfma32(wph[1], k1, mfma32(wph[0], k0, zero16));
      unsigned ph16[16];
      softmax2(p, ph16);
      LAS u8* row = L + SCR + (w * 32 + r) * PV_ROW;
#pragma unroll
      for (int c = 0; c < 4; ++c) *(LAS v4u*)(row + h * 64 + c * 16) = v4u{ph16[4 * c], ph16[4 * c + 1], ph16[4 * c + 2], ph16[4 * c + 3]};
      *(LAS v4u*)(row + 128 + h * 32) = v0;
      *(LAS v4u*)(row + 128 + h * 32 + 16) = v1;
      if (threadIdx.x < 4) *(LAS v4u*)(L + KVI_ZERO + threadIdx.x * 16) = v4u{0, 0, 0, 0};
    }
    BAR();
    {
      f32x4 kv = {}, z = {};
      LAS u8* base = L + SCR + (8 * (lane >> 4) + ((lane & 15) >> 2)) * PV_ROW + (lane & 3) * 8;
#pragma unroll
      for (int c = 0; c < 4; ++c) {
        LAS u8* pa = base + c * 32 * PV_ROW + w * 32;
        LAS u8* pb = base + c * 32 * PV_ROW + 128 + (w >> 1) * 32;
        const v4u a = tr2(pa, pa + 4 * PV_ROW), bb = tr2(pb, pb + 4 * PV_ROW);
        kv = mfma16(a, bb, kv);
        z = mfma16(a, ones, z);
      }
      st4(__builtin_bit_cast(v4u, kv), rw, lane * 16, KV_OFF + l * MB + ((j * 4 + w) * 16 + g) * KB);
      if ((lane & 15) == 0) st4(__builtin_bit_cast(v4u, z), rw, (lane >> 4) * 16, Z_OFF + l * 64 * KB + ((j * 4 + w) * 16 + g) * 64);
    }
    STAMP(sb + 2);
    publish(rw, ph, idA, tag, w, lane);
    STAMP(sb + 3);
    dma_tile(sw, Lb, 0, ubase, w, lane);
    dma_tile(sw, Lb, 2, ubase + 8, w, lane);
    unsigned phq[16];
    {
      f32x16 aq = zero16;
#pragma unroll
      for (int ks = 0; ks < 32; ++ks) aq = mfma32(lds4(L + 1 * SLOT + ks * KB + lane * 16), xf[ks], aq);
      aq = mfma32(lds4(L + 1 * SLOT + 32 * KB + r * 16), mfix, aq);
      v4u q0, q1;
      pack16(aq, rstd, q0, q1);
      softmax2(mfma32(wph[1], q1, mfma32(wph[0], q0, zero16)), phq);
    }
    STAMP(sb + 4);
    wait_flags(rw, ph, (lane & 15) * 16 + j, tag, w == 0);
    STAMP(sb + 5);
    {
      v4u pz = ld4(rw, lane * 16, Z_OFF + l * 64 * KB + (j * 4 + w) * KB);
      v4u pp[16];
#pragma unroll
      for (int i = 0; i < 16; ++i) pp[i] = ld4(rw, lane * 16 + (i & 3) * KB, KV_OFF + l * MB + ((j * 4 + w) * 16 + (i & 12)) * KB);
      __builtin_amdgcn_sched_barrier(0);
      f32x4 z = __builtin_bit_cast(f32x4, pz);
#pragma unroll
      for (int o = 4; o < 64; o <<= 1)
#pragma unroll
        for (int e = 0; e < 4; ++e) z[e] += __shfl_xor(z[e], o);
      f32x4 kv = __builtin_bit_cast(f32x4, pp[0]);
#pragma unroll
      for (int i = 1; i < 16; ++i) kv += __builtin_bit_cast(f32x4, pp[i]);
      LAS u8* img = L + KVI + (w >> 1) * 17 * KVI_ROW + (w & 1) * 32;
      *(LAS v2u*)(img + (lane & 15) * KVI_ROW + (lane >> 4) * 8) = v2u{pk(kv[0], kv[1]), pk(kv[2], kv[3])};
      if (lane < 4) *(LAS v2u*)(img + 16 * KVI_ROW + lane * 8) = v2u{pk(z[0], z[1]), pk(z[2], z[3])};
      BAR();
      LAS u8* pn = L + (h == hp ? KVI + (h * 17 + ip) * KVI_ROW : KVI_ZERO);
      LAS u8* pd = L + (h == hp ? KVI + (h * 17 + 16) * KVI_ROW : KVI_ZERO);
      f32x16 num = zero16, den = zero16;
#pragma unroll
      for (int s = 0; s < 4; ++s) {
        const v4u bq = {phq[4 * s], phq[4 * s + 1], phq[4 * s + 2], phq[4 * s + 3]};
        num = mfma32(lds4(pn + 16 * s), bq, num);
        den = mfma32(lds4(pd + 16 * s), bq, den);
      }
      v4u o0, o1;
      pack16(num, __builtin_amdgcn_rcpf(den[0] + 1e-6f), o0, o1);
      st4(o0, rw, lane * 16, O_OFF + l * 2 * MB + tt * 32 * KB + 2 * j * KB);
      st4(o1, rw, lane * 16 + KB, O_OFF + l * 2 * MB + tt * 32 * KB + 2 * j * KB);
    }
    STAMP(sb + 6);
    publish(rw, ph + 1, idA, tag, w, lane);
    STAMP(sb + 7);
    v4u wor[2][8];
#pragma unroll
    for (int n = 0; n < 2; ++n)
#pragma unroll
      for (int i = 0; i < 8; ++i) wor[n][i] = ld4(rw, lane * 16 + (i & 3) * KB, WO_OFF + ((2 * cb + (n ^ (w & 1))) * 32 + 8 * w + (i & 4)) * KB);
    __builtin_amdgcn_sched_barrier(0);
    wait_flags(rw, ph + 1, (tb >> 1) * 16 + (lane & 15), tag, w == 0);
    STAMP(sb + 8);
    const int ttB = 2 * tb + (w >> 1), ntB = 2 * cb + (w & 1);
    {
      v4u of[2][8];
#pragma unroll
      for (int a = 1; a >= 0; --a)
#pragma unroll
        for (int i = 0; i < 8; ++i) of[a][i] = ld4(rw, lane * 16 + (i & 3) * KB, O_OFF + l * 2 * MB + (ttB ^ a) * 32 * KB + (8 * w + (i & 4)) * KB);
      __builtin_amdgcn_sched_barrier(0);
      dma_tile(sw, Lb, 1, ubase + 4, w, lane);
      f32x16 acc[2][2] = {{unpack16(res0, res1), zero16}, {zero16, zero16}};
#pragma unroll
      for (int a = 1; a >= 0; --a)
#pragma unroll
        for (int i = 0; i < 8; ++i)
#pragma unroll
          for (int n = 0; n < 2; ++n) acc[a][n] = mfma32(wor[n][i], of[a][i], acc[a][n]);
      STAMP(sb + 9);
      ksplit_reduce(L, acc, w, lane);
      pack16(acc[0][0], 1.f, res0, res1);
      store_x(rw, 2 * l + 1, ttB, ntB, lane, res0, res1);
    }
    STAMP(sb + 10);
    publish(rw, ph + 2, idB, tag, w, lane);
    STAMP(sb + 11);
    wait_flags(rw, ph + 2, g * 16 + (lane & 15), tag, w == 0);
    STAMP(sb + 12);
    {
      load_x(rw, 2 * l + 1, tt, lane, xf, rstd, mfix);
      dma_tile(sw, Lb, 3, ubase + 12, w, lane);
      const int hb = H_OFF + l * H_SZ + (tt * 88 + q * 22 + qa) * KB;
      f32x16 a0 = zero16, a1 = zero16;
#pragma unroll
      for (int ks = 0; ks < 32; ++ks) {
        a0 = mfma32(lds4(L + 0 * SLOT + ks * KB + lane * 16), xf[ks], a0);
        a1 = mfma32(lds4(L + 1 * SLOT + ks * KB + lane * 16), xf[ks], a1);
      }
      a0 = mfma32(lds4(L + 0 * SLOT + 32 * KB + r * 16), mfix, a0);
      a1 = mfma32(lds4(L + 1 * SLOT + 32 * KB + r * 16), mfix, a1);
      DRAIN();
      BAR();
      dma_tile(sw, Lb, 0, ubase + 16, w, lane);
      if (six) dma_tile(sw, Lb, 1, ubase + 20, w, lane);
      f32x16 a2 = zero16, a3 = zero16;
#pragma unroll
      for (int ks = 0; ks < 32; ++ks) a2 = mfma32(lds4(L + 2 * SLOT + ks * KB + lane * 16), xf[ks], a2);
      a2 = mfma32(lds4(L + 2 * SLOT + 32 * KB + r * 16), mfix, a2);
      st4(geglu(a0, rstd), rw, lane * 16, hb);
#pragma unroll
      for (int ks = 0; ks < 32; ++ks) a3 = mfma32(lds4(L + 3 * SLOT + ks * KB + lane * 16), xf[ks], a3);
      a3 = mfma32(lds4(L + 3 * SLOT + 32 * KB + r * 16), mfix, a3);
      st4(geglu(a1, rstd), rw, lane * 16, hb + 4 * KB);
      asm volatile("s_waitcnt vmcnt(2)" ::: "memory");
      BAR();
      if (l == 0) dma_tile(sw, Lb, 2, 16 + j, w, lane);
      a0 = zero16;
#pragma unroll
      for (int ks = 0; ks < 32; ++ks) a0 = mfma32(lds4(L + 0 * SLOT + ks * KB + lane * 16), xf[ks], a0);
      a0 = mfma32(lds4(L + 0 * SLOT + 32 * KB + r * 16), mfix, a0);
      st4(geglu(a2, rstd), rw, lane * 16, hb + 8 * KB);
      if (six) {
        a1 = zero16;
#pragma unroll
        for (int ks = 0; ks < 32; ++ks) a1 = mfma32(lds4(L + 1 * SLOT + ks * KB + lane * 16), xf[ks], a1);
        a1 = mfma32(lds4(L + 1 * SLOT + 32 * KB + r * 16), mfix, a1);
        st4(geglu(a3, rstd), rw, lane * 16, hb + 12 * KB);
        st4(geglu(a0, rstd), rw, lane * 16, hb + 16 * KB);
        st4(geglu(a1, rstd), rw, lane * 16, hb + 20 * KB);
      } else {
        st4(geglu(a3, rstd), rw, lane * 16, hb + 12 * KB);
        st4(geglu(a0, rstd), rw, lane * 16, hb + 16 * KB);
      }
    }
    STAMP(sb + 13);
    publish(rw, ph + 3, idA, tag, w, lane);
    STAMP(sb + 14);
    v4u wdr[2][22];
#pragma unroll
    for (int n = 0; n < 2; ++n)
#pragma unroll
      for (int i = 0; i < 22; ++i)
        wdr[n][i] = ld4(rw, lane * 16 + (i & 3) * KB, (i == 21 && w >= 2) ? WS_BYTES : WD_OFF + ((2 * cb + (n ^ (w & 1))) * 86 + wbase + (i & ~3)) * KB);
    __builtin_amdgcn_sched_barrier(0);
    wait_flags(rw, ph + 3, (tb >> 1) * 16 + (lane & 15), tag, w == 0);
    STAMP(sb + 15);
    {
      f32x16 acc[2][2] = {{unpack16(res0, res1), zero16}, {zero16, zero16}};
#pragma unroll
      for (int a = 1; a >= 0; --a) {
        v4u hf[22];
#pragma unroll
        for (int i = 0; i < 22; ++i)
          hf[i] = ld4(rw, lane * 16 + (i & 3) * KB, (i == 21 && w >= 2) ? WS_BYTES : H_OFF + l * H_SZ + ((ttB ^ a) * 88 + w * 22 + (i & ~3)) * KB);
        __builtin_amdgcn_sched_barrier(0);
#pragma unroll
        for (int i = 0; i < 22; ++i)
#pragma unroll
          for (int n = 0; n < 2; ++n) acc[a][n] = mfma32(wdr[n][i], hf[i], acc[a][n]);
        __builtin_amdgcn_sched_barrier(0);
      }
      STAMP(sb + 16);
      ksplit_reduce(L, acc, w, lane);
      pack16(acc[0][0], 1.f, res0, res1);
      if (l == 0) {
        store_x(rw, 2, ttB, ntB, lane, res0, res1);
        STAMP(sb + 17);
        publish(rw, ph + 4, idB, tag, w, lane);
        STAMP(sb + 18);
        if (w != 0) { dma_tile(sw, Lb, 0, 32 + j, w, lane); dma_tile(sw, Lb, 1, j, w, lane); }
        wait_flags(rw, ph + 4, g * 16 + (lane & 15), tag, w == 0);
        if (w == 0) { dma_tile(sw, Lb, 0, 32 + j, w, lane); dma_tile(sw, Lb, 1, j, w, lane); }
      }
    }
  }
  {
    u16* p = out + ((2 * tb + (w >> 1)) * 32 + r) * 512 + cb * 64 + (w & 1) * 32 + h * 16;
    *(v4u*)p = res0; *(v4u*)(p + 8) = res1;
    if (b == 0 && threadIdx.x == 0) __builtin_amdgcn_raw_buffer_store_b64(tag, rw, 0, 0, SC1);
  }
  STAMP(62);
}

extern "C" void kernel_launch(void* const* d_in, const int* in_sizes, int n_in, void* d_out, int out_size, void* d_ws, size_t ws_size,
                              hipStream_t stream) {
  fused_block<<<256, 256, 0, stream>>>((const u16*)d_in[0], (const u16*)d_in[1], (const u16*)d_in[2], (const u16*)d_in[3], (const u16*)d_in[5],
                                        (const u16*)d_in[6], (const u16*)d_in[7], (u16*)d_out, (u8*)d_ws);
}
```

```cpp
#include <hip/hip_runtime.h>
#include <stdint.h>

typedef unsigned short u16;
typedef unsigned char u8;
typedef unsigned long long u64;
typedef unsigned v4u __attribute__((ext_vector_type(4)));
typedef unsigned v2u __attribute__((ext_vector_type(2)));
typedef float f32x2 __attribute__((ext_vector_type(2)));
typedef float f32x4 __attribute__((ext_vector_type(4)));
typedef float f32x16 __attribute__((ext_vector_type(16)));
typedef short s16x4 __attribute__((ext_vector_type(4)));
typedef __bf16 bf16x2 __attribute__((ext_vector_type(2)));
typedef __bf16 bf16x8 __attribute__((ext_vector_type(8)));
#define LAS __attribute__((address_space(3)))
#define DI __device__ __forceinline__
typedef __amdgpu_buffer_rsrc_t rsrc_t;

constexpr int KB = 1024, MB = 1024 * 1024;
constexpr int FLAG_OFF = 4096, X_OFF = 1 * MB, ST_OFF = 9 * MB, O_OFF = 10 * MB, H_OFF = 14 * MB, H_SZ = 64 * 88 * KB,
              KV_OFF = 25 * MB, Z_OFF = 27 * MB, NT_OFF = 28 * MB, CS_OFF = 33 * MB, WO_OFF = 34 * MB, WD_OFF = 35 * MB,
              DBG_OFF = 40 * MB, WS_BYTES = 48 * MB;
constexpr int SLOT = 33280, SCR = 3 * SLOT;
constexpr int PV_ROW = 208, KVI = SCR + 28 * KB, KVI_ROW = 80, KVI_ZERO = KVI + 2 * 17 * KVI_ROW;
constexpr int SC1 = 16;
constexpr float LOG2E = 1.4426950408889634f;

DI f32x16 mfma32(v4u a, v4u b, f32x16 c) {
  return __builtin_amdgcn_mfma_f32_32x32x16_bf16(__builtin_bit_cast(bf16x8, a), __builtin_bit_cast(bf16x8, b), c, 0, 0, 0);
}
DI f32x4 mfma16(v4u a, v4u b, f32x4 c) {
  return __builtin_amdgcn_mfma_f32_16x16x32_bf16(__builtin_bit_cast(bf16x8, a), __builtin_bit_cast(bf16x8, b), c, 0, 0, 0);
}
DI unsigned pk(float a, float b) { f32x2 v = {a, b}; return __builtin_bit_cast(unsigned, __builtin_convertvector(v, bf16x2)); }
DI float lo16(unsigned u) { return __uint_as_float(u << 16); }
DI float hi16(unsigned u) { return __uint_as_float(u & 0xffff0000u); }
DI float dot2(unsigned a, unsigned b, float c) {
  return __builtin_amdgcn_fdot2_f32_bf16(__builtin_bit_cast(bf16x2, a), __builtin_bit_cast(bf16x2, b), c, false);
}
DI v4u ld4(rsrc_t r, int voff, int soff) { return __builtin_amdgcn_raw_buffer_load_b128(r, voff, soff, SC1); }
DI void st4(v4u v, rsrc_t r, int voff, int soff) { __builtin_amdgcn_raw_buffer_store_b128(v, r, voff, soff, SC1); }
DI v4u lds4(LAS u8* p) { return *(LAS v4u*)p; }
DI v4u tr2(LAS u8* p0, LAS u8* p1) {
  s16x4 a = __builtin_amdgcn_ds_read_tr16_b64_v4i16((LAS s16x4*)p0), b = __builtin_amdgcn_ds_read_tr16_b64_v4i16((LAS s16x4*)p1);
  v2u x = __builtin_bit_cast(v2u, a), y = __builtin_bit_cast(v2u, b);
  return v4u{x[0], x[1], y[0], y[1]};
}
DI void dma16(v4u srd, unsigned lds, int voff, int soff) {
  asm volatile("s_mov_b32 m0, %0\n\ts_nop 4\n\tbuffer_load_dwordx4 %1, %2, %3 offen sc1 lds" ::"s"(lds), "v"(voff), "s"(srd), "s"(soff) : "memory");
}
DI void dma4(v4u srd, unsigned lds, int voff, int soff) {
  asm volatile("s_mov_b32 m0, %0\n\ts_nop 4\n\tbuffer_load_dword %1, %2, %3 offen sc1 lds" ::"s"(lds), "v"(voff), "s"(srd), "s"(soff) : "memory");
}
DI v4u mk_srd(const void* p, unsigned bytes) {
  u64 a = (u64)p;
  return v4u{(unsigned)a, (unsigned)(a >> 32) & 0xffffu, bytes, 0x00020000u};
}
#define DRAIN() asm volatile("s_waitcnt vmcnt(0)" ::: "memory")
#define BAR() do { asm volatile("s_waitcnt lgkmcnt(0)" ::: "memory"); __builtin_amdgcn_s_barrier(); asm volatile("" ::: "memory"); } while (0)
#define STAMP(i)

DI void publish(rsrc_t rw, int ph, int id, v2u tag, int w, int lane) {
  DRAIN();
  BAR();
  if (w == 0) {
    if (lane == 0) __builtin_amdgcn_raw_buffer_store_b64(tag, rw, FLAG_OFF + (ph * 256 + id) * 128, 0, SC1);
    DRAIN();
  }
}
DI void wait_flags(rsrc_t rw, int ph, int id_lane, v2u tag, bool poller) {
  if (poller) {
    for (int sp = 0; sp < (1 << 22); ++sp) {
      asm volatile("" ::: "memory");
      v2u f = __builtin_amdgcn_raw_buffer_load_b64(rw, FLAG_OFF + (ph * 256 + id_lane) * 128, 0, SC1);
      if (__all(f[0] == tag[0] && f[1] == tag[1])) break;
      __builtin_amdgcn_s_sleep(1);
    }
  }
  BAR();
}
DI void dma_tile(v4u srd, unsigned lds_base, int slot, int nt, int w, int lane) {
  int ntv = nt, wv = w;
  asm volatile("" : "+s"(ntv), "+s"(wv));
  unsigned l0 = lds_base + slot * SLOT + wv * 8 * KB;
  int s0 = NT_OFF + ntv * 32 * KB + wv * 8 * KB;
  asm volatile("s_mov_b32 m0, %0\n\ts_nop 4\n\t"
               "buffer_load_dwordx4 %2, %3, %1 offen sc1 lds\n\t"
               "buffer_load_dwordx4 %2, %3, %1 offen offset:1024 sc1 lds\n\t"
               "buffer_load_dwordx4 %2, %3, %1 offen offset:2048 sc1 lds\n\t"
               "buffer_load_dwordx4 %2, %3, %1 offen offset:3072 sc1 lds\n\t"
               "s_add_u32 m0, %0, 4096\n\ts_add_u32 %1, %1, 4096\n\t"
               "buffer_load_dwordx4 %2, %3, %1 offen sc1 lds\n\t"
               "buffer_load_dwordx4 %2, %3, %1 offen offset:1024 sc1 lds\n\t"
               "buffer_load_dwordx4 %2, %3, %1 offen offset:2048 sc1 lds\n\t"
               "buffer_load_dwordx4 %2, %3, %1 offen offset:3072 sc1 lds"
               : "+s"(l0), "+s"(s0) : "v"(lane * 16), "s"(srd) : "memory", "scc");
  dma4(srd, lds_base + slot * SLOT + 32 * KB + (wv & 1) * 256, (lane & 3) * 128 + (lane >> 2) * 4, CS_OFF + ntv * 512 + (wv & 1) * 64);
}
DI void softmax2(const f32x16& p, unsigned (&o)[16]) {
  float mx = p[0], mn = p[0];
#pragma unroll
  for (int i = 1; i < 16; ++i) { mx = fmaxf(mx, p[i]); mn = fminf(mn, p[i]); }
  const float a = -mx * LOG2E, b = mn * LOG2E;
  float e1[16], e2[16], s1 = 0.f, s2 = 0.f;
#pragma unroll
  for (int i = 0; i < 16; ++i) {
    e1[i] = __builtin_amdgcn_exp2f(fmaf(p[i], LOG2E, a)); s1 += e1[i];
    e2[i] = __builtin_amdgcn_exp2f(fmaf(p[i], -LOG2E, b)); s2 += e2[i];
  }
  const float r1 = __builtin_amdgcn_rcpf(s1), r2 = __builtin_amdgcn_rcpf(s2);
#pragma unroll
  for (int i = 0; i < 8; ++i) { o[i] = pk(e1[2 * i] * r1, e1[2 * i + 1] * r1); o[8 + i] = pk(e2[2 * i] * r2, e2[2 * i + 1] * r2); }
}
DI void pack16(const f32x16& a, float s, v4u& f0, v4u& f1) {
#pragma unroll
  for (int i = 0; i < 4; ++i) { f0[i] = pk(a[2 * i] * s, a[2 * i + 1] * s); f1[i] = pk(a[8 + 2 * i] * s, a[9 + 2 * i] * s); }
}
DI float xhalf_sum(float v) {
  auto r = __builtin_amdgcn_permlane32_swap(__float_as_uint(v), __float_as_uint(v), false, false);
  return __uint_as_float(r[0]) + __uint_as_float(r[1]);
}
DI void load_x(rsrc_t rw, int xi, int tt, int lane, v4u (&xf)[32], float& rstd, v4u& mfix) {
  v2u st[8];
#pragma unroll
  for (int i = 0; i < 8; ++i)
    st[i] = __builtin_amdgcn_raw_buffer_load_b64(rw, (lane >> 5) * 2048 + (lane & 31) * 8 + i * 256, ST_OFF + xi * 256 * KB + tt * 4096, SC1);
#pragma unroll
  for (int i = 0; i < 32; ++i) xf[i] = ld4(rw, lane * 16 + (i & 3) * KB, X_OFF + xi * 2 * MB + tt * 32 * KB + (i >> 2) * 4 * KB);
  __builtin_amdgcn_sched_barrier(0);
  float s = 0.f, q = 0.f;
#pragma unroll
  for (int i = 0; i < 8; ++i) { s += __uint_as_float(st[i][0]); q += __uint_as_float(st[i][1]); }
  s = xhalf_sum(s); q = xhalf_sum(q);
  const float mean = s * (1.f / 512.f);
  const float var = fmaxf(q * (1.f / 512.f) - mean * mean, 0.f);
  rstd = __builtin_amdgcn_rsqf(var + 1e-5f);
  const unsigned mh = pk(-mean, -mean);
  const float ml = -mean - lo16(mh);
  const unsigned m = lane < 32 ? mh : pk(ml, ml);
  mfix = v4u{m, m, m, m};
}
DI v4u geglu(const f32x16& a, float rstd) {
  const float ir = __builtin_amdgcn_rcpf(rstd);
  constexpr float K1 = -2.f * 0.7978845608028654f * LOG2E, K2 = K1 * 0.044715f;
  float o[8];
#pragma unroll
  for (int i = 0; i < 8; ++i) {
    const float g = a[i] * rstd;
    const float e = __builtin_amdgcn_exp2f(g * fmaf(g * g, K2, K1));
    o[i] = g * a[8 + i] * __builtin_amdgcn_rcpf(fmaf(e, ir, ir));
  }
  return v4u{pk(o[0], o[1]), pk(o[2], o[3]), pk(o[4], o[5]), pk(o[6], o[7])};
}
DI void ksplit_reduce(LAS u8* L, f32x16 (&acc)[2][2], int w, int lane) {
#pragma unroll
  for (int t = 1; t < 4; ++t)
#pragma unroll
    for (int c = 0; c < 4; ++c) {
      const f32x16& a = acc[t >> 1][t & 1];
      *(LAS f32x4*)(L + SCR + (((w ^ t) * 3 + t - 1) * 4 + c) * KB + lane * 16) = f32x4{a[4 * c], a[4 * c + 1], a[4 * c + 2], a[4 * c + 3]};
    }
  BAR();
#pragma unroll
  for (int t = 0; t < 3; ++t)
#pragma unroll
    for (int c = 0; c < 4; ++c) {
      const f32x4 v = *(LAS f32x4*)(L + SCR + ((w * 3 + t) * 4 + c) * KB + lane * 16);
#pragma unroll
      for (int e = 0; e < 4; ++e) acc[0][0][4 * c + e] += v[e];
    }
}
DI void store_x(rsrc_t rw, int xi, int tt, int nt, int lane, v4u f0, v4u f1) {
  st4(f0, rw, lane * 16, X_OFF + xi * 2 * MB + tt * 32 * KB + nt * 2 * KB);
  st4(f1, rw, lane * 16 + KB, X_OFF + xi * 2 * MB + tt * 32 * KB + nt * 2 * KB);
  float s = 0.f, q = 0.f;
#pragma unroll
  for (int i = 0; i < 4; ++i) {
    s = dot2(f0[i], 0x3f803f80u, s); s = dot2(f1[i], 0x3f803f80u, s);
    q = dot2(f0[i], f0[i], q); q = dot2(f1[i], f1[i], q);
  }
  s = xhalf_sum(s); q = xhalf_sum(q);
  if (lane < 32)
    __builtin_amdgcn_raw_buffer_store_b64(v2u{__float_as_uint(s), __float_as_uint(q)}, rw, lane * 8, ST_OFF + xi * 256 * KB + tt * 4096 + nt * 256, SC1);
}
DI f32x16 unpack16(v4u f0, v4u f1) {
  f32x16 r;
#pragma unroll
  for (int i = 0; i < 4; ++i) { r[2 * i] = lo16(f0[i]); r[2 * i + 1] = hi16(f0[i]); r[8 + 2 * i] = lo16(f1[i]); r[9 + 2 * i] = hi16(f1[i]); }
  return r;
}

__global__ __launch_bounds__(256, 1) void fused_block(const u16* __restrict__ x, const u16* __restrict__ wqkv, const u16* __restrict__ wphi,
                                                      const u16* __restrict__ wo, const u16* __restrict__ wg, const u16* __restrict__ wu,
                                                      const u16* __restrict__ wd, u16* __restrict__ out, u8* __restrict__ ws) {
  __shared__ __attribute__((aligned(16))) u8 smem[163840];
  LAS u8* L = (LAS u8*)smem;
  const unsigned Lb = (unsigned)(size_t)L;
  const int b = blockIdx.x, lane = threadIdx.x & 63, w = __builtin_amdgcn_readfirstlane(threadIdx.x >> 6);
  const int r = lane & 31, h = lane >> 5;
  const int xcd = b & 7, m = b >> 3;
  const int g = (xcd >> 1) * 4 + (m & 3), j = (xcd & 1) * 8 + (m >> 2);
  const int tb = xcd * 4 + (m & 3), cb = m >> 2;
  const int idA = g * 16 + j, idB = tb * 8 + cb;
  const rsrc_t rw = __builtin_amdgcn_make_buffer_rsrc((void*)ws, 0, WS_BYTES, 0x00020000);
  const v4u sw = mk_srd(ws, WS_BYTES);
  const f32x16 zero16 = {};
  const v4u ones = {0x3f803f80u, 0x3f803f80u, 0x3f803f80u, 0x3f803f80u};
  STAMP(0);
  {
    u64 pc; asm volatile("s_getpc_b64 %0" : "=s"(pc));
    if (w == 3 && lane < 8) { unsigned pf = *(const volatile unsigned*)((pc & ~127ull) + (m * 8 + lane) * 128); asm volatile("" ::"v"(pf)); }
  }
  const v2u ep = __builtin_amdgcn_raw_buffer_load_b64(rw, 0, 0, SC1);

  const int hp = (r >> 2) & 1, ip = (r & 3) + 4 * (r >> 3);
  v4u res0, res1;
  {
    const u16* src; int ld, row0, col0, nrows = 512, kind;
    if (b < 48) { kind = 0; src = wqkv; ld = 1536; row0 = (b & 3) * 128; col0 = (b >> 2) * 128; }
    else if (b < 64) { kind = 1; src = wo; ld = 512; row0 = (b & 3) * 128; col0 = ((b - 48) >> 2) * 128; }
    else if (b < 152) { kind = 2; src = wg; ld = 1376; row0 = (b & 3) * 128; col0 = ((b - 64) >> 2) * 64; }
    else { kind = 3; src = wd; ld = 512; nrows = 1376; row0 = ((b - 152) >> 2) * 128; col0 = (b & 3) * 128; }
    if (b < 196) {
      if (kind != 2) {
        const v4u sr = mk_srd(src, nrows * ld * 2);
#pragma unroll
        for (int i = 0; i < 8; ++i)
          dma16(sr, Lb + (w * 8 + i) * KB, ((lane >> 4) * ld + (lane & 15) * 8) * 2, ((row0 + (w * 8 + i) * 4) * ld + col0) * 2);
      } else {
        const v4u sr = mk_srd(w < 2 ? wg : wu, 512 * 1376 * 2);
#pragma unroll
        for (int i = 0; i < 8; ++i)
          dma16(sr, Lb + (w * 8 + i) * KB, ((lane >> 3) * ld + (lane & 7) * 8) * 2, ((row0 + ((w & 1) * 8 + i) * 8) * ld + col0) * 2);
      }
    }
    {
      const int t8 = lane & 7, c = lane >> 3, tok = g * 128 + j * 8 + t8, tt = tok >> 5, rr = tok & 31;
#pragma unroll
      for (int i = 0; i < 2; ++i) {
        const int pc = 16 * w + 8 * i + c;
        const v4u v = *(const v4u*)(x + tok * 512 + pc * 8);
        const int ks = 2 * (pc >> 2) + (pc & 1), hh = (pc >> 1) & 1;
        st4(v, rw, tt * 32 * KB + ks * KB + (hh * 32 + rr) * 16, X_OFF);
        float s = 0.f, q = 0.f;
#pragma unroll
        for (int e = 0; e < 4; ++e) { s = dot2(v[e], 0x3f803f80u, s); q = dot2(v[e], v[e], q); }
        s += __shfl_xor(s, 8); q += __shfl_xor(q, 8);
        s += __shfl_xor(s, 16); q += __shfl_xor(q, 16);
        if ((c & 3) == 0)
          __builtin_amdgcn_raw_buffer_store_b64(v2u{__float_as_uint(s), __float_as_uint(q)}, rw, tt * 4096 + (pc >> 2) * 256 + rr * 8, ST_OFF, SC1);
      }
    }
    {
      const u16* p = x + ((2 * tb + (w >> 1)) * 32 + r) * 512 + cb * 64 + (w & 1) * 32 + h * 16;
      res0 = *(const v4u*)p; res1 = *(const v4u*)(p + 8);
    }
    DRAIN();
    BAR();
    if (b < 196) {
      const int G = (lane >> 4) & 1, q4 = (lane & 15) >> 2, p4 = lane & 3;
      int colb, rstride, dst, nks = 8;
      if (kind != 2) { colb = (w * 32 + 16 * (p4 & 1) + 4 * (2 * G + (p4 >> 1))) * 2; rstride = 256; }
      else { colb = G * 16 * KB + (w * 16 + 8 * (p4 & 1) + 4 * (p4 >> 1)) * 2; rstride = 128; }
      int nt;
      bool valid = true;
      if (kind == 0) { nt = (col0 >> 5) + w; dst = NT_OFF + nt * 32 * KB + (b & 3) * 8 * KB; }
      else if (kind == 1) { nt = (col0 >> 5) + w; dst = WO_OFF + nt * 32 * KB + (b & 3) * 8 * KB; }
      else if (kind == 2) { nt = 48 + (col0 >> 4) + w; dst = NT_OFF + nt * 32 * KB + (b & 3) * 8 * KB; valid = nt < 134; }
      else { nt = (col0 >> 5) + w; dst = WD_OFF + (nt * 86 + (row0 >> 4)) * KB; nks = row0 == 1280 ? 6 : 8; }
      f32x16 cs = zero16;
#pragma unroll
      for (int ks = 0; ks < 8; ++ks) {
        const int row = (kind == 3 ? 16 * ks + 8 * h : 32 * (ks >> 1) + 16 * h + 8 * (ks & 1)) + q4;
        const v4u f = tr2(L + row * rstride + colb, L + (row + 4) * rstride + colb);
        if (valid && ks < nks) st4(f, rw, lane * 16, dst + ks * KB);
        cs = mfma32(ones, f, cs);
      }
      if (valid && (kind == 0 || kind == 2) && lane < 32) {
        const unsigned hi = pk(cs[0], cs[0]);
        const unsigned v = pk(cs[0], cs[0] - lo16(hi));
        __builtin_amdgcn_raw_buffer_store_b32(v, rw, lane * 4, CS_OFF + nt * 512 + (b & 3) * 128, SC1);
      }
    }
  }
  v4u wph[2];
#pragma unroll
  for (int s = 0; s < 2; ++s)
#pragma unroll
    for (int e = 0; e < 4; ++e) {
      const u16* p = wphi + (2 * j + hp) * 256 + (8 * s + 2 * e) * 16 + ip;
      wph[s][e] = h == hp ? ((unsigned)p[0] | ((unsigned)p[16] << 16)) : 0u;
    }
  v2u tag;
  {
    const u64 e = (((u64)__builtin_amdgcn_readfirstlane(ep[1]) << 32) | (unsigned)__builtin_amdgcn_readfirstlane(ep[0])) * 6364136223846793005ull + 1442695040888963407ull;
    tag = v2u{(unsigned)e, (unsigned)(e >> 32)};
  }
  STAMP(1);
  publish(rw, 0, b, tag, w, lane);
  wait_flags(rw, 0, w * 64 + lane, tag, true);
  STAMP(2);

  const int q = j >> 2, qa = j & 3;
  const int ubase = 48 + (q == 0 ? 0 : q == 1 ? 22 : q == 2 ? 44 : 65) + qa;
  const bool six = qa < (q < 2 ? 2 : 1);
  const int wbase = w == 0 ? 0 : w == 1 ? 22 : w == 2 ? 44 : 65;
  dma_tile(sw, Lb, 2, 16 + j, w, lane);
  dma_tile(sw, Lb, 0, 32 + j, w, lane);
  dma_tile(sw, Lb, 1, j, w, lane);
#pragma unroll 1
  for (int l = 0; l < 2; ++l) {
    const int ph = 1 + 5 * l, sb = 3 + 20 * l;
    const int tt = 4 * g + w;
    v4u xf[32], mfix; float rstd;
    DRAIN();
    BAR();
    STAMP(sb);
    load_x(rw, 2 * l, tt, lane, xf, rstd, mfix);
    f32x16 ak = zero16, av = zero16;
#pragma unroll
    for (int ks = 0; ks < 32; ++ks) {
      ak = mfma32(lds4(L + 2 * SLOT + ks * KB + lane * 16), xf[ks], ak);
      av = mfma32(lds4(L + 0 * SLOT + ks * KB + lane * 16), xf[ks], av);
    }
    ak = mfma32(lds4(L + 2 * SLOT + 32 * KB + r * 16), mfix, ak);
    av = mfma32(lds4(L + 0 * SLOT + 32 * KB + r * 16), mfix, av);
    STAMP(sb + 1);
    {
      v4u k0, k1, v0, v1;
      pack16(ak, rstd, k0, k1);
      pack16(av, rstd, v0, v1);
      f32x16 p = mfma32(wph[1], k1, mfma32(wph[0], k0, zero16));
      unsigned ph16[16];
      softmax2(p, ph16);
      LAS u8* row = L + SCR + (w * 32 + r) * PV_ROW;
#pragma unroll
      for (int c = 0; c < 4; ++c) *(LAS v4u*)(row + h * 64 + c * 16) = v4u{ph16[4 * c], ph16[4 * c + 1], ph16[4 * c + 2], ph16[4 * c + 3]};
      *(LAS v4u*)(row + 128 + h * 32) = v0;
      *(LAS v4u*)(row + 128 + h * 32 + 16) = v1;
      if (threadIdx.x < 4) *(LAS v4u*)(L + KVI_ZERO + threadIdx.x * 16) = v4u{0, 0, 0, 0};
    }
    BAR();
    {
      f32x4 kv = {}, z = {};
      LAS u8* base = L + SCR + (8 * (lane >> 4) + ((lane & 15) >> 2)) * PV_ROW + (lane & 3) * 8;
#pragma unroll
      for (int c = 0; c < 4; ++c) {
        LAS u8* pa = base + c * 32 * PV_ROW + w * 32;
        LAS u8* pb = base + c * 32 * PV_ROW + 128 + (w >> 1) * 32;
        const v4u a = tr2(pa, pa + 4 * PV_ROW), bb = tr2(pb, pb + 4 * PV_ROW);
        kv = mfma16(a, bb, kv);
        z = mfma16(a, ones, z);
      }
      st4(__builtin_bit_cast(v4u, kv), rw, lane * 16, KV_OFF + l * MB + ((j * 4 + w) * 16 + g) * KB);
      if ((lane & 15) == 0) st4(__builtin_bit_cast(v4u, z), rw, (lane >> 4) * 16, Z_OFF + l * 64 * KB + ((j * 4 + w) * 16 + g) * 64);
    }
    STAMP(sb + 2);
    publish(rw, ph, idA, tag, w, lane);
    STAMP(sb + 3);
    dma_tile(sw, Lb, 0, ubase, w, lane);
    dma_tile(sw, Lb, 2, ubase + 8, w, lane);
    unsigned phq[16];
    {
      f32x16 aq = zero16;
#pragma unroll
      for (int ks = 0; ks < 32; ++ks) aq = mfma32(lds4(L + 1 * SLOT + ks * KB + lane * 16), xf[ks], aq);
      aq = mfma32(lds4(L + 1 * SLOT + 32 * KB + r * 16), mfix, aq);
      v4u q0, q1;
      pack16(aq, rstd, q0, q1);
      softmax2(mfma32(wph[1], q1, mfma32(wph[0], q0, zero16)), phq);
    }
    STAMP(sb + 4);
    wait_flags(rw, ph, (lane & 15) * 16 + j, tag, w == 0);
    STAMP(sb + 5);
    {
      v4u pz = ld4(rw, lane * 16, Z_OFF + l * 64 * KB + (j * 4 + w) * KB);
      v4u pp[16];
#pragma unroll
      for (int i = 0; i < 16; ++i) pp[i] = ld4(rw, lane * 16 + (i & 3) * KB, KV_OFF + l * MB + ((j * 4 + w) * 16 + (i & 12)) * KB);
      __builtin_amdgcn_sched_barrier(0);
      f32x4 z = __builtin_bit_cast(f32x4, pz);
#pragma unroll
      for (int o = 4; o < 64; o <<= 1)
#pragma unroll
        for (int e = 0; e < 4; ++e) z[e] += __shfl_xor(z[e], o);
      f32x4 kv = __builtin_bit_cast(f32x4, pp[0]);
#pragma unroll
      for (int i = 1; i < 16; ++i) kv += __builtin_bit_cast(f32x4, pp[i]);
      LAS u8* img = L + KVI + (w >> 1) * 17 * KVI_ROW + (w & 1) * 32;
      *(LAS v2u*)(img + (lane & 15) * KVI_ROW + (lane >> 4) * 8) = v2u{pk(kv[0], kv[1]), pk(kv[2], kv[3])};
      if (lane < 4) *(LAS v2u*)(img + 16 * KVI_ROW + lane * 8) = v2u{pk(z[0], z[1]), pk(z[2], z[3])};
      BAR();
      LAS u8* pn = L + (h == hp ? KVI + (h * 17 + ip) * KVI_ROW : KVI_ZERO);
      LAS u8* pd = L + (h == hp ? KVI + (h * 17 + 16) * KVI_ROW : KVI_ZERO);
      f32x16 num = zero16, den = zero16;
#pragma unroll
      for (int s = 0; s < 4; ++s) {
        const v4u bq = {phq[4 * s], phq[4 * s + 1], phq[4 * s + 2], phq[4 * s + 3]};
        num = mfma32(lds4(pn + 16 * s), bq, num);
        den = mfma32(lds4(pd + 16 * s), bq, den);
      }
      v4u o0, o1;
      pack16(num, __builtin_amdgcn_rcpf(den[0] + 1e-6f), o0, o1);
      st4(o0, rw, lane * 16, O_OFF + l * 2 * MB + tt * 32 * KB + 2 * j * KB);
      st4(o1, rw, lane * 16 + KB, O_OFF + l * 2 * MB + tt * 32 * KB + 2 * j * KB);
    }
    STAMP(sb + 6);
    publish(rw, ph + 1, idA, tag, w, lane);
    STAMP(sb + 7);
    v4u wor[2][8];
#pragma unroll
    for (int n = 0; n < 2; ++n)
#pragma unroll
      for (int i = 0; i < 8; ++i) wor[n][i] = ld4(rw, lane * 16 + (i & 3) * KB, WO_OFF + ((2 * cb + (n ^ (w & 1))) * 32 + 8 * w + (i & 4)) * KB);
    __builtin_amdgcn_sched_barrier(0);
    wait_flags(rw, ph + 1, (tb >> 1) * 16 + (lane & 15), tag, w == 0);
    STAMP(sb + 8);
    const int ttB = 2 * tb + (w >> 1), ntB = 2 * cb + (w & 1);
    {
      v4u of[2][8];
#pragma unroll
      for (int a = 1; a >= 0; --a)
#pragma unroll
        for (int i = 0; i < 8; ++i) of[a][i] = ld4(rw, lane * 16 + (i & 3) * KB, O_OFF + l * 2 * MB + (ttB ^ a) * 32 * KB + (8 * w + (i & 4)) * KB);
      __builtin_amdgcn_sched_barrier(0);
      dma_tile(sw, Lb, 1, ubase + 4, w, lane);
      f32x16 acc[2][2] = {{unpack16(res0, res1), zero16}, {zero16, zero16}};
#pragma unroll
      for (int a = 1; a >= 0; --a)
#pragma unroll
        for (int i = 0; i < 8; ++i)
#pragma unroll
          for (int n = 0; n < 2; ++n) acc[a][n] = mfma32(wor[n][i], of[a][i], acc[a][n]);
      STAMP(sb + 9);
      ksplit_reduce(L, acc, w, lane);
      pack16(acc[0][0], 1.f, res0, res1);
      store_x(rw, 2 * l + 1, ttB, ntB, lane, res0, res1);
    }
    STAMP(sb + 10);
    publish(rw, ph + 2, idB, tag, w, lane);
    STAMP(sb + 11);
    wait_flags(rw, ph + 2, g * 16 + (lane & 15), tag, w == 0);
    STAMP(sb + 12);
    {
      load_x(rw, 2 * l + 1, tt, lane, xf, rstd, mfix);
      dma_tile(sw, Lb, 3, ubase + 12, w, lane);
      const int hb = H_OFF + l * H_SZ + (tt * 88 + q * 22 + qa) * KB;
      f32x16 a0 = zero16, a1 = zero16;
#pragma unroll
      for (int ks = 0; ks < 32; ++ks) {
        a0 = mfma32(lds4(L + 0 * SLOT + ks * KB + lane * 16), xf[ks], a0);
        a1 = mfma32(lds4(L + 1 * SLOT + ks * KB + lane * 16), xf[ks], a1);
      }
      a0 = mfma32(lds4(L + 0 * SLOT + 32 * KB + r * 16), mfix, a0);
      a1 = mfma32(lds4(L + 1 * SLOT + 32 * KB + r * 16), mfix, a1);
      DRAIN();
      BAR();
      dma_tile(sw, Lb, 0, ubase + 16, w, lane);
      if (six) dma_tile(sw, Lb, 1, ubase + 20, w, lane);
      f32x16 a2 = zero16, a3 = zero16;
#pragma unroll
      for (int ks = 0; ks < 32; ++ks) a2 = mfma32(lds4(L + 2 * SLOT + ks * KB + lane * 16), xf[ks], a2);
      a2 = mfma32(lds4(L + 2 * SLOT + 32 * KB + r * 16), mfix, a2);
      st4(geglu(a0, rstd), rw, lane * 16, hb);
#pragma unroll
      for (int ks = 0; ks < 32; ++ks) a3 = mfma32(lds4(L + 3 * SLOT + ks * KB + lane * 16), xf[ks], a3);
      a3 = mfma32(lds4(L + 3 * SLOT + 32 * KB + r * 16), mfix, a3);
      st4(geglu(a1, rstd), rw, lane * 16, hb + 4 * KB);
      asm volatile("s_waitcnt vmcnt(2)" ::: "memory");
      BAR();
      if (l == 0) dma_tile(sw, Lb, 2, 16 + j, w, lane);
      a0 = zero16;
#pragma unroll
      for (int ks = 0; ks < 32; ++ks) a0 = mfma32(lds4(L + 0 * SLOT + ks * KB + lane * 16), xf[ks], a0);
      a0 = mfma32(lds4(L + 0 * SLOT + 32 * KB + r * 16), mfix, a0);
      st4(geglu(a2, rstd), rw, lane * 16, hb + 8 * KB);
      if (six) {
        a1 = zero16;
#pragma unroll
        for (int ks = 0; ks < 32; ++ks) a1 = mfma32(lds4(L + 1 * SLOT + ks * KB + lane * 16), xf[ks], a1);
        a1 = mfma32(lds4(L + 1 * SLOT + 32 * KB + r * 16), mfix, a1);
        st4(geglu(a3, rstd), rw, lane * 16, hb + 12 * KB);
        st4(geglu(a0, rstd), rw, lane * 16, hb + 16 * KB);
        st4(geglu(a1, rstd), rw, lane * 16, hb + 20 * KB);
      } else {
        st4(geglu(a3, rstd), rw, lane * 16, hb + 12 * KB);
        st4(geglu(a0, rstd), rw, lane * 16, hb + 16 * KB);
      }
    }
    STAMP(sb + 13);
    publish(rw, ph + 3, idA, tag, w, lane);
    STAMP(sb + 14);
    v4u wdr[2][22];
#pragma unroll
    for (int n = 0; n < 2; ++n)
#pragma unroll
      for (int i = 0; i < 22; ++i)
        wdr[n][i] = ld4(rw, lane * 16 + (i & 3) * KB, (i == 21 && w >= 2) ? WS_BYTES : WD_OFF + ((2 * cb + (n ^ (w & 1))) * 86 + wbase + (i & ~3)) * KB);
    __builtin_amdgcn_sched_barrier(0);
    wait_flags(rw, ph + 3, (tb >> 1) * 16 + (lane & 15), tag, w == 0);
    STAMP(sb + 15);
    {
      f32x16 acc[2][2] = {{unpack16(res0, res1), zero16}, {zero16, zero16}};
#pragma unroll
      for (int a = 1; a >= 0; --a) {
        v4u hf[22];
#pragma unroll
        for (int i = 0; i < 22; ++i)
          hf[i] = ld4(rw, lane * 16 + (i & 3) * KB, (i == 21 && w >= 2) ? WS_BYTES : H_OFF + l * H_SZ + ((ttB ^ a) * 88 + w * 22 + (i & ~3)) * KB);
        __builtin_amdgcn_sched_barrier(0);
#pragma unroll
        for (int i = 0; i < 22; ++i)
#pragma unroll
          for (int n = 0; n < 2; ++n) acc[a][n] = mfma32(wdr[n][i], hf[i], acc[a][n]);
        __builtin_amdgcn_sched_barrier(0);
      }
      STAMP(sb + 16);
      ksplit_reduce(L, acc, w, lane);
      pack16(acc[0][0], 1.f, res0, res1);
      if (l == 0) {
        store_x(rw, 2, ttB, ntB, lane, res0, res1);
        STAMP(sb + 17);
        publish(rw, ph + 4, idB, tag, w, lane);
        STAMP(sb + 18);
        if (w != 0) { dma_tile(sw, Lb, 0, 32 + j, w, lane); dma_tile(sw, Lb, 1, j, w, lane); }
        wait_flags(rw, ph + 4, g * 16 + (lane & 15), tag, w == 0);
        if (w == 0) { dma_tile(sw, Lb, 0, 32 + j, w, lane); dma_tile(sw, Lb, 1, j, w, lane); }
      }
    }
  }
  {
    u16* p = out + ((2 * tb + (w >> 1)) * 32 + r) * 512 + cb * 64 + (w & 1) * 32 + h * 16;
    *(v4u*)p = res0; *(v4u*)(p + 8) = res1;
    if (b == 0 && threadIdx.x == 0) __builtin_amdgcn_raw_buffer_store_b64(tag, rw, 0, 0, SC1);
  }
  STAMP(62);
}

#define WHICH 1
__global__ __launch_bounds__(64) void k_probe(const unsigned char* p0, const unsigned char* p1, unsigned* ws) {
  unsigned long long t0, t1, t2; unsigned v, v2;
  asm volatile("s_memtime %0\n\ts_waitcnt lgkmcnt(0)" : "=s"(t0) :: "memory");
  asm volatile("global_load_dword %0, %1, off\n\ts_waitcnt vmcnt(0)" : "=&v"(v) : "v"(p0) : "memory");
  asm volatile("s_memtime %0\n\ts_waitcnt lgkmcnt(0)" : "=s"(t1) :: "memory");
  asm volatile("global_load_dword %0, %1, off\n\ts_waitcnt vmcnt(0)" : "=&v"(v2) : "v"(p1) : "memory");
  asm volatile("s_memtime %0\n\ts_waitcnt lgkmcnt(0)" : "=s"(t2) :: "memory");
  unsigned long long cyc = (WHICH == 0 ? t1 - t0 : t2 - t1) * 4;
  asm volatile("" :: "v"(v), "v"(v2));
  unsigned long long s = __builtin_amdgcn_s_memrealtime();
  while (__builtin_amdgcn_s_memrealtime() - s < cyc) __builtin_amdgcn_s_sleep(2);
}

extern "C" void kernel_launch(void* const* d_in, const int* in_sizes, int n_in, void* d_out, int out_size, void* d_ws, size_t ws_size,
                              hipStream_t stream) {
  k_probe<<<1, 64, 0, stream>>>((const u8*)d_ws + 64 * MB, (const u8*)d_ws + 66 * MB, (unsigned*)d_ws);
  fused_block<<<256, 256, 0, stream>>>((const u16*)d_in[0], (const u16*)d_in[1], (const u16*)d_in[2], (const u16*)d_in[3], (const u16*)d_in[5],
                                        (const u16*)d_in[6], (const u16*)d_in[7], (u16*)d_out, (u8*)d_ws);
}
```

```cpp
#define LIGHT
#define AMP_MODE 0
#include <hip/hip_runtime.h>
#include <stdint.h>

typedef unsigned short u16;
typedef unsigned char u8;
typedef unsigned long long u64;
typedef unsigned v4u __attribute__((ext_vector_type(4)));
typedef unsigned v2u __attribute__((ext_vector_type(2)));
typedef float f32x2 __attribute__((ext_vector_type(2)));
typedef float f32x4 __attribute__((ext_vector_type(4)));
typedef float f32x16 __attribute__((ext_vector_type(16)));
typedef short s16x4 __attribute__((ext_vector_type(4)));
typedef __bf16 bf16x2 __attribute__((ext_vector_type(2)));
typedef __bf16 bf16x8 __attribute__((ext_vector_type(8)));
#define LAS __attribute__((address_space(3)))
#define DI __device__ __forceinline__
typedef __amdgpu_buffer_rsrc_t rsrc_t;

constexpr int KB = 1024, MB = 1024 * 1024;
constexpr int FLAG_OFF = 4096, X_OFF = 1 * MB, ST_OFF = 9 * MB, O_OFF = 10 * MB, H_OFF = 14 * MB, H_SZ = 64 * 88 * KB,
              KV_OFF = 25 * MB, Z_OFF = 27 * MB, NT_OFF = 28 * MB, CS_OFF = 33 * MB, WO_OFF = 34 * MB, WD_OFF = 35 * MB,
              DBG_OFF = 40 * MB, WS_BYTES = 48 * MB;
constexpr int SLOT = 33280, SCR = 3 * SLOT;
constexpr int PV_ROW = 208, KVI = SCR + 28 * KB, KVI_ROW = 80, KVI_ZERO = KVI + 2 * 17 * KVI_ROW;
constexpr int SC1 = 16;
constexpr float LOG2E = 1.4426950408889634f;

DI f32x16 mfma32(v4u a, v4u b, f32x16 c) {
  return __builtin_amdgcn_mfma_f32_32x32x16_bf16(__builtin_bit_cast(bf16x8, a), __builtin_bit_cast(bf16x8, b), c, 0, 0, 0);
}
DI f32x4 mfma16(v4u a, v4u b, f32x4 c) {
  return __builtin_amdgcn_mfma_f32_16x16x32_bf16(__builtin_bit_cast(bf16x8, a), __builtin_bit_cast(bf16x8, b), c, 0, 0, 0);
}
DI unsigned pk(float a, float b) { f32x2 v = {a, b}; return __builtin_bit_cast(unsigned, __builtin_convertvector(v, bf16x2)); }
DI float lo16(unsigned u) { return __uint_as_float(u << 16); }
DI float hi16(unsigned u) { return __uint_as_float(u & 0xffff0000u); }
DI float dot2(unsigned a, unsigned b, float c) {
  return __builtin_amdgcn_fdot2_f32_bf16(__builtin_bit_cast(bf16x2, a), __builtin_bit_cast(bf16x2, b), c, false);
}
DI v4u ld4(rsrc_t r, int voff, int soff) { return __builtin_amdgcn_raw_buffer_load_b128(r, voff, soff, SC1); }
DI void st4(v4u v, rsrc_t r, int voff, int soff) { __builtin_amdgcn_raw_buffer_store_b128(v, r, voff, soff, SC1); }
DI v4u lds4(LAS u8* p) { return *(LAS v4u*)p; }
DI v4u tr2(LAS u8* p0, LAS u8* p1) {
  s16x4 a = __builtin_amdgcn_ds_read_tr16_b64_v4i16((LAS s16x4*)p0), b = __builtin_amdgcn_ds_read_tr16_b64_v4i16((LAS s16x4*)p1);
  v2u x = __builtin_bit_cast(v2u, a), y = __builtin_bit_cast(v2u, b);
  return v4u{x[0], x[1], y[0], y[1]};
}
DI void dma16(v4u srd, unsigned lds, int voff, int soff) {
  asm volatile("s_mov_b32 m0, %0\n\ts_nop 4\n\tbuffer_load_dwordx4 %1, %2, %3 offen sc1 lds" ::"s"(lds), "v"(voff), "s"(srd), "s"(soff) : "memory");
}
DI void dma4(v4u srd, unsigned lds, int voff, int soff) {
  asm volatile("s_mov_b32 m0, %0\n\ts_nop 4\n\tbuffer_load_dword %1, %2, %3 offen sc1 lds" ::"s"(lds), "v"(voff), "s"(srd), "s"(soff) : "memory");
}
DI v4u mk_srd(const void* p, unsigned bytes) {
  u64 a = (u64)p;
  return v4u{(unsigned)a, (unsigned)(a >> 32) & 0xffffu, bytes, 0x00020000u};
}
#define DRAIN() asm volatile("s_waitcnt vmcnt(0)" ::: "memory")
#define BAR() do { asm volatile("s_waitcnt lgkmcnt(0)" ::: "memory"); __builtin_amdgcn_s_barrier(); asm volatile("" ::: "memory"); } while (0)
#define STAMP(i)

DI void publish(rsrc_t rw, int ph, int id, v2u tag, int w, int lane) {
  DRAIN();
  BAR();
  if (w == 0) {
    if (lane == 0) __builtin_amdgcn_raw_buffer_store_b64(tag, rw, FLAG_OFF + (ph * 256 + id) * 128, 0, SC1);
    DRAIN();
  }
}
DI void wait_flags(rsrc_t rw, int ph, int id_lane, v2u tag, bool poller) {
  if (poller) {
    for (int sp = 0; sp < (1 << 22); ++sp) {
      asm volatile("" ::: "memory");
      v2u f = __builtin_amdgcn_raw_buffer_load_b64(rw, FLAG_OFF + (ph * 256 + id_lane) * 128, 0, SC1);
      if (__all(f[0] == tag[0] && f[1] == tag[1])) break;
      __builtin_amdgcn_s_sleep(1);
    }
  }
  BAR();
}
DI void dma_tile(v4u srd, unsigned lds_base, int slot, int nt, int w, int lane) {
  int ntv = nt, wv = w;
  asm volatile("" : "+s"(ntv), "+s"(wv));
  unsigned l0 = lds_base + slot * SLOT + wv * 8 * KB;
  int s0 = NT_OFF + ntv * 32 * KB + wv * 8 * KB;
  asm volatile("s_mov_b32 m0, %0\n\ts_nop 4\n\t"
               "buffer_load_dwordx4 %2, %3, %1 offen sc1 lds\n\t"
               "buffer_load_dwordx4 %2, %3, %1 offen offset:1024 sc1 lds\n\t"
               "buffer_load_dwordx4 %2, %3, %1 offen offset:2048 sc1 lds\n\t"
               "buffer_load_dwordx4 %2, %3, %1 offen offset:3072 sc1 lds\n\t"
               "s_add_u32 m0, %0, 4096\n\ts_add_u32 %1, %1, 4096\n\t"
               "buffer_load_dwordx4 %2, %3, %1 offen sc1 lds\n\t"
               "buffer_load_dwordx4 %2, %3, %1 offen offset:1024 sc1 lds\n\t"
               "buffer_load_dwordx4 %2, %3, %1 offen offset:2048 sc1 lds\n\t"
               "buffer_load_dwordx4 %2, %3, %1 offen offset:3072 sc1 lds"
               : "+s"(l0), "+s"(s0) : "v"(lane * 16), "s"(srd) : "memory", "scc");
  dma4(srd, lds_base + slot * SLOT + 32 * KB + (wv & 1) * 256, (lane & 3) * 128 + (lane >> 2) * 4, CS_OFF + ntv * 512 + (wv & 1) * 64);
}
DI void softmax2(const f32x16& p, unsigned (&o)[16]) {
  float mx = p[0], mn = p[0];
#pragma unroll
  for (int i = 1; i < 16; ++i) { mx = fmaxf(mx, p[i]); mn = fminf(mn, p[i]); }
  const float a = -mx * LOG2E, b = mn * LOG2E;
  float e1[16], e2[16], s1 = 0.f, s2 = 0.f;
#pragma unroll
  for (int i = 0; i < 16; ++i) {
    e1[i] = __builtin_amdgcn_exp2f(fmaf(p[i], LOG2E, a)); s1 += e1[i];
    e2[i] = __builtin_amdgcn_exp2f(fmaf(p[i], -LOG2E, b)); s2 += e2[i];
  }
  const float r1 = __builtin_amdgcn_rcpf(s1), r2 = __builtin_amdgcn_rcpf(s2);
#pragma unroll
  for (int i = 0; i < 8; ++i) { o[i] = pk(e1[2 * i] * r1, e1[2 * i + 1] * r1); o[8 + i] = pk(e2[2 * i] * r2, e2[2 * i + 1] * r2); }
}
DI void pack16(const f32x16& a, float s, v4u& f0, v4u& f1) {
#pragma unroll
  for (int i = 0; i < 4; ++i) { f0[i] = pk(a[2 * i] * s, a[2 * i + 1] * s); f1[i] = pk(a[8 + 2 * i] * s, a[9 + 2 * i] * s); }
}
DI float xhalf_sum(float v) {
  auto r = __builtin_amdgcn_permlane32_swap(__float_as_uint(v), __float_as_uint(v), false, false);
  return __uint_as_float(r[0]) + __uint_as_float(r[1]);
}
DI void load_x(rsrc_t rw, int xi, int tt, int lane, v4u (&xf)[32], float& rstd, v4u& mfix) {
  v2u st[8];
#pragma unroll
  for (int i = 0; i < 8; ++i)
    st[i] = __builtin_amdgcn_raw_buffer_load_b64(rw, (lane >> 5) * 2048 + (lane & 31) * 8 + i * 256, ST_OFF + xi * 256 * KB + tt * 4096, SC1);
#pragma unroll
  for (int i = 0; i < 32; ++i) xf[i] = ld4(rw, lane * 16 + (i & 3) * KB, X_OFF + xi * 2 * MB + tt * 32 * KB + (i >> 2) * 4 * KB);
  __builtin_amdgcn_sched_barrier(0);
  float s = 0.f, q = 0.f;
#pragma unroll
  for (int i = 0; i < 8; ++i) { s += __uint_as_float(st[i][0]); q += __uint_as_float(st[i][1]); }
  s = xhalf_sum(s); q = xhalf_sum(q);
  const float mean = s * (1.f / 512.f);
  const float var = fmaxf(q * (1.f / 512.f) - mean * mean, 0.f);
  rstd = __builtin_amdgcn_rsqf(var + 1e-5f);
  const unsigned mh = pk(-mean, -mean);
  const float ml = -mean - lo16(mh);
  const unsigned m = lane < 32 ? mh : pk(ml, ml);
  mfix = v4u{m, m, m, m};
}
DI v4u geglu(const f32x16& a, float rstd) {
  const float ir = __builtin_amdgcn_rcpf(rstd);
  constexpr float K1 = -2.f * 0.7978845608028654f * LOG2E, K2 = K1 * 0.044715f;
  float o[8];
#pragma unroll
  for (int i = 0; i < 8; ++i) {
    const float g = a[i] * rstd;
    const float e = __builtin_amdgcn_exp2f(g * fmaf(g * g, K2, K1));
    o[i] = g * a[8 + i] * __builtin_amdgcn_rcpf(fmaf(e, ir, ir));
  }
  return v4u{pk(o[0], o[1]), pk(o[2], o[3]), pk(o[4], o[5]), pk(o[6], o[7])};
}
DI void ksplit_reduce(LAS u8* L, f32x16 (&acc)[2][2], int w, int lane) {
#pragma unroll
  for (int t = 1; t < 4; ++t)
#pragma unroll
    for (int c = 0; c < 4; ++c) {
      const f32x16& a = acc[t >> 1][t & 1];
      *(LAS f32x4*)(L + SCR + (((w ^ t) * 3 + t - 1) * 4 + c) * KB + lane * 16) = f32x4{a[4 * c], a[4 * c + 1], a[4 * c + 2], a[4 * c + 3]};
    }
  BAR();
#pragma unroll
  for (int t = 0; t < 3; ++t)
#pragma unroll
    for (int c = 0; c < 4; ++c) {
      const f32x4 v = *(LAS f32x4*)(L + SCR + ((w * 3 + t) * 4 + c) * KB + lane * 16);
#pragma unroll
      for (int e = 0; e < 4; ++e) acc[0][0][4 * c + e] += v[e];
    }
}
DI void store_x(rsrc_t rw, int xi, int tt, int nt, int lane, v4u f0, v4u f1) {
  st4(f0, rw, lane * 16, X_OFF + xi * 2 * MB + tt * 32 * KB + nt * 2 * KB);
  st4(f1, rw, lane * 16 + KB, X_OFF + xi * 2 * MB + tt * 32 * KB + nt * 2 * KB);
  float s = 0.f, q = 0.f;
#pragma unroll
  for (int i = 0; i < 4; ++i) {
    s = dot2(f0[i], 0x3f803f80u, s); s = dot2(f1[i], 0x3f803f80u, s);
    q = dot2(f0[i], f0[i], q); q = dot2(f1[i], f1[i], q);
  }
  s = xhalf_sum(s); q = xhalf_sum(q);
  if (lane < 32)
    __builtin_amdgcn_raw_buffer_store_b64(v2u{__float_as_uint(s), __float_as_uint(q)}, rw, lane * 8, ST_OFF + xi * 256 * KB + tt * 4096 + nt * 256, SC1);
}
DI f32x16 unpack16(v4u f0, v4u f1) {
  f32x16 r;
#pragma unroll
  for (int i = 0; i < 4; ++i) { r[2 * i] = lo16(f0[i]); r[2 * i + 1] = hi16(f0[i]); r[8 + 2 * i] = lo16(f1[i]); r[9 + 2 * i] = hi16(f1[i]); }
  return r;
}

__global__ __launch_bounds__(256, 1) void fused_block(const u16* __restrict__ x, const u16* __restrict__ wqkv, const u16* __restrict__ wphi,
                                                      const u16* __restrict__ wo, const u16* __restrict__ wg, const u16* __restrict__ wu,
                                                      const u16* __restrict__ wd, u16* __restrict__ out, u8* __restrict__ ws) {
  __shared__ __attribute__((aligned(16))) u8 smem[163840];
  LAS u8* L = (LAS u8*)smem;
  const unsigned Lb = (unsigned)(size_t)L;
  const int b = blockIdx.x, lane = threadIdx.x & 63, w = __builtin_amdgcn_readfirstlane(threadIdx.x >> 6);
  const int r = lane & 31, h = lane >> 5;
  const int xcd = b & 7, m = b >> 3;
  const int g = (xcd >> 1) * 4 + (m & 3), j = (xcd & 1) * 8 + (m >> 2);
  const int tb = xcd * 4 + (m & 3), cb = m >> 2;
  const int idA = g * 16 + j, idB = tb * 8 + cb;
  const rsrc_t rw = __builtin_amdgcn_make_buffer_rsrc((void*)ws, 0, WS_BYTES, 0x00020000);
  const v4u sw = mk_srd(ws, WS_BYTES);
  const f32x16 zero16 = {};
  const v4u ones = {0x3f803f80u, 0x3f803f80u, 0x3f803f80u, 0x3f803f80u};
  const u64 rt0 = __builtin_amdgcn_s_memrealtime();
  STAMP(0);
  {
    u64 pc; asm volatile("s_getpc_b64 %0" : "=s"(pc));
    if (w == 3 && lane < 8) { unsigned pf = *(const volatile unsigned*)((pc & ~127ull) + (m * 8 + lane) * 128); asm volatile("" ::"v"(pf)); }
  }
  const v2u ep = __builtin_amdgcn_raw_buffer_load_b64(rw, 0, 0, SC1);

  const int hp = (r >> 2) & 1, ip = (r & 3) + 4 * (r >> 3);
  v4u res0, res1;
  {
    const u16* src; int ld, row0, col0, nrows = 512, kind;
    if (b < 48) { kind = 0; src = wqkv; ld = 1536; row0 = (b & 3) * 128; col0 = (b >> 2) * 128; }
    else if (b < 64) { kind = 1; src = wo; ld = 512; row0 = (b & 3) * 128; col0 = ((b - 48) >> 2) * 128; }
    else if (b < 152) { kind = 2; src = wg; ld = 1376; row0 = (b & 3) * 128; col0 = ((b - 64) >> 2) * 64; }
    else { kind = 3; src = wd; ld = 512; nrows = 1376; row0 = ((b - 152) >> 2) * 128; col0 = (b & 3) * 128; }
    if (b < 196) {
      if (kind != 2) {
        const v4u sr = mk_srd(src, nrows * ld * 2);
#pragma unroll
        for (int i = 0; i < 8; ++i)
          dma16(sr, Lb + (w * 8 + i) * KB, ((lane >> 4) * ld + (lane & 15) * 8) * 2, ((row0 + (w * 8 + i) * 4) * ld + col0) * 2);
      } else {
        const v4u sr = mk_srd(w < 2 ? wg : wu, 512 * 1376 * 2);
#pragma unroll
        for (int i = 0; i < 8; ++i)
          dma16(sr, Lb + (w * 8 + i) * KB, ((lane >> 3) * ld + (lane & 7) * 8) * 2, ((row0 + ((w & 1) * 8 + i) * 8) * ld + col0) * 2);
      }
    }
    {
      const int t8 = lane & 7, c = lane >> 3, tok = g * 128 + j * 8 + t8, tt = tok >> 5, rr = tok & 31;
#pragma unroll
      for (int i = 0; i < 2; ++i) {
        const int pc = 16 * w + 8 * i + c;
        const v4u v = *(const v4u*)(x + tok * 512 + pc * 8);
        const int ks = 2 * (pc >> 2) + (pc & 1), hh = (pc >> 1) & 1;
        st4(v, rw, tt * 32 * KB + ks * KB + (hh * 32 + rr) * 16, X_OFF);
        float s = 0.f, q = 0.f;
#pragma unroll
        for (int e = 0; e < 4; ++e) { s = dot2(v[e], 0x3f803f80u, s); q = dot2(v[e], v[e], q); }
        s += __shfl_xor(s, 8); q += __shfl_xor(q, 8);
        s += __shfl_xor(s, 16); q += __shfl_xor(q, 16);
        if ((c & 3) == 0)
          __builtin_amdgcn_raw_buffer_store_b64(v2u{__float_as_uint(s), __float_as_uint(q)}, rw, tt * 4096 + (pc >> 2) * 256 + rr * 8, ST_OFF, SC1);
      }
    }
    {
      const u16* p = x + ((2 * tb + (w >> 1)) * 32 + r) * 512 + cb * 64 + (w & 1) * 32 + h * 16;
      res0 = *(const v4u*)p; res1 = *(const v4u*)(p + 8);
    }
    DRAIN();
    BAR();
    if (b < 196) {
      const int G = (lane >> 4) & 1, q4 = (lane & 15) >> 2, p4 = lane & 3;
      int colb, rstride, dst, nks = 8;
      if (kind != 2) { colb = (w * 32 + 16 * (p4 & 1) + 4 * (2 * G + (p4 >> 1))) * 2; rstride = 256; }
      else { colb = G * 16 * KB + (w * 16 + 8 * (p4 & 1) + 4 * (p4 >> 1)) * 2; rstride = 128; }
      int nt;
      bool valid = true;
      if (kind == 0) { nt = (col0 >> 5) + w; dst = NT_OFF + nt * 32 * KB + (b & 3) * 8 * KB; }
      else if (kind == 1) { nt = (col0 >> 5) + w; dst = WO_OFF + nt * 32 * KB + (b & 3) * 8 * KB; }
      else if (kind == 2) { nt = 48 + (col0 >> 4) + w; dst = NT_OFF + nt * 32 * KB + (b & 3) * 8 * KB; valid = nt < 134; }
      else { nt = (col0 >> 5) + w; dst = WD_OFF + (nt * 86 + (row0 >> 4)) * KB; nks = row0 == 1280 ? 6 : 8; }
      f32x16 cs = zero16;
#pragma unroll
      for (int ks = 0; ks < 8; ++ks) {
        const int row = (kind == 3 ? 16 * ks + 8 * h : 32 * (ks >> 1) + 16 * h + 8 * (ks & 1)) + q4;
        const v4u f = tr2(L + row * rstride + colb, L + (row + 4) * rstride + colb);
        if (valid && ks < nks) st4(f, rw, lane * 16, dst + ks * KB);
        cs = mfma32(ones, f, cs);
      }
      if (valid && (kind == 0 || kind == 2) && lane < 32) {
        const unsigned hi = pk(cs[0], cs[0]);
        const unsigned v = pk(cs[0], cs[0] - lo16(hi));
        __builtin_amdgcn_raw_buffer_store_b32(v, rw, lane * 4, CS_OFF + nt * 512 + (b & 3) * 128, SC1);
      }
    }
  }
  v4u wph[2];
#pragma unroll
  for (int s = 0; s < 2; ++s)
#pragma unroll
    for (int e = 0; e < 4; ++e) {
      const u16* p = wphi + (2 * j + hp) * 256 + (8 * s + 2 * e) * 16 + ip;
      wph[s][e] = h == hp ? ((unsigned)p[0] | ((unsigned)p[16] << 16)) : 0u;
    }
  v2u tag;
  {
    const u64 e = (((u64)__builtin_amdgcn_readfirstlane(ep[1]) << 32) | (unsigned)__builtin_amdgcn_readfirstlane(ep[0])) * 6364136223846793005ull + 1442695040888963407ull;
    tag = v2u{(unsigned)e, (unsigned)(e >> 32)};
  }
  STAMP(1);
  publish(rw, 0, b, tag, w, lane);
  wait_flags(rw, 0, w * 64 + lane, tag, true);
  STAMP(2);

  const int q = j >> 2, qa = j & 3;
  const int ubase = 48 + (q == 0 ? 0 : q == 1 ? 22 : q == 2 ? 44 : 65) + qa;
  const bool six = qa < (q < 2 ? 2 : 1);
  const int wbase = w == 0 ? 0 : w == 1 ? 22 : w == 2 ? 44 : 65;
  dma_tile(sw, Lb, 2, 16 + j, w, lane);
  dma_tile(sw, Lb, 0, 32 + j, w, lane);
  dma_tile(sw, Lb, 1, j, w, lane);
#pragma unroll 1
  for (int l = 0; l < 2; ++l) {
    const int ph = 1 + 5 * l, sb = 3 + 20 * l;
    const int tt = 4 * g + w;
    v4u xf[32], mfix; float rstd;
    DRAIN();
    BAR();
    STAMP(sb);
    load_x(rw, 2 * l, tt, lane, xf, rstd, mfix);
    f32x16 ak = zero16, av = zero16;
#pragma unroll
    for (int ks = 0; ks < 32; ++ks) {
      ak = mfma32(lds4(L + 2 * SLOT + ks * KB + lane * 16), xf[ks], ak);
      av = mfma32(lds4(L + 0 * SLOT + ks * KB + lane * 16), xf[ks], av);
    }
    ak = mfma32(lds4(L + 2 * SLOT + 32 * KB + r * 16), mfix, ak);
    av = mfma32(lds4(L + 0 * SLOT + 32 * KB + r * 16), mfix, av);
    STAMP(sb + 1);
    {
      v4u k0, k1, v0, v1;
      pack16(ak, rstd, k0, k1);
      pack16(av, rstd, v0, v1);
      f32x16 p = mfma32(wph[1], k1, mfma32(wph[0], k0, zero16));
      unsigned ph16[16];
      softmax2(p, ph16);
      LAS u8* row = L + SCR + (w * 32 + r) * PV_ROW;
#pragma unroll
      for (int c = 0; c < 4; ++c) *(LAS v4u*)(row + h * 64 + c * 16) = v4u{ph16[4 * c], ph16[4 * c + 1], ph16[4 * c + 2], ph16[4 * c + 3]};
      *(LAS v4u*)(row + 128 + h * 32) = v0;
      *(LAS v4u*)(row + 128 + h * 32 + 16) = v1;
      if (threadIdx.x < 4) *(LAS v4u*)(L + KVI_ZERO + threadIdx.x * 16) = v4u{0, 0, 0, 0};
    }
    BAR();
    {
      f32x4 kv = {}, z = {};
      LAS u8* base = L + SCR + (8 * (lane >> 4) + ((lane & 15) >> 2)) * PV_ROW + (lane & 3) * 8;
#pragma unroll
      for (int c = 0; c < 4; ++c) {
        LAS u8* pa = base + c * 32 * PV_ROW + w * 32;
        LAS u8* pb = base + c * 32 * PV_ROW + 128 + (w >> 1) * 32;
        const v4u a = tr2(pa, pa + 4 * PV_ROW), bb = tr2(pb, pb + 4 * PV_ROW);
        kv = mfma16(a, bb, kv);
        z = mfma16(a, ones, z);
      }
      st4(__builtin_bit_cast(v4u, kv), rw, lane * 16, KV_OFF + l * MB + ((j * 4 + w) * 16 + g) * KB);
      if ((lane & 15) == 0) st4(__builtin_bit_cast(v4u, z), rw, (lane >> 4) * 16, Z_OFF + l * 64 * KB + ((j * 4 + w) * 16 + g) * 64);
    }
    STAMP(sb + 2);
    publish(rw, ph, idA, tag, w, lane);
    STAMP(sb + 3);
    dma_tile(sw, Lb, 0, ubase, w, lane);
    dma_tile(sw, Lb, 2, ubase + 8, w, lane);
    unsigned phq[16];
    {
      f32x16 aq = zero16;
#pragma unroll
      for (int ks = 0; ks < 32; ++ks) aq = mfma32(lds4(L + 1 * SLOT + ks * KB + lane * 16), xf[ks], aq);
      aq = mfma32(lds4(L + 1 * SLOT + 32 * KB + r * 16), mfix, aq);
      v4u q0, q1;
      pack16(aq, rstd, q0, q1);
      softmax2(mfma32(wph[1], q1, mfma32(wph[0], q0, zero16)), phq);
    }
    STAMP(sb + 4);
    wait_flags(rw, ph, (lane & 15) * 16 + j, tag, w == 0);
    STAMP(sb + 5);
    {
      v4u pz = ld4(rw, lane * 16, Z_OFF + l * 64 * KB + (j * 4 + w) * KB);
      v4u pp[16];
#pragma unroll
      for (int i = 0; i < 16; ++i) pp[i] = ld4(rw, lane * 16 + (i & 3) * KB, KV_OFF + l * MB + ((j * 4 + w) * 16 + (i & 12)) * KB);
      __builtin_amdgcn_sched_barrier(0);
      f32x4 z = __builtin_bit_cast(f32x4, pz);
#pragma unroll
      for (int o = 4; o < 64; o <<= 1)
#pragma unroll
        for (int e = 0; e < 4; ++e) z[e] += __shfl_xor(z[e], o);
      f32x4 kv = __builtin_bit_cast(f32x4, pp[0]);
#pragma unroll
      for (int i = 1; i < 16; ++i) kv += __builtin_bit_cast(f32x4, pp[i]);
      LAS u8* img = L + KVI + (w >> 1) * 17 * KVI_ROW + (w & 1) * 32;
      *(LAS v2u*)(img + (lane & 15) * KVI_ROW + (lane >> 4) * 8) = v2u{pk(kv[0], kv[1]), pk(kv[2], kv[3])};
      if (lane < 4) *(LAS v2u*)(img + 16 * KVI_ROW + lane * 8) = v2u{pk(z[0], z[1]), pk(z[2], z[3])};
      BAR();
      LAS u8* pn = L + (h == hp ? KVI + (h * 17 + ip) * KVI_ROW : KVI_ZERO);
      LAS u8* pd = L + (h == hp ? KVI + (h * 17 + 16) * KVI_ROW : KVI_ZERO);
      f32x16 num = zero16, den = zero16;
#pragma unroll
      for (int s = 0; s < 4; ++s) {
        const v4u bq = {phq[4 * s], phq[4 * s + 1], phq[4 * s + 2], phq[4 * s + 3]};
        num = mfma32(lds4(pn + 16 * s), bq, num);
        den = mfma32(lds4(pd + 16 * s), bq, den);
      }
      v4u o0, o1;
      pack16(num, __builtin_amdgcn_rcpf(den[0] + 1e-6f), o0, o1);
      st4(o0, rw, lane * 16, O_OFF + l * 2 * MB + tt * 32 * KB + 2 * j * KB);
      st4(o1, rw, lane * 16 + KB, O_OFF + l * 2 * MB + tt * 32 * KB + 2 * j * KB);
    }
    STAMP(sb + 6);
    publish(rw, ph + 1, idA, tag, w, lane);
    STAMP(sb + 7);
    v4u wor[2][8];
#pragma unroll
    for (int n = 0; n < 2; ++n)
#pragma unroll
      for (int i = 0; i < 8; ++i) wor[n][i] = ld4(rw, lane * 16 + (i & 3) * KB, WO_OFF + ((2 * cb + (n ^ (w & 1))) * 32 + 8 * w + (i & 4)) * KB);
    __builtin_amdgcn_sched_barrier(0);
    wait_flags(rw, ph + 1, (tb >> 1) * 16 + (lane & 15), tag, w == 0);
    STAMP(sb + 8);
    const int ttB = 2 * tb + (w >> 1), ntB = 2 * cb + (w & 1);
    {
      v4u of[2][8];
#pragma unroll
      for (int a = 1; a >= 0; --a)
#pragma unroll
        for (int i = 0; i < 8; ++i) of[a][i] = ld4(rw, lane * 16 + (i & 3) * KB, O_OFF + l * 2 * MB + (ttB ^ a) * 32 * KB + (8 * w + (i & 4)) * KB);
      __builtin_amdgcn_sched_barrier(0);
      dma_tile(sw, Lb, 1, ubase + 4, w, lane);
      f32x16 acc[2][2] = {{unpack16(res0, res1), zero16}, {zero16, zero16}};
#pragma unroll
      for (int a = 1; a >= 0; --a)
#pragma unroll
        for (int i = 0; i < 8; ++i)
#pragma unroll
          for (int n = 0; n < 2; ++n) acc[a][n] = mfma32(wor[n][i], of[a][i], acc[a][n]);
      STAMP(sb + 9);
      ksplit_reduce(L, acc, w, lane);
      pack16(acc[0][0], 1.f, res0, res1);
      store_x(rw, 2 * l + 1, ttB, ntB, lane, res0, res1);
    }
    STAMP(sb + 10);
    publish(rw, ph + 2, idB, tag, w, lane);
    STAMP(sb + 11);
    wait_flags(rw, ph + 2, g * 16 + (lane & 15), tag, w == 0);
    STAMP(sb + 12);
    {
      load_x(rw, 2 * l + 1, tt, lane, xf, rstd, mfix);
      dma_tile(sw, Lb, 3, ubase + 12, w, lane);
      const int hb = H_OFF + l * H_SZ + (tt * 88 + q * 22 + qa) * KB;
      f32x16 a0 = zero16, a1 = zero16;
#pragma unroll
      for (int ks = 0; ks < 32; ++ks) {
        a0 = mfma32(lds4(L + 0 * SLOT + ks * KB + lane * 16), xf[ks], a0);
        a1 = mfma32(lds4(L + 1 * SLOT + ks * KB + lane * 16), xf[ks], a1);
      }
      a0 = mfma32(lds4(L + 0 * SLOT + 32 * KB + r * 16), mfix, a0);
      a1 = mfma32(lds4(L + 1 * SLOT + 32 * KB + r * 16), mfix, a1);
      DRAIN();
      BAR();
      dma_tile(sw, Lb, 0, ubase + 16, w, lane);
      if (six) dma_tile(sw, Lb, 1, ubase + 20, w, lane);
      f32x16 a2 = zero16, a3 = zero16;
#pragma unroll
      for (int ks = 0; ks < 32; ++ks) a2 = mfma32(lds4(L + 2 * SLOT + ks * KB + lane * 16), xf[ks], a2);
      a2 = mfma32(lds4(L + 2 * SLOT + 32 * KB + r * 16), mfix, a2);
      st4(geglu(a0, rstd), rw, lane * 16, hb);
#pragma unroll
      for (int ks = 0; ks < 32; ++ks) a3 = mfma32(lds4(L + 3 * SLOT + ks * KB + lane * 16), xf[ks], a3);
      a3 = mfma32(lds4(L + 3 * SLOT + 32 * KB + r * 16), mfix, a3);
      st4(geglu(a1, rstd), rw, lane * 16, hb + 4 * KB);
      asm volatile("s_waitcnt vmcnt(2)" ::: "memory");
      BAR();
      if (l == 0) dma_tile(sw, Lb, 2, 16 + j, w, lane);
      a0 = zero16;
#pragma unroll
      for (int ks = 0; ks < 32; ++ks) a0 = mfma32(lds4(L + 0 * SLOT + ks * KB + lane * 16), xf[ks], a0);
      a0 = mfma32(lds4(L + 0 * SLOT + 32 * KB + r * 16), mfix, a0);
      st4(geglu(a2, rstd), rw, lane * 16, hb + 8 * KB);
      if (six) {
        a1 = zero16;
#pragma unroll
        for (int ks = 0; ks < 32; ++ks) a1 = mfma32(lds4(L + 1 * SLOT + ks * KB + lane * 16), xf[ks], a1);
        a1 = mfma32(lds4(L + 1 * SLOT + 32 * KB + r * 16), mfix, a1);
        st4(geglu(a3, rstd), rw, lane * 16, hb + 12 * KB);
        st4(geglu(a0, rstd), rw, lane * 16, hb + 16 * KB);
        st4(geglu(a1, rstd), rw, lane * 16, hb + 20 * KB);
      } else {
        st4(geglu(a3, rstd), rw, lane * 16, hb + 12 * KB);
        st4(geglu(a0, rstd), rw, lane * 16, hb + 16 * KB);
      }
    }
    STAMP(sb + 13);
    publish(rw, ph + 3, idA, tag, w, lane);
    STAMP(sb + 14);
    v4u wdr[2][22];
#pragma unroll
    for (int n = 0; n < 2; ++n)
#pragma unroll
      for (int i = 0; i < 22; ++i)
        wdr[n][i] = ld4(rw, lane * 16 + (i & 3) * KB, (i == 21 && w >= 2) ? WS_BYTES : WD_OFF + ((2 * cb + (n ^ (w & 1))) * 86 + wbase + (i & ~3)) * KB);
    __builtin_amdgcn_sched_barrier(0);
    wait_flags(rw, ph + 3, (tb >> 1) * 16 + (lane & 15), tag, w == 0);
    STAMP(sb + 15);
    {
      f32x16 acc[2][2] = {{unpack16(res0, res1), zero16}, {zero16, zero16}};
#pragma unroll
      for (int a = 1; a >= 0; --a) {
        v4u hf[22];
#pragma unroll
        for (int i = 0; i < 22; ++i)
          hf[i] = ld4(rw, lane * 16 + (i & 3) * KB, (i == 21 && w >= 2) ? WS_BYTES : H_OFF + l * H_SZ + ((ttB ^ a) * 88 + w * 22 + (i & ~3)) * KB);
        __builtin_amdgcn_sched_barrier(0);
#pragma unroll
        for (int i = 0; i < 22; ++i)
#pragma unroll
          for (int n = 0; n < 2; ++n) acc[a][n] = mfma32(wdr[n][i], hf[i], acc[a][n]);
        __builtin_amdgcn_sched_barrier(0);
      }
      STAMP(sb + 16);
      ksplit_reduce(L, acc, w, lane);
      pack16(acc[0][0], 1.f, res0, res1);
      if (l == 0) {
        store_x(rw, 2, ttB, ntB, lane, res0, res1);
        STAMP(sb + 17);
        publish(rw, ph + 4, idB, tag, w, lane);
        STAMP(sb + 18);
        if (w != 0) { dma_tile(sw, Lb, 0, 32 + j, w, lane); dma_tile(sw, Lb, 1, j, w, lane); }
        wait_flags(rw, ph + 4, g * 16 + (lane & 15), tag, w == 0);
        if (w == 0) { dma_tile(sw, Lb, 0, 32 + j, w, lane); dma_tile(sw, Lb, 1, j, w, lane); }
      }
    }
  }
  {
    u16* p = out + ((2 * tb + (w >> 1)) * 32 + r) * 512 + cb * 64 + (w & 1) * 32 + h * 16;
    const rsrc_t ro = __builtin_amdgcn_make_buffer_rsrc((void*)out, 0, 2 * MB, 0x00020000); st4(res0, ro, (int)((u8*)p - (u8*)out), 0); st4(res1, ro, (int)((u8*)p - (u8*)out) + 16, 0);
    if (b == 0 && threadIdx.x == 0) __builtin_amdgcn_raw_buffer_store_b64(tag, rw, 0, 0, SC1);
  }
  STAMP(62);
  DRAIN();
  if (threadIdx.x == 0) { u64* p = (u64*)(ws + DBG_OFF + blockIdx.x * 512); p[0] = rt0; p[1] = __builtin_amdgcn_s_memrealtime(); }
}

__global__ __launch_bounds__(64) void k_pre(u8* ws) { if (threadIdx.x == 0) *(u64*)(ws + DBG_OFF + MB) = __builtin_amdgcn_s_memrealtime(); }
__global__ __launch_bounds__(64) void k_nop(u8* ws) { }
__global__ __launch_bounds__(256) void k_amp(const u8* ws) {
  const u64 me = __builtin_amdgcn_s_memrealtime();
  __shared__ u64 mn[256], mx[256];
  const u64* p = (const u64*)(ws + DBG_OFF + threadIdx.x * 512);
  mn[threadIdx.x] = p[0]; mx[threadIdx.x] = p[1];
  __syncthreads();
  if (threadIdx.x == 0) {
    u64 a = ~0ull, b = 0;
    for (int i = 0; i < 256; ++i) { a = mn[i] < a ? mn[i] : a; b = mx[i] > b ? mx[i] : b; }
    u64 d = AMP_MODE == 0 ? 0 : AMP_MODE == 1 ? (a - *(const u64*)(ws + DBG_OFF + MB)) * 20 : (me - b) * 20;
    if (d > 200000) d = 200000;
    u64 s = __builtin_amdgcn_s_memrealtime();
    while (__builtin_amdgcn_s_memrealtime() - s < d) __builtin_amdgcn_s_sleep(2);
  }
}

extern "C" void kernel_launch(void* const* d_in, const int* in_sizes, int n_in, void* d_out, int out_size, void* d_ws, size_t ws_size,
                              hipStream_t stream) {
  fused_block<<<256, 256, 0, stream>>>((const u16*)d_in[0], (const u16*)d_in[1], (const u16*)d_in[2], (const u16*)d_in[3], (const u16*)d_in[5],
                                        (const u16*)d_in[6], (const u16*)d_in[7], (u16*)d_out, (u8*)d_ws);
}
```

```cpp
#include <hip/hip_runtime.h>

typedef unsigned short u16;
typedef unsigned char u8;

__global__ __launch_bounds__(256, 1) void fused_block(const u16* x, const u16* wqkv, const u16* wphi, const u16* wo, const u16* wg,
                                                      const u16* wu, const u16* wd, u16* out, u8* ws) {
  __shared__ __attribute__((aligned(16))) u8 smem[163840];
  unsigned tid = threadIdx.x;
  asm volatile(
    "Lbegin_%=:\n"
    "s_load_dwordx16 s[8:23], %0, 0x0\n"
    "s_load_dwordx2 s[24:25], %0, 0x40\n"
    "s_mov_b32 s36, %1\n"
    "s_getpc_b64 s[42:43]\n"
    "v_readfirstlane_b32 s37, v0\n"
    "v_and_b32 v80, 63, v0\n"
    "s_lshr_b32 s87, s36, 3\n"
    "v_and_b32 v95, 15, v80\n"
    "v_lshl_add_u32 v95, s87, 4, v95\n"
    "v_min_u32 v95, (Lend_%= - Lbegin_%=) / 128 - 1, v95\n"
    "v_lshlrev_b32 v15, 7, v95\n"
    "global_load_dword v15, v15, s[42:43]\n"
    "s_lshr_b32 s37, s37, 6\n"
    "s_sub_u32 s45, s36, 208\n"
    "s_cselect_b32 s94, s36, s45\n"
    "s_cselect_b32 s45, 0, 12\n"
    "s_lshr_b32 s46, s94, 2\n"
    "s_add_u32 s35, s46, s45\n"
    "s_and_b32 s45, s94, 3\n"
    "s_lshl_b32 s78, s37, 13\n"
    "v_lshlrev_b32 v0, 4, v80\n"
    "v_mov_b32 v92, 0\n"
    "s_mov_b32 s43, 3\n"
    "s_cmp_ge_u32 s94, 48\n"
    "s_cbranch_scc1 Lnq1_%=\n"
    "s_mov_b32 s42, 8\n"
    "s_movk_i32 s44, 2944\n"
    "s_movk_i32 s85, 24576\n"
    "s_mov_b32 s30, 0x180000\n"
    "s_lshl2_add_u32 s45, s45, s37\n"
    "s_mulk_i32 s45, 768\n"
    "s_add_u32 s45, s45, s35\n"
    "s_lshl_b32 s84, s45, 7\n"
    "s_lshr_b32 s47, s78, 1\n"
    "s_mov_b32 s48, 3\n"
    "s_movk_i32 s49, 6144\n"
    "s_branch Lgo2_%=\n"
    "Lnq1_%=:\n"
    "s_mov_b32 s30, 0x158000\n"
    "s_mov_b32 s48, 7\n"
    "s_movk_i32 s49, 1024\n"
    "s_and_b32 s42, s37, 2\n"
    "s_lshl2_add_u32 s42, s42, 32\n"
    "s_movk_i32 s44, 5376\n"
    "s_mov_b32 s85, 44032\n"
    "s_and_b32 s47, s37, 1\n"
    "s_and_b32 s84, s45, 2\n"
    "s_add_u32 s47, s47, s84\n"
    "s_mulk_i32 s47, 2752\n"
    "s_and_b32 s84, s45, 1\n"
    "s_mulk_i32 s84, 21\n"
    "s_add_u32 s84, s84, s47\n"
    "s_add_u32 s84, s84, s46\n"
    "s_sub_u32 s84, s84, 12\n"
    "s_lshl_b32 s84, s84, 7\n"
    "s_mov_b32 s47, s78\n"
    "s_cmpk_lt_u32 s94, 136\n"
    "s_cbranch_scc1 Lgo2_%=\n"
    "s_mov_b32 s43, 4\n"
    "s_movk_i32 s44, 768\n"
    "s_movk_i32 s85, 4096\n"
    "s_cmpk_ge_u32 s94, 180\n"
    "s_cselect_b32 s84, 45, 34\n"
    "s_cselect_b32 s42, 24, 48\n"
    "s_cselect_b32 s30, 0x80000, s30\n"
    "s_sub_u32 s46, s46, s84\n"
    "s_lshl2_add_u32 s46, s46, s37\n"
    "s_lshl_b32 s46, s46, 7\n"
    "s_add_u32 s46, s46, s45\n"
    "s_lshl_b32 s84, s46, 8\n"
    "Lgo2_%=:\n"
    "s_load_dwordx2 s[28:29], %0, s42\n"
    "s_add_u32 m0, s47, 0x20000\n"
    "v_lshrrev_b32 v86, s43, v80\n"
    "v_mad_u32_u24 v86, v86, s44, v0\n"
    "s_mov_b32 s31, 0x20000\n"
    "s_lshr_b32 s41, s87, 2\n"
    "s_and_b32 s45, s87, 3\n"
    "s_and_b32 s46, s36, 6\n"
    "s_lshl1_add_u32 s38, s46, s45\n"
    "s_lshl2_add_u32 s72, s38, s37\n"
    "s_lshl_b32 s72, s72, 15\n"
    "s_lshl_b32 s74, s37, 15\n"
    "s_lshl_b32 s73, s41, 12\n"
    "s_and_b32 s45, s41, 3\n"
    "s_lshl_b32 s45, s45, 6\n"
    "v_xor_b32 v24, s45, v0\n"
    "s_mov_b32 s42, 0\n"
    "s_cmpk_ge_u32 s94, 196\n"
    "s_cmovk_i32 s49, 11264\n"
    "s_waitcnt lgkmcnt(0)\n"
    "global_load_dwordx2 v[92:93], v92, s[24:25] sc1\n"
    "s_cbranch_scc1 Lnb4_%=\n"
    "s_and_b32 s29, s29, 0xffff\n"
    "Llp3_%=:\n"
    "buffer_load_dwordx4 v86, s[28:31], s84 offen lds\n"
    "s_addk_i32 m0, 1024\n"
    "s_add_u32 s84, s84, s85\n"
    "s_sub_u32 s48, s48, 1\n"
    "s_cbranch_scc0 Llp3_%=\n"
    "Lnb4_%=:\n"
    "s_and_b32 s9, s9, 0xffff\n"
    "s_mov_b32 s10, 0x200000\n"
    "s_mov_b32 s11, s31\n"
    "Llx5_%=:\n"
    "s_xor_b32 s47, s73, s42\n"
    "s_add_u32 m0, s47, s74\n"
    "s_add_u32 s47, s47, s72\n"
    "s_lshr_b32 s46, s42, 6\n"
    "v_xor_b32 v32, s46, v24\n"
    "s_addk_i32 s42, 1024\n"
    "buffer_load_dwordx4 v32, s[8:11], s47 offen lds\n"
    "s_cmp_lt_u32 s42, s49\n"
    "s_cbranch_scc1 Llx5_%=\n"
    "s_mov_b32 s50, 0x400000\n"
    "v_lshlrev_b32 v2, 2, v80\n"
    "v_and_b32 v81, 31, v80\n"
    "v_lshrrev_b32 v82, 5, v80\n"
    "s_and_b32 s46, s36, 1\n"
    "s_lshl1_add_u32 s40, s38, s46\n"
    "s_and_b32 s47, s41, 6\n"
    "s_add_u32 s46, s46, s47\n"
    "s_and_b32 s47, s41, 1\n"
    "s_lshl1_add_u32 s39, s46, s47\n"
    "s_and_b32 s25, s25, 0xffff\n"
    "s_mov_b32 s26, 0x3000000\n"
    "s_mov_b32 s27, 0x20000\n"
    "s_mov_b32 s93, 0\n"
    "s_mul_i32 s83, s37, 0x1a00\n"
    "s_add_u32 s83, s83, 0x20800\n"
    "s_lshr_b32 s42, s37, 1\n"
    "s_lshl1_add_u32 s42, s40, s42\n"
    "s_and_b32 s43, s37, 1\n"
    "s_lshl1_add_u32 s43, s41, s43\n"
    "s_lshl_b32 s45, s42, 15\n"
    "s_lshl_b32 s46, s43, 6\n"
    "s_add_u32 s34, s45, s46\n"
    "s_add_u32 s22, s22, s34\n"
    "s_addc_u32 s23, s23, 0\n"
    "s_lshl2_add_u32 s44, s38, s37\n"
    "s_lshl_b32 s45, s44, 15\n"
    "s_and_b32 s46, s39, 3\n"
    "s_lshl_b32 s46, s46, 13\n"
    "s_add_u32 s45, s45, s46\n"
    "s_lshr_b32 s46, s39, 2\n"
    "s_lshl_b32 s46, s46, 10\n"
    "s_add_u32 s45, s45, s46\n"
    "s_add_u32 s57, s45, 0xa00000\n"
    "s_lshl2_add_u32 s45, s39, s37\n"
    "s_mul_i32 s45, s45, 0x2800\n"
    "s_add_u32 s56, s45, 0x1900000\n"
    "s_lshl_b32 s46, s38, 9\n"
    "s_add_u32 s54, s56, s46\n"
    "s_lshl_b32 s46, s38, 7\n"
    "s_add_u32 s55, s56, s46\n"
    "s_add_u32 s55, s55, 0x2000\n"
    "s_cmp_eq_u32 s37, 0\n"
    "s_cselect_b64 s[16:17], 1, 0\n"
    "s_cselect_b32 s18, 0xffff, 0\n"
    "s_mov_b32 s19, 0\n"
    "s_lshr_b32 s45, s40, 1\n"
    "s_lshl_b32 s45, s45, 11\n"
    "s_add_u32 s90, s45, 0x40000\n"
    "v_and_b32 v95, 15, v80\n"
    "v_cmp_gt_u32 vcc, 2, v95\n"
    "v_mov_b32 v14, 0x40000000\n"
    "v_mul_u32_u24 v95, 63, v95\n"
    "v_add_u32 v95, v95, v80\n"
    "v_cndmask_b32 v14, v14, v95, vcc\n"
    "v_lshlrev_b32 v95, 3, v80\n"
    "s_lshl_b32 s45, s39, 7\n"
    "s_add_u32 s89, s45, 0x40000\n"
    "s_lshl_b32 s45, s38, 11\n"
    "s_add_u32 s91, s45, s89\n"
    "s_add_u32 s88, s45, 0x48000\n"
    "s_lshl3_add_u32 s45, s40, s41\n"
    "s_lshl_b32 s45, s45, 7\n"
    "s_add_u32 s92, s45, 0x48000\n"
    "v_mov_b32 v48, 0\n"
    "v_mov_b32 v49, 0\n"
    "v_lshlrev_b32 v50, 3, v80\n"
    "v_mov_b32 v36, -1\n"
    "v_mov_b32 v37, -1\n"
    "v_mov_b32 v38, -1\n"
    "v_mov_b32 v39, -1\n"
    "s_mul_i32 s51, s44, 0x9000\n"
    "s_add_u32 s51, s51, 0x340000\n"
    "s_mul_i32 s45, s42, 0x9000\n"
    "s_add_u32 s45, s45, 0x340000\n"
    "s_lshl_b32 s46, s43, 11\n"
    "s_add_u32 s52, s45, s46\n"
    "s_lshl_b32 s46, s41, 8\n"
    "s_add_u32 s53, s45, s46\n"
    "s_bitcmp0_b32 s37, 0\n"
    "s_cselect_b32 s46, 0x8000, 0x40000000\n"
    "s_add_u32 s53, s53, s46\n"
    "s_lshl_b32 s45, s42, 15\n"
    "s_lshl_b32 s46, s37, 10\n"
    "s_add_u32 s45, s45, s46\n"
    "s_add_u32 s58, s45, 0xa00000\n"
    "s_xor_b32 s59, s58, 0x8000\n"
    "s_mul_i32 s46, s37, 0x5800\n"
    "s_add_u32 s46, s46, 0xe00000\n"
    "s_mul_i32 s45, s42, 0x16000\n"
    "s_add_u32 s62, s45, s46\n"
    "s_xor_b32 s45, s42, 1\n"
    "s_mul_i32 s45, s45, 0x16000\n"
    "s_add_u32 s63, s45, s46\n"
    "s_cmp_lt_u32 s37, 2\n"
    "s_cselect_b32 s45, 0x5400, 0x40000000\n"
    "s_add_u32 s64, s62, s45\n"
    "s_add_u32 s65, s63, s45\n"
    "s_lshr_b32 s42, s39, 2\n"
    "s_and_b32 s43, s39, 3\n"
    "s_mul_i32 s45, s42, 22\n"
    "s_add_u32 s45, s45, s43\n"
    "s_mul_i32 s46, s44, 88\n"
    "s_add_u32 s46, s46, s45\n"
    "s_lshl_b32 s46, s46, 10\n"
    "s_add_u32 s60, s46, 0xe00000\n"
    "s_cmp_eq_u32 s42, 3\n"
    "s_subb_u32 s45, s45, 0\n"
    "s_mul_i32 s45, s45, 0x8200\n"
    "s_add_u32 s45, s45, 0x1d86000\n"
    "s_add_u32 s46, s37, s93\n"
    "s_lshl_b32 s46, s46, 1\n"
    "s_add_u32 s47, s46, 1\n"
    "s_and_b32 s46, s46, 7\n"
    "s_lshl_b32 s46, s46, 12\n"
    "s_and_b32 s47, s47, 7\n"
    "s_lshl_b32 s47, s47, 12\n"
    "s_add_u32 s68, s45, s46\n"
    "s_add_u32 s69, s45, s47\n"
    "s_mul_i32 s45, s39, 0x8200\n"
    "s_add_u32 s45, s45, 0x1c00000\n"
    "s_add_u32 s66, s45, s46\n"
    "s_add_u32 s67, s45, s47\n"
    "s_lshr_b32 s45, s42, 1\n"
    "s_add_u32 s45, s45, s43\n"
    "s_cmp_lt_u32 s45, 2\n"
    "s_cselect_b32 s45, 0, 0x40000000\n"
    "s_add_u32 s70, s68, s45\n"
    "s_add_u32 s71, s69, s45\n"
    "s_add_u32 s61, s60, s45\n"
    "s_add_u32 s82, s78, 0x10000\n"
    "s_xor_b32 s79, s82, 0x2000\n"
    "s_xor_b32 s80, s82, 0x5000\n"
    "s_xor_b32 s45, s37, 3\n"
    "s_mul_i32 s81, s45, 0x1a00\n"
    "s_add_u32 s81, s81, 0x20800\n"
    "s_and_b32 s45, s37, 1\n"
    "s_lshl_b32 s46, s45, 6\n"
    "s_lshl2_add_u32 s86, s46, 0x20000\n"
    "s_sub_u32 s46, s46, s47\n"
    "s_add_u32 s46, s46, 0x8000\n"
    "v_and_b32 v91, 3, v80\n"
    "v_and_b32 v3, 60, v80\n"
    "v_lshl_add_u32 v3, v91, 7, v3\n"
    "v_add_u32 v3, s46, v3\n"
    "v_and_b32 v84, 1, v80\n"
    "v_lshlrev_b32 v84, 5, v84\n"
    "v_and_b32 v88, 16, v80\n"
    "v_add_u32 v84, v84, v88\n"
    "v_and_b32 v88, 8, v2\n"
    "v_add_u32 v84, v84, v88\n"
    "v_bfe_u32 v85, v80, 2, 2\n"
    "v_mov_b32 v20, 0x3f803f80\n"
    "v_mov_b32 v21, 0x3f803f80\n"
    "v_mov_b32 v22, 0x3f803f80\n"
    "v_mov_b32 v23, 0x3f803f80\n"
    "s_and_b32 s45, s37, 1\n"
    "v_add_u32 v1, 0x10000, v0\n"
    "v_mov_b32 v4, 0x20000\n"
    "v_lshl_add_u32 v4, v81, 4, v4\n"
    "v_mul_u32_u24 v88, 0xd0, v81\n"
    "v_add_u32 v88, s83, v88\n"
    "v_lshl_add_u32 v5, v82, 6, v88\n"
    "v_lshl_add_u32 v6, v82, 5, v88\n"
    "v_lshrrev_b32 v89, 4, v80\n"
    "v_lshl_add_u32 v88, v89, 3, v85\n"
    "v_mul_u32_u24 v88, 0xd0, v88\n"
    "v_lshl_add_u32 v88, v91, 3, v88\n"
    "s_lshl_b32 s46, s37, 5\n"
    "s_add_u32 s46, s46, 0x20800\n"
    "v_add_u32 v7, s46, v88\n"
    "s_lshr_b32 s47, s37, 1\n"
    "s_lshl_b32 s46, s47, 5\n"
    "s_add_u32 s46, s46, 0x20880\n"
    "v_add_u32 v8, s46, v88\n"
    "s_mul_i32 s47, s47, 0x550\n"
    "s_lshl_b32 s45, s45, 5\n"
    "s_add_u32 s47, s47, s45\n"
    "s_add_u32 s47, s47, 0x27000\n"
    "v_and_b32 v88, 7, v80\n"
    "v_bfe_u32 v90, v80, 4, 1\n"
    "v_lshl_add_u32 v88, v88, 1, v90\n"
    "v_mul_u32_u24 v88, 0x50, v88\n"
    "v_and_b32 v90, 8, v80\n"
    "v_lshl_add_u32 v90, v82, 4, v90\n"
    "v_add3_u32 v9, s47, v88, v90\n"
    "v_bfe_u32 v88, v80, 4, 1\n"
    "v_lshl_add_u32 v88, v88, 1, v82\n"
    "v_lshl_add_u32 v88, v91, 2, v88\n"
    "s_add_u32 s47, s47, 0x500\n"
    "v_lshl_add_u32 v10, v88, 1, s47\n"
    "v_bfe_u32 v88, v80, 2, 1\n"
    "v_cmp_eq_u32 vcc, v88, v82\n"
    "v_lshrrev_b32 v90, 3, v81\n"
    "v_lshl_add_u32 v90, v90, 2, v91\n"
    "v_mul_u32_u24 v88, 0x550, v82\n"
    "v_add_u32 v88, 0x27000, v88\n"
    "v_mul_u32_u24 v90, 0x50, v90\n"
    "v_add_u32 v90, v90, v88\n"
    "v_add_u32 v88, 0x500, v88\n"
    "v_mov_b32 v89, 0x27aa0\n"
    "v_cndmask_b32 v11, v89, v90, vcc\n"
    "v_cndmask_b32 v12, v89, v88, vcc\n"
    "v_and_b32 v88, 1, v80\n"
    "v_lshlrev_b32 v88, 7, v88\n"
    "v_and_b32 v89, 2, v80\n"
    "v_lshl_add_u32 v88, v89, 5, v88\n"
    "v_and_b32 v89, 28, v80\n"
    "v_add_u32 v13, v88, v89\n"
    "s_and_b32 s43, s94, 0xfc\n"
    "s_add_u32 s43, s43, s37\n"
    "s_and_b32 s45, s94, 1\n"
    "s_lshl_b32 s45, s45, 1\n"
    "s_sub_u32 s45, s43, s45\n"
    "s_cmp_ge_u32 s45, 48\n"
    "s_cselect_b32 s45, s45, 0xff\n"
    "s_lshl1_add_u32 s43, s35, s37\n"
    "s_cmp_lt_u32 s37, 2\n"
    "s_cselect_b32 s43, s43, 0xff\n"
    "s_cmp_ge_u32 s94, 48\n"
    "s_cselect_b32 s43, s45, s43\n"
    "s_and_b32 s45, s94, 3\n"
    "s_mov_b32 s49, 1024\n"
    "v_lshl_add_u32 v88, v82, 4, v85\n"
    "s_mov_b32 s75, 1024\n"
    "s_mov_b32 s76, 1024\n"
    "s_mov_b32 s77, 5120\n"
    "s_cmp_ge_u32 s94, 136\n"
    "s_cbranch_scc1 Lngu6_%=\n"
    "s_mul_i32 s42, s43, 0x8200\n"
    "s_add_u32 s42, s42, 0x1c00000\n"
    "s_cmp_lt_u32 s43, 134\n"
    "s_cselect_b32 s42, s42, 0x40000000\n"
    "s_lshl_b32 s48, s45, 7\n"
    "s_add_u32 s48, s48, s42\n"
    "s_add_u32 s48, s48, 0x8000\n"
    "s_cmp_ge_u32 s94, 48\n"
    "s_cbranch_scc1 Lgu7_%=\n"
    "s_lshl_b32 s45, s45, 13\n"
    "s_add_u32 s42, s42, s45\n"
    "s_lshl_b32 s45, s37, 6\n"
    "v_lshl_add_u32 v94, v88, 7, v84\n"
    "s_mov_b32 s75, 512\n"
    "s_mov_b32 s76, 512\n"
    "s_mov_b32 s77, 2560\n"
    "s_branch Lgo9_%=\n"
    "Lgu7_%=:\n"
    "s_and_b32 s47, s45, 2\n"
    "s_lshl_b32 s47, s47, 13\n"
    "s_add_u32 s42, s42, s47\n"
    "s_and_b32 s47, s45, 1\n"
    "s_lshl_b32 s47, s47, 10\n"
    "s_add_u32 s42, s42, s47\n"
    "s_mov_b32 s49, 2048\n"
    "s_lshl_b32 s45, s37, 5\n"
    "v_lshl_add_u32 v88, v82, 3, v85\n"
    "v_and_b32 v94, 8, v84\n"
    "v_lshl_add_u32 v94, v88, 7, v94\n"
    "v_and_b32 v88, 1, v80\n"
    "v_lshl_add_u32 v94, v88, 4, v94\n"
    "v_and_b32 v88, 16, v80\n"
    "v_lshl_add_u32 v94, v88, 10, v94\n"
    "s_mov_b32 s75, 512\n"
    "s_mov_b32 s76, 1536\n"
    "s_mov_b32 s77, 1536\n"
    "s_branch Lgo9_%=\n"
    "Lngu6_%=:\n"
    "s_lshl2_add_u32 s45, s45, s37\n"
    "s_lshr_b32 s47, s94, 2\n"
    "s_mov_b32 s48, 0x40000000\n"
    "s_cmp_ge_u32 s94, 180\n"
    "s_cbranch_scc1 Lwo8_%=\n"
    "s_mul_i32 s42, s45, 0x16000\n"
    "s_lshl_b32 s47, s47, 13\n"
    "s_add_u32 s42, s42, s47\n"
    "s_add_u32 s42, s42, 0x21bc000\n"
    "v_lshl_add_u32 v88, v82, 3, v85\n"
    "s_mov_b32 s76, 3072\n"
    "s_mov_b32 s77, 3072\n"
    "s_lshl_b32 s45, s37, 6\n"
    "v_lshl_add_u32 v94, v88, 8, v84\n"
    "s_branch Lgo9_%=\n"
    "Lwo8_%=:\n"
    "s_lshl_b32 s42, s45, 15\n"
    "s_lshl_b32 s47, s47, 10\n"
    "s_add_u32 s42, s42, s47\n"
    "s_add_u32 s42, s42, 0x23f4c00\n"
    "s_cmp_lt_u32 s94, 196\n"
    "s_cselect_b32 s42, s42, 0x40000000\n"
    "s_mov_b32 s49, 4096\n"
    "s_lshl_b32 s45, s37, 6\n"
    "v_lshl_add_u32 v94, v88, 8, v84\n"
    "Lgo9_%=:\n"
    "s_add_u32 s45, s45, 0x20000\n"
    "v_add_u32 v94, s45, v94\n"
    "s_mov_b32 s45, s43\n"
    "s_and_b32 s21, s45, 15\n"
    "s_sub_u32 s46, s45, 48\n"
    "s_mov_b32 s47, 0\n"
    "s_cmp_ge_u32 s46, 22\n"
    "s_cselect_b32 s47, 0x10016, s47\n"
    "s_cmp_ge_u32 s46, 44\n"
    "s_cselect_b32 s47, 0x2002c, s47\n"
    "s_cmp_ge_u32 s46, 65\n"
    "s_cselect_b32 s47, 0x30041, s47\n"
    "s_sub_u32 s46, s46, s47\n"
    "s_and_b32 s46, s46, 3\n"
    "s_lshr_b32 s47, s47, 16\n"
    "s_lshl2_add_u32 s46, s47, s46\n"
    "s_lshl_b32 s46, s46, 7\n"
    "s_add_u32 s46, s46, 0x40048\n"
    "s_lshl_b32 s21, s21, 7\n"
    "s_add_u32 s21, s21, 0x40040\n"
    "s_cmp_ge_u32 s94, 48\n"
    "s_cselect_b32 s21, s46, s21\n"
    "s_cmp_lt_u32 s45, 134\n"
    "s_cselect_b32 s14, 0xffff, 0\n"
    "s_mov_b32 s15, 0\n"
    "v_lshlrev_b32 v18, 7, v0\n"
    "s_cmp_lt_u32 s94, 136\n"
    "s_cbranch_scc1 Lgo10_%=\n"
    "s_and_b32 s45, s94, 3\n"
    "s_lshr_b32 s46, s37, 1\n"
    "s_lshl1_add_u32 s45, s45, s46\n"
    "s_lshl_b32 s45, s45, 7\n"
    "s_add_u32 s21, s45, 0x48040\n"
    "s_cmp_lt_u32 s94, 196\n"
    "s_cselect_b32 s14, -1, 0\n"
    "v_lshlrev_b32 v18, 6, v0\n"
    "Lgo10_%=:\n"
    "v_and_b32 v88, 15, v80\n"
    "v_lshlrev_b32 v89, 1, v82\n"
    "v_xor_b32 v88, v88, v89\n"
    "v_lshlrev_b32 v88, 4, v88\n"
    "v_lshl_add_u32 v29, v81, 10, v88\n"
    "v_add_u32 v29, s74, v29\n"
    "v_mov_b32 v34, 0\n"
    "v_mov_b32 v35, 0\n"
    "s_waitcnt vmcnt(9)\n"
    "v_readfirstlane_b32 s45, v92\n"
    "v_readfirstlane_b32 s46, v93\n"
    "s_nop 0\n"
    "s_mul_i32 s46, s46, 0x4c957f2d\n"
    "s_mul_i32 s47, s45, 0x5851f42d\n"
    "s_add_u32 s46, s46, s47\n"
    "s_mul_hi_u32 s47, s45, 0x4c957f2d\n"
    "s_add_u32 s46, s46, s47\n"
    "s_mul_i32 s45, s45, 0x4c957f2d\n"
    "s_add_u32 s32, s45, 0xf767814f\n"
    "s_addc_u32 s33, s46, 0x14057b7e\n"
    "s_and_b32 s20, s32, 0xffffff00\n"
    "v_writelane_b32 v48, s20, 8\n"
    "v_writelane_b32 v49, s33, 8\n"
    "v_writelane_b32 v48, s20, 9\n"
    "v_writelane_b32 v49, s33, 9\n"
    "s_cmp_eq_u32 s37, 0\n"
    "s_cselect_b32 s45, s91, 0x40000000\n"
    "s_cselect_b32 s46, s92, 0x40000000\n"
    "s_mov_b64 exec, 0xffff\n"
    "buffer_store_dwordx2 v[48:49], v50, s[24:27], s45 offen sc1\n"
    "buffer_store_dwordx2 v[48:49], v50, s[24:27], s46 offen sc1\n"
    "s_mov_b64 exec, -1\n"
    "global_load_dword v16, v2, s[12:13]\n"
    "s_cmp_ge_u32 s94, 48\n"
    "s_cbranch_scc1 Lb14_%=\n"
    "s_waitcnt vmcnt(9)\n"
    "s_barrier\n"
    "s_cmp_ge_u32 s37, 2\n"
    "s_cbranch_scc1 Lnb21_%=\n"
    "ds_read_b64_tr_b16 v[96:97], v94\n"
    "v_add_u32 v94, s75, v94\n"
    "ds_read_b64_tr_b16 v[98:99], v94\n"
    "v_add_u32 v94, s76, v94\n"
    "ds_read_b64_tr_b16 v[100:101], v94\n"
    "v_add_u32 v94, s75, v94\n"
    "ds_read_b64_tr_b16 v[102:103], v94\n"
    "v_add_u32 v94, s77, v94\n"
    "ds_read_b64_tr_b16 v[104:105], v94\n"
    "v_add_u32 v94, s75, v94\n"
    "ds_read_b64_tr_b16 v[106:107], v94\n"
    "v_add_u32 v94, s76, v94\n"
    "ds_read_b64_tr_b16 v[108:109], v94\n"
    "v_add_u32 v94, s75, v94\n"
    "ds_read_b64_tr_b16 v[110:111], v94\n"
    "v_add_u32 v94, s77, v94\n"
    "ds_read_b64_tr_b16 v[112:113], v94\n"
    "v_add_u32 v94, s75, v94\n"
    "ds_read_b64_tr_b16 v[114:115], v94\n"
    "v_add_u32 v94, s76, v94\n"
    "ds_read_b64_tr_b16 v[116:117], v94\n"
    "v_add_u32 v94, s75, v94\n"
    "ds_read_b64_tr_b16 v[118:119], v94\n"
    "v_add_u32 v94, s77, v94\n"
    "ds_read_b64_tr_b16 v[120:121], v94\n"
    "v_add_u32 v94, s75, v94\n"
    "ds_read_b64_tr_b16 v[122:123], v94\n"
    "v_add_u32 v94, s76, v94\n"
    "ds_read_b64_tr_b16 v[124:125], v94\n"
    "v_add_u32 v94, s75, v94\n"
    "ds_read_b64_tr_b16 v[126:127], v94\n"
    "s_waitcnt lgkmcnt(14)\n"
    "v_mfma_f32_32x32x16_bf16 v[64:79], v[20:23], v[96:99], 0\n"
    "buffer_store_dwordx4 v[96:99], v0, s[24:27], s42 offen sc1\n"
    "s_waitcnt lgkmcnt(12)\n"
    "v_mfma_f32_32x32x16_bf16 v[64:79], v[20:23], v[100:103], v[64:79]\n"
    "s_add_u32 s42, s42, s49\n"
    "buffer_store_dwordx4 v[100:103], v0, s[24:27], s42 offen sc1\n"
    "s_waitcnt lgkmcnt(10)\n"
    "v_mfma_f32_32x32x16_bf16 v[64:79], v[20:23], v[104:107], v[64:79]\n"
    "s_add_u32 s42, s42, s49\n"
    "buffer_store_dwordx4 v[104:107], v0, s[24:27], s42 offen sc1\n"
    "s_waitcnt lgkmcnt(8)\n"
    "v_mfma_f32_32x32x16_bf16 v[64:79], v[20:23], v[108:111], v[64:79]\n"
    "s_add_u32 s42, s42, s49\n"
    "buffer_store_dwordx4 v[108:111], v0, s[24:27], s42 offen sc1\n"
    "s_waitcnt lgkmcnt(6)\n"
    "v_mfma_f32_32x32x16_bf16 v[64:79], v[20:23], v[112:115], v[64:79]\n"
    "s_add_u32 s42, s42, s49\n"
    "buffer_store_dwordx4 v[112:115], v0, s[24:27], s42 offen sc1\n"
    "s_waitcnt lgkmcnt(4)\n"
    "v_mfma_f32_32x32x16_bf16 v[64:79], v[20:23], v[116:119], v[64:79]\n"
    "s_add_u32 s42, s42, s49\n"
    "buffer_store_dwordx4 v[116:119], v0, s[24:27], s42 offen sc1\n"
    "s_waitcnt lgkmcnt(2)\n"
    "v_mfma_f32_32x32x16_bf16 v[64:79], v[20:23], v[120:123], v[64:79]\n"
    "s_add_u32 s42, s42, s49\n"
    "buffer_store_dwordx4 v[120:123], v0, s[24:27], s42 offen sc1\n"
    "s_waitcnt lgkmcnt(0)\n"
    "v_mfma_f32_32x32x16_bf16 v[64:79], v[20:23], v[124:127], v[64:79]\n"
    "s_add_u32 s42, s42, s49\n"
    "buffer_store_dwordx4 v[124:127], v0, s[24:27], s42 offen sc1\n"
    "v_lshlrev_b32 v93, 2, v81\n"
    "s_nop 8\n"
    "v_cvt_pk_bf16_f32 v92, v64, v64\n"
    "v_lshlrev_b32 v92, 16, v92\n"
    "v_sub_f32 v92, v64, v92\n"
    "v_cvt_pk_bf16_f32 v92, v64, v92\n"
    "buffer_store_dword v92, v93, s[24:27], s48 offen sc1\n"
    "Lnb21_%=:\n"
    "s_xor_b32 s47, s73, 6144\n"
    "s_add_u32 m0, s47, s74\n"
    "s_add_u32 s47, s47, s72\n"
    "v_xor_b32 v32, 96, v24\n"
    "buffer_load_dwordx4 v32, s[8:11], s47 offen lds\n"
    "s_xor_b32 s47, s73, 7168\n"
    "s_add_u32 m0, s47, s74\n"
    "s_add_u32 s47, s47, s72\n"
    "v_xor_b32 v33, 112, v24\n"
    "buffer_load_dwordx4 v33, s[8:11], s47 offen lds\n"
    "s_xor_b32 s47, s73, 8192\n"
    "s_add_u32 m0, s47, s74\n"
    "s_add_u32 s47, s47, s72\n"
    "v_xor_b32 v32, 128, v24\n"
    "buffer_load_dwordx4 v32, s[8:11], s47 offen lds\n"
    "s_xor_b32 s47, s73, 9216\n"
    "s_add_u32 m0, s47, s74\n"
    "s_add_u32 s47, s47, s72\n"
    "v_xor_b32 v33, 144, v24\n"
    "buffer_load_dwordx4 v33, s[8:11], s47 offen lds\n"
    "s_xor_b32 s47, s73, 10240\n"
    "s_add_u32 m0, s47, s74\n"
    "s_add_u32 s47, s47, s72\n"
    "v_xor_b32 v32, 160, v24\n"
    "buffer_load_dwordx4 v32, s[8:11], s47 offen lds\n"
    "s_waitcnt vmcnt(5)\n"
    "v_mov_b32 v44, 1\n"
    "v_mov_b32 v45, 0\n"
    "s_mov_b64 exec, s[14:15]\n"
    "buffer_atomic_add_x2 v[44:45], v18, s[24:27], s21 offen sc0 sc1\n"
    "s_mov_b64 exec, -1\n"
    "v_mov_b32 v48, s32\n"
    "v_mov_b32 v49, s33\n"
    "s_cmp_eq_u32 s37, 0\n"
    "s_cselect_b32 s45, s91, 0x40000000\n"
    "s_cselect_b32 s46, s92, 0x40000000\n"
    "s_mov_b64 exec, 0xffff\n"
    "s_add_u32 s47, s45, 0x10000\n"
    "buffer_store_dwordx2 v[48:49], v50, s[24:27], s47 offen sc1\n"
    "s_add_u32 s47, s46, 0x10000\n"
    "buffer_store_dwordx2 v[48:49], v50, s[24:27], s47 offen sc1\n"
    "s_mov_b64 exec, -1\n"
    "s_xor_b32 s47, s73, 11264\n"
    "s_add_u32 m0, s47, s74\n"
    "s_add_u32 s47, s47, s72\n"
    "v_xor_b32 v33, 176, v24\n"
    "buffer_load_dwordx4 v33, s[8:11], s47 offen lds\n"
    "s_xor_b32 s47, s73, 12288\n"
    "s_add_u32 m0, s47, s74\n"
    "s_add_u32 s47, s47, s72\n"
    "v_xor_b32 v32, 192, v24\n"
    "buffer_load_dwordx4 v32, s[8:11], s47 offen lds\n"
    "s_xor_b32 s47, s73, 13312\n"
    "s_add_u32 m0, s47, s74\n"
    "s_add_u32 s47, s47, s72\n"
    "v_xor_b32 v33, 208, v24\n"
    "buffer_load_dwordx4 v33, s[8:11], s47 offen lds\n"
    "s_xor_b32 s47, s73, 14336\n"
    "s_add_u32 m0, s47, s74\n"
    "s_add_u32 s47, s47, s72\n"
    "v_xor_b32 v32, 224, v24\n"
    "buffer_load_dwordx4 v32, s[8:11], s47 offen lds\n"
    "s_xor_b32 s47, s73, 15360\n"
    "s_add_u32 m0, s47, s74\n"
    "s_add_u32 s47, s47, s72\n"
    "v_xor_b32 v33, 240, v24\n"
    "buffer_load_dwordx4 v33, s[8:11], s47 offen lds\n"
    "s_xor_b32 s47, s73, 16384\n"
    "s_add_u32 m0, s47, s74\n"
    "s_add_u32 s47, s47, s72\n"
    "v_xor_b32 v32, 0, v24\n"
    "buffer_load_dwordx4 v32, s[8:11], s47 offen lds\n"
    "s_xor_b32 s47, s73, 17408\n"
    "s_add_u32 m0, s47, s74\n"
    "s_add_u32 s47, s47, s72\n"
    "v_xor_b32 v33, 16, v24\n"
    "buffer_load_dwordx4 v33, s[8:11], s47 offen lds\n"
    "s_xor_b32 s47, s73, 18432\n"
    "s_add_u32 m0, s47, s74\n"
    "s_add_u32 s47, s47, s72\n"
    "v_xor_b32 v32, 32, v24\n"
    "buffer_load_dwordx4 v32, s[8:11], s47 offen lds\n"
    "s_xor_b32 s47, s73, 19456\n"
    "s_add_u32 m0, s47, s74\n"
    "s_add_u32 s47, s47, s72\n"
    "v_xor_b32 v33, 48, v24\n"
    "buffer_load_dwordx4 v33, s[8:11], s47 offen lds\n"
    "s_xor_b32 s47, s73, 20480\n"
    "s_add_u32 m0, s47, s74\n"
    "s_add_u32 s47, s47, s72\n"
    "v_xor_b32 v32, 64, v24\n"
    "buffer_load_dwordx4 v32, s[8:11], s47 offen lds\n"
    "s_xor_b32 s47, s73, 21504\n"
    "s_add_u32 m0, s47, s74\n"
    "s_add_u32 s47, s47, s72\n"
    "v_xor_b32 v33, 80, v24\n"
    "buffer_load_dwordx4 v33, s[8:11], s47 offen lds\n"
    "s_xor_b32 s47, s73, 22528\n"
    "s_add_u32 m0, s47, s74\n"
    "s_add_u32 s47, s47, s72\n"
    "v_xor_b32 v32, 96, v24\n"
    "buffer_load_dwordx4 v32, s[8:11], s47 offen lds\n"
    "s_xor_b32 s47, s73, 23552\n"
    "s_add_u32 m0, s47, s74\n"
    "s_add_u32 s47, s47, s72\n"
    "v_xor_b32 v33, 112, v24\n"
    "buffer_load_dwordx4 v33, s[8:11], s47 offen lds\n"
    "s_xor_b32 s47, s73, 24576\n"
    "s_add_u32 m0, s47, s74\n"
    "s_add_u32 s47, s47, s72\n"
    "v_xor_b32 v32, 128, v24\n"
    "buffer_load_dwordx4 v32, s[8:11], s47 offen lds\n"
    "s_xor_b32 s47, s73, 25600\n"
    "s_add_u32 m0, s47, s74\n"
    "s_add_u32 s47, s47, s72\n"
    "v_xor_b32 v33, 144, v24\n"
    "buffer_load_dwordx4 v33, s[8:11], s47 offen lds\n"
    "s_xor_b32 s47, s73, 26624\n"
    "s_add_u32 m0, s47, s74\n"
    "s_add_u32 s47, s47, s72\n"
    "v_xor_b32 v32, 160, v24\n"
    "buffer_load_dwordx4 v32, s[8:11], s47 offen lds\n"
    "s_xor_b32 s47, s73, 27648\n"
    "s_add_u32 m0, s47, s74\n"
    "s_add_u32 s47, s47, s72\n"
    "v_xor_b32 v33, 176, v24\n"
    "buffer_load_dwordx4 v33, s[8:11], s47 offen lds\n"
    "s_xor_b32 s47, s73, 28672\n"
    "s_add_u32 m0, s47, s74\n"
    "s_add_u32 s47, s47, s72\n"
    "v_xor_b32 v32, 192, v24\n"
    "buffer_load_dwordx4 v32, s[8:11], s47 offen lds\n"
    "s_xor_b32 s47, s73, 29696\n"
    "s_add_u32 m0, s47, s74\n"
    "s_add_u32 s47, s47, s72\n"
    "v_xor_b32 v33, 208, v24\n"
    "buffer_load_dwordx4 v33, s[8:11], s47 offen lds\n"
    "s_xor_b32 s47, s73, 30720\n"
    "s_add_u32 m0, s47, s74\n"
    "s_add_u32 s47, s47, s72\n"
    "v_xor_b32 v32, 224, v24\n"
    "buffer_load_dwordx4 v32, s[8:11], s47 offen lds\n"
    "s_xor_b32 s47, s73, 31744\n"
    "s_add_u32 m0, s47, s74\n"
    "s_add_u32 s47, s47, s72\n"
    "v_xor_b32 v33, 240, v24\n"
    "buffer_load_dwordx4 v33, s[8:11], s47 offen lds\n"
    "v_mov_b32 v46, 0\n"
    "v_mov_b32 v47, 0\n"
    "s_mov_b64 exec, s[16:17]\n"
    "buffer_atomic_add_x2 v[46:47], off, s[24:27], s91 offset:64 sc0 sc1\n"
    "s_mov_b64 exec, -1\n"
    "s_waitcnt vmcnt(23)\n"
    "v_and_b32 v44, 0xffffff00, v44\n"
    "v_xor_b32 v44, s20, v44\n"
    "v_xor_b32 v45, s33, v45\n"
    "v_or_b32 v44, v44, v45\n"
    "v_cmp_ne_u32 vcc, 0, v44\n"
    "s_nop 0\n"
    "s_and_b64 s[14:15], s[14:15], vcc\n"
    "s_cbranch_scc1 Lrep19_%=\n"
    "Lvok20_%=:\n"
    "s_mov_b32 s42, 0x40000000\n"
    "s_mov_b32 s48, 0x40000000\n"
    "s_waitcnt vmcnt(0)\n"
    "ds_read_b128 v[128:131], v29\n"
    "ds_read_b128 v[160:163], v29 offset:256\n"
    "ds_read_b128 v[192:195], v29 offset:512\n"
    "ds_read_b128 v[224:227], v29 offset:768\n"
    "v_xor_b32 v33, 16, v29\n"
    "ds_read_b128 v[132:135], v33\n"
    "ds_read_b128 v[164:167], v33 offset:256\n"
    "ds_read_b128 v[196:199], v33 offset:512\n"
    "s_waitcnt lgkmcnt(6)\n"
    "v_dot2c_f32_bf16 v34, v128, v20\n"
    "ds_read_b128 v[228:231], v33 offset:768\n"
    "v_dot2c_f32_bf16 v35, v128, v128\n"
    "v_xor_b32 v32, 64, v29\n"
    "ds_read_b128 v[136:139], v32\n"
    "v_dot2c_f32_bf16 v34, v129, v20\n"
    "ds_read_b128 v[168:171], v32 offset:256\n"
    "v_dot2c_f32_bf16 v35, v129, v129\n"
    "ds_read_b128 v[200:203], v32 offset:512\n"
    "v_dot2c_f32_bf16 v34, v130, v20\n"
    "ds_read_b128 v[232:235], v32 offset:768\n"
    "v_dot2c_f32_bf16 v35, v130, v130\n"
    "v_xor_b32 v33, 80, v29\n"
    "ds_read_b128 v[140:143], v33\n"
    "v_dot2c_f32_bf16 v34, v131, v20\n"
    "ds_read_b128 v[172:175], v33 offset:256\n"
    "v_dot2c_f32_bf16 v35, v131, v131\n"
    "ds_read_b128 v[204:207], v33 offset:512\n"
    "s_waitcnt lgkmcnt(13)\n"
    "v_dot2c_f32_bf16 v34, v160, v20\n"
    "ds_read_b128 v[236:239], v33 offset:768\n"
    "v_dot2c_f32_bf16 v35, v160, v160\n"
    "v_xor_b32 v32, 128, v29\n"
    "ds_read_b128 v[144:147], v32\n"
    "v_dot2c_f32_bf16 v34, v161, v20\n"
    "ds_read_b128 v[176:179], v32 offset:256\n"
    "v_dot2c_f32_bf16 v35, v161, v161\n"
    "ds_read_b128 v[208:211], v32 offset:512\n"
    "v_dot2c_f32_bf16 v34, v162, v20\n"
    "ds_read_b128 v[240:243], v32 offset:768\n"
    "v_dot2c_f32_bf16 v35, v162, v162\n"
    "v_xor_b32 v33, 144, v29\n"
    "ds_read_b128 v[148:151], v33\n"
    "v_dot2c_f32_bf16 v34, v163, v20\n"
    "ds_read_b128 v[180:183], v33 offset:256\n"
    "v_dot2c_f32_bf16 v35, v163, v163\n"
    "ds_read_b128 v[212:215], v33 offset:512\n"
    "s_waitcnt lgkmcnt(15)\n"
    "v_dot2c_f32_bf16 v34, v192, v20\n"
    "ds_read_b128 v[244:247], v33 offset:768\n"
    "v_dot2c_f32_bf16 v35, v192, v192\n"
    "v_xor_b32 v32, 192, v29\n"
    "ds_read_b128 v[152:155], v32\n"
    "v_dot2c_f32_bf16 v34, v193, v20\n"
    "ds_read_b128 v[184:187], v32 offset:256\n"
    "v_dot2c_f32_bf16 v35, v193, v193\n"
    "ds_read_b128 v[216:219], v32 offset:512\n"
    "v_dot2c_f32_bf16 v34, v194, v20\n"
    "ds_read_b128 v[248:251], v32 offset:768\n"
    "v_dot2c_f32_bf16 v35, v194, v194\n"
    "v_xor_b32 v33, 208, v29\n"
    "ds_read_b128 v[156:159], v33\n"
    "v_dot2c_f32_bf16 v34, v195, v20\n"
    "ds_read_b128 v[188:191], v33 offset:256\n"
    "v_dot2c_f32_bf16 v35, v195, v195\n"
    "ds_read_b128 v[220:223], v33 offset:512\n"
    "s_waitcnt lgkmcnt(15)\n"
    "v_dot2c_f32_bf16 v34, v224, v20\n"
    "ds_read_b128 v[252:255], v33 offset:768\n"
    "v_dot2c_f32_bf16 v35, v224, v224\n"
    "v_dot2c_f32_bf16 v34, v225, v20\n"
    "v_dot2c_f32_bf16 v35, v225, v225\n"
    "v_dot2c_f32_bf16 v34, v226, v20\n"
    "v_dot2c_f32_bf16 v35, v226, v226\n"
    "v_dot2c_f32_bf16 v34, v227, v20\n"
    "v_dot2c_f32_bf16 v35, v227, v227\n"
    "s_add_u32 s44, s20, 12\n"
    "s_waitcnt vmcnt(0)\n"
    "v_cmp_ne_u32 vcc, s44, v46\n"
    "s_nop 0\n"
    "s_and_b64 vcc, vcc, s[16:17]\n"
    "s_cbranch_scc1 Lrd17_%=\n"
    "Lrk18_%=:\n"
    "s_waitcnt lgkmcnt(0)\n"
    "s_barrier\n"
    "s_add_u32 m0, s78, 0x0\n"
    "s_add_u32 s47, s66, 0x82000\n"
    "buffer_load_dwordx4 v0, s[24:27], s47 offen sc1 lds\n"
    "buffer_load_dwordx4 v0, s[24:27], s47 offen offset:1024 sc1 lds\n"
    "v_dot2c_f32_bf16 v34, v132, v20\n"
    "v_dot2c_f32_bf16 v35, v132, v132\n"
    "v_dot2c_f32_bf16 v34, v133, v20\n"
    "v_dot2c_f32_bf16 v35, v133, v133\n"
    "v_dot2c_f32_bf16 v34, v134, v20\n"
    "v_dot2c_f32_bf16 v35, v134, v134\n"
    "v_dot2c_f32_bf16 v34, v135, v20\n"
    "buffer_load_dwordx4 v0, s[24:27], s47 offen offset:2048 sc1 lds\n"
    "v_dot2c_f32_bf16 v35, v135, v135\n"
    "v_dot2c_f32_bf16 v34, v164, v20\n"
    "v_dot2c_f32_bf16 v35, v164, v164\n"
    "v_dot2c_f32_bf16 v34, v165, v20\n"
    "v_dot2c_f32_bf16 v35, v165, v165\n"
    "v_dot2c_f32_bf16 v34, v166, v20\n"
    "v_dot2c_f32_bf16 v35, v166, v166\n"
    "v_dot2c_f32_bf16 v34, v167, v20\n"
    "v_dot2c_f32_bf16 v35, v167, v167\n"
    "v_dot2c_f32_bf16 v34, v196, v20\n"
    "v_dot2c_f32_bf16 v35, v196, v196\n"
    "v_dot2c_f32_bf16 v34, v197, v20\n"
    "v_dot2c_f32_bf16 v35, v197, v197\n"
    "v_dot2c_f32_bf16 v34, v198, v20\n"
    "buffer_load_dwordx4 v0, s[24:27], s47 offen offset:3072 sc1 lds\n"
    "v_dot2c_f32_bf16 v35, v198, v198\n"
    "v_dot2c_f32_bf16 v34, v199, v20\n"
    "v_dot2c_f32_bf16 v35, v199, v199\n"
    "v_dot2c_f32_bf16 v34, v228, v20\n"
    "v_dot2c_f32_bf16 v35, v228, v228\n"
    "v_dot2c_f32_bf16 v34, v229, v20\n"
    "v_dot2c_f32_bf16 v35, v229, v229\n"
    "v_dot2c_f32_bf16 v34, v230, v20\n"
    "v_dot2c_f32_bf16 v35, v230, v230\n"
    "v_dot2c_f32_bf16 v34, v231, v20\n"
    "v_dot2c_f32_bf16 v35, v231, v231\n"
    "v_dot2c_f32_bf16 v34, v136, v20\n"
    "v_dot2c_f32_bf16 v35, v136, v136\n"
    "s_add_u32 m0, s78, 0x1000\n"
    "s_add_u32 s47, s67, 0x82000\n"
    "buffer_load_dwordx4 v0, s[24:27], s47 offen sc1 lds\n"
    "v_dot2c_f32_bf16 v34, v137, v20\n"
    "v_dot2c_f32_bf16 v35, v137, v137\n"
    "v_dot2c_f32_bf16 v34, v138, v20\n"
    "v_dot2c_f32_bf16 v35, v138, v138\n"
    "v_dot2c_f32_bf16 v34, v139, v20\n"
    "v_dot2c_f32_bf16 v35, v139, v139\n"
    "v_dot2c_f32_bf16 v34, v168, v20\n"
    "v_dot2c_f32_bf16 v35, v168, v168\n"
    "v_dot2c_f32_bf16 v34, v169, v20\n"
    "v_dot2c_f32_bf16 v35, v169, v169\n"
    "v_dot2c_f32_bf16 v34, v170, v20\n"
    "v_dot2c_f32_bf16 v35, v170, v170\n"
    "v_dot2c_f32_bf16 v34, v171, v20\n"
    "buffer_load_dwordx4 v0, s[24:27], s47 offen offset:1024 sc1 lds\n"
    "v_dot2c_f32_bf16 v35, v171, v171\n"
    "v_dot2c_f32_bf16 v34, v200, v20\n"
    "v_dot2c_f32_bf16 v35, v200, v200\n"
    "v_dot2c_f32_bf16 v34, v201, v20\n"
    "v_dot2c_f32_bf16 v35, v201, v201\n"
    "v_dot2c_f32_bf16 v34, v202, v20\n"
    "v_dot2c_f32_bf16 v35, v202, v202\n"
    "v_dot2c_f32_bf16 v34, v203, v20\n"
    "v_dot2c_f32_bf16 v35, v203, v203\n"
    "v_dot2c_f32_bf16 v34, v232, v20\n"
    "v_dot2c_f32_bf16 v35, v232, v232\n"
    "v_dot2c_f32_bf16 v34, v233, v20\n"
    "v_dot2c_f32_bf16 v35, v233, v233\n"
    "buffer_load_dwordx4 v0, s[24:27], s47 offen offset:2048 sc1 lds\n"
    "v_dot2c_f32_bf16 v34, v234, v20\n"
    "v_dot2c_f32_bf16 v35, v234, v234\n"
    "v_dot2c_f32_bf16 v34, v235, v20\n"
    "v_dot2c_f32_bf16 v35, v235, v235\n"
    "v_dot2c_f32_bf16 v34, v140, v20\n"
    "v_dot2c_f32_bf16 v35, v140, v140\n"
    "v_dot2c_f32_bf16 v34, v141, v20\n"
    "v_dot2c_f32_bf16 v35, v141, v141\n"
    "v_dot2c_f32_bf16 v34, v142, v20\n"
    "v_dot2c_f32_bf16 v35, v142, v142\n"
    "v_dot2c_f32_bf16 v34, v143, v20\n"
    "v_dot2c_f32_bf16 v35, v143, v143\n"
    "v_dot2c_f32_bf16 v34, v172, v20\n"
    "v_dot2c_f32_bf16 v35, v172, v172\n"
    "buffer_load_dwordx4 v0, s[24:27], s47 offen offset:3072 sc1 lds\n"
    "v_dot2c_f32_bf16 v34, v173, v20\n"
    "v_dot2c_f32_bf16 v35, v173, v173\n"
    "v_dot2c_f32_bf16 v34, v174, v20\n"
    "v_dot2c_f32_bf16 v35, v174, v174\n"
    "v_dot2c_f32_bf16 v34, v175, v20\n"
    "v_dot2c_f32_bf16 v35, v175, v175\n"
    "v_dot2c_f32_bf16 v34, v204, v20\n"
    "v_dot2c_f32_bf16 v35, v204, v204\n"
    "v_dot2c_f32_bf16 v34, v205, v20\n"
    "v_dot2c_f32_bf16 v35, v205, v205\n"
    "v_dot2c_f32_bf16 v34, v206, v20\n"
    "v_dot2c_f32_bf16 v35, v206, v206\n"
    "v_dot2c_f32_bf16 v34, v207, v20\n"
    "s_add_u32 m0, s86, 0x0\n"
    "s_nop 0\n"
    "buffer_load_dword v3, s[24:27], s47 offen sc1 lds\n"
    "v_dot2c_f32_bf16 v35, v207, v207\n"
    "v_dot2c_f32_bf16 v34, v236, v20\n"
    "v_dot2c_f32_bf16 v35, v236, v236\n"
    "v_dot2c_f32_bf16 v34, v237, v20\n"
    "v_dot2c_f32_bf16 v35, v237, v237\n"
    "v_dot2c_f32_bf16 v34, v238, v20\n"
    "v_dot2c_f32_bf16 v35, v238, v238\n"
    "v_dot2c_f32_bf16 v34, v239, v20\n"
    "v_dot2c_f32_bf16 v35, v239, v239\n"
    "v_dot2c_f32_bf16 v34, v144, v20\n"
    "v_dot2c_f32_bf16 v35, v144, v144\n"
    "v_dot2c_f32_bf16 v34, v145, v20\n"
    "v_dot2c_f32_bf16 v35, v145, v145\n"
    "s_add_u32 m0, s78, 0x10000\n"
    "s_add_u32 s47, s66, 0x104000\n"
    "buffer_load_dwordx4 v0, s[24:27], s47 offen sc1 lds\n"
    "v_dot2c_f32_bf16 v34, v146, v20\n"
    "v_dot2c_f32_bf16 v35, v146, v146\n"
    "v_dot2c_f32_bf16 v34, v147, v20\n"
    "v_dot2c_f32_bf16 v35, v147, v147\n"
    "v_dot2c_f32_bf16 v34, v176, v20\n"
    "v_dot2c_f32_bf16 v35, v176, v176\n"
    "v_dot2c_f32_bf16 v34, v177, v20\n"
    "v_dot2c_f32_bf16 v35, v177, v177\n"
    "v_dot2c_f32_bf16 v34, v178, v20\n"
    "v_dot2c_f32_bf16 v35, v178, v178\n"
    "v_dot2c_f32_bf16 v34, v179, v20\n"
    "v_dot2c_f32_bf16 v35, v179, v179\n"
    "v_dot2c_f32_bf16 v34, v208, v20\n"
    "v_dot2c_f32_bf16 v35, v208, v208\n"
    "buffer_load_dwordx4 v0, s[24:27], s47 offen offset:1024 sc1 lds\n"
    "v_dot2c_f32_bf16 v34, v209, v20\n"
    "v_dot2c_f32_bf16 v35, v209, v209\n"
    "v_dot2c_f32_bf16 v34, v210, v20\n"
    "v_dot2c_f32_bf16 v35, v210, v210\n"
    "v_dot2c_f32_bf16 v34, v211, v20\n"
    "v_dot2c_f32_bf16 v35, v211, v211\n"
    "v_dot2c_f32_bf16 v34, v240, v20\n"
    "v_dot2c_f32_bf16 v35, v240, v240\n"
    "v_dot2c_f32_bf16 v34, v241, v20\n"
    "v_dot2c_f32_bf16 v35, v241, v241\n"
    "v_dot2c_f32_bf16 v34, v242, v20\n"
    "v_dot2c_f32_bf16 v35, v242, v242\n"
    "v_dot2c_f32_bf16 v34, v243, v20\n"
    "buffer_load_dwordx4 v0, s[24:27], s47 offen offset:2048 sc1 lds\n"
    "v_dot2c_f32_bf16 v35, v243, v243\n"
    "v_dot2c_f32_bf16 v34, v148, v20\n"
    "v_dot2c_f32_bf16 v35, v148, v148\n"
    "v_dot2c_f32_bf16 v34, v149, v20\n"
    "v_dot2c_f32_bf16 v35, v149, v149\n"
    "v_dot2c_f32_bf16 v34, v150, v20\n"
    "v_dot2c_f32_bf16 v35, v150, v150\n"
    "v_dot2c_f32_bf16 v34, v151, v20\n"
    "v_dot2c_f32_bf16 v35, v151, v151\n"
    "v_dot2c_f32_bf16 v34, v180, v20\n"
    "v_dot2c_f32_bf16 v35, v180, v180\n"
    "v_dot2c_f32_bf16 v34, v181, v20\n"
    "v_dot2c_f32_bf16 v35, v181, v181\n"
    "buffer_load_dwordx4 v0, s[24:27], s47 offen offset:3072 sc1 lds\n"
    "v_dot2c_f32_bf16 v34, v182, v20\n"
    "v_dot2c_f32_bf16 v35, v182, v182\n"
    "v_dot2c_f32_bf16 v34, v183, v20\n"
    "v_dot2c_f32_bf16 v35, v183, v183\n"
    "v_dot2c_f32_bf16 v34, v212, v20\n"
    "v_dot2c_f32_bf16 v35, v212, v212\n"
    "v_dot2c_f32_bf16 v34, v213, v20\n"
    "v_dot2c_f32_bf16 v35, v213, v213\n"
    "v_dot2c_f32_bf16 v34, v214, v20\n"
    "v_dot2c_f32_bf16 v35, v214, v214\n"
    "v_dot2c_f32_bf16 v34, v215, v20\n"
    "v_dot2c_f32_bf16 v35, v215, v215\n"
    "v_dot2c_f32_bf16 v34, v244, v20\n"
    "v_dot2c_f32_bf16 v35, v244, v244\n"
    "s_add_u32 m0, s78, 0x11000\n"
    "s_add_u32 s47, s67, 0x104000\n"
    "buffer_load_dwordx4 v0, s[24:27], s47 offen sc1 lds\n"
    "v_dot2c_f32_bf16 v34, v245, v20\n"
    "v_dot2c_f32_bf16 v35, v245, v245\n"
    "v_dot2c_f32_bf16 v34, v246, v20\n"
    "v_dot2c_f32_bf16 v35, v246, v246\n"
    "v_dot2c_f32_bf16 v34, v247, v20\n"
    "v_dot2c_f32_bf16 v35, v247, v247\n"
    "v_dot2c_f32_bf16 v34, v152, v20\n"
    "v_dot2c_f32_bf16 v35, v152, v152\n"
    "v_dot2c_f32_bf16 v34, v153, v20\n"
    "v_dot2c_f32_bf16 v35, v153, v153\n"
    "v_dot2c_f32_bf16 v34, v154, v20\n"
    "v_dot2c_f32_bf16 v35, v154, v154\n"
    "v_dot2c_f32_bf16 v34, v155, v20\n"
    "buffer_load_dwordx4 v0, s[24:27], s47 offen offset:1024 sc1 lds\n"
    "v_dot2c_f32_bf16 v35, v155, v155\n"
    "v_dot2c_f32_bf16 v34, v184, v20\n"
    "v_dot2c_f32_bf16 v35, v184, v184\n"
    "v_dot2c_f32_bf16 v34, v185, v20\n"
    "v_dot2c_f32_bf16 v35, v185, v185\n"
    "v_dot2c_f32_bf16 v34, v186, v20\n"
    "v_dot2c_f32_bf16 v35, v186, v186\n"
    "v_dot2c_f32_bf16 v34, v187, v20\n"
    "v_dot2c_f32_bf16 v35, v187, v187\n"
    "v_dot2c_f32_bf16 v34, v216, v20\n"
    "v_dot2c_f32_bf16 v35, v216, v216\n"
    "v_dot2c_f32_bf16 v34, v217, v20\n"
    "v_dot2c_f32_bf16 v35, v217, v217\n"
    "buffer_load_dwordx4 v0, s[24:27], s47 offen offset:2048 sc1 lds\n"
    "v_dot2c_f32_bf16 v34, v218, v20\n"
    "v_dot2c_f32_bf16 v35, v218, v218\n"
    "v_dot2c_f32_bf16 v34, v219, v20\n"
    "v_dot2c_f32_bf16 v35, v219, v219\n"
    "v_dot2c_f32_bf16 v34, v248, v20\n"
    "v_dot2c_f32_bf16 v35, v248, v248\n"
    "v_dot2c_f32_bf16 v34, v249, v20\n"
    "v_dot2c_f32_bf16 v35, v249, v249\n"
    "v_dot2c_f32_bf16 v34, v250, v20\n"
    "v_dot2c_f32_bf16 v35, v250, v250\n"
    "v_dot2c_f32_bf16 v34, v251, v20\n"
    "v_dot2c_f32_bf16 v35, v251, v251\n"
    "v_dot2c_f32_bf16 v34, v156, v20\n"
    "buffer_load_dwordx4 v0, s[24:27], s47 offen offset:3072 sc1 lds\n"
    "v_dot2c_f32_bf16 v35, v156, v156\n"
    "v_dot2c_f32_bf16 v34, v157, v20\n"
    "v_dot2c_f32_bf16 v35, v157, v157\n"
    "v_dot2c_f32_bf16 v34, v158, v20\n"
    "v_dot2c_f32_bf16 v35, v158, v158\n"
    "v_dot2c_f32_bf16 v34, v159, v20\n"
    "v_dot2c_f32_bf16 v35, v159, v159\n"
    "v_dot2c_f32_bf16 v34, v188, v20\n"
    "v_dot2c_f32_bf16 v35, v188, v188\n"
    "v_dot2c_f32_bf16 v34, v189, v20\n"
    "v_dot2c_f32_bf16 v35, v189, v189\n"
    "v_dot2c_f32_bf16 v34, v190, v20\n"
    "v_dot2c_f32_bf16 v35, v190, v190\n"
    "v_dot2c_f32_bf16 v34, v191, v20\n"
    "s_add_u32 m0, s86, 0x400\n"
    "s_nop 0\n"
    "buffer_load_dword v3, s[24:27], s47 offen sc1 lds\n"
    "v_dot2c_f32_bf16 v35, v191, v191\n"
    "v_dot2c_f32_bf16 v34, v220, v20\n"
    "v_dot2c_f32_bf16 v35, v220, v220\n"
    "v_dot2c_f32_bf16 v34, v221, v20\n"
    "v_dot2c_f32_bf16 v35, v221, v221\n"
    "v_dot2c_f32_bf16 v34, v222, v20\n"
    "v_dot2c_f32_bf16 v35, v222, v222\n"
    "v_dot2c_f32_bf16 v34, v223, v20\n"
    "v_dot2c_f32_bf16 v35, v223, v223\n"
    "v_dot2c_f32_bf16 v34, v252, v20\n"
    "v_dot2c_f32_bf16 v35, v252, v252\n"
    "v_dot2c_f32_bf16 v34, v253, v20\n"
    "v_dot2c_f32_bf16 v35, v253, v253\n"
    "s_mov_b32 m0, s83\n"
    "s_lshl_b32 s45, s39, 10\n"
    "v_add_u32 v95, s45, v0\n"
    "global_load_lds_dwordx4 v95, s[12:13]\n"
    "v_mov_b32 v88, 0\n"
    "v_and_b32 v89, 60, v2\n"
    "v_add_u32 v89, 0x27aa0, v89\n"
    "ds_write_b32 v89, v88\n"
    "v_dot2c_f32_bf16 v34, v254, v20\n"
    "v_dot2c_f32_bf16 v35, v254, v254\n"
    "v_dot2c_f32_bf16 v34, v255, v20\n"
    "v_dot2c_f32_bf16 v35, v255, v255\n"
    "s_waitcnt vmcnt(10)\n"
    "s_branch Lj15_%=\n"
    "Lb14_%=:\n"
    "s_cmp_ge_u32 s94, 196\n"
    "s_cbranch_scc1 Lnb27_%=\n"
    "s_xor_b32 s47, s73, 1024\n"
    "s_add_u32 m0, s47, s74\n"
    "s_add_u32 s47, s47, s72\n"
    "v_xor_b32 v33, 16, v24\n"
    "buffer_load_dwordx4 v33, s[8:11], s47 offen lds\n"
    "s_xor_b32 s47, s73, 2048\n"
    "s_add_u32 m0, s47, s74\n"
    "s_add_u32 s47, s47, s72\n"
    "v_xor_b32 v32, 32, v24\n"
    "buffer_load_dwordx4 v32, s[8:11], s47 offen lds\n"
    "s_xor_b32 s47, s73, 3072\n"
    "s_add_u32 m0, s47, s74\n"
    "s_add_u32 s47, s47, s72\n"
    "v_xor_b32 v33, 48, v24\n"
    "buffer_load_dwordx4 v33, s[8:11], s47 offen lds\n"
    "s_xor_b32 s47, s73, 4096\n"
    "s_add_u32 m0, s47, s74\n"
    "s_add_u32 s47, s47, s72\n"
    "v_xor_b32 v32, 64, v24\n"
    "buffer_load_dwordx4 v32, s[8:11], s47 offen lds\n"
    "s_xor_b32 s47, s73, 5120\n"
    "s_add_u32 m0, s47, s74\n"
    "s_add_u32 s47, s47, s72\n"
    "v_xor_b32 v33, 80, v24\n"
    "buffer_load_dwordx4 v33, s[8:11], s47 offen lds\n"
    "s_xor_b32 s47, s73, 6144\n"
    "s_add_u32 m0, s47, s74\n"
    "s_add_u32 s47, s47, s72\n"
    "v_xor_b32 v32, 96, v24\n"
    "buffer_load_dwordx4 v32, s[8:11], s47 offen lds\n"
    "s_xor_b32 s47, s73, 7168\n"
    "s_add_u32 m0, s47, s74\n"
    "s_add_u32 s47, s47, s72\n"
    "v_xor_b32 v33, 112, v24\n"
    "buffer_load_dwordx4 v33, s[8:11], s47 offen lds\n"
    "s_xor_b32 s47, s73, 8192\n"
    "s_add_u32 m0, s47, s74\n"
    "s_add_u32 s47, s47, s72\n"
    "v_xor_b32 v32, 128, v24\n"
    "buffer_load_dwordx4 v32, s[8:11], s47 offen lds\n"
    "s_xor_b32 s47, s73, 9216\n"
    "s_add_u32 m0, s47, s74\n"
    "s_add_u32 s47, s47, s72\n"
    "v_xor_b32 v33, 144, v24\n"
    "buffer_load_dwordx4 v33, s[8:11], s47 offen lds\n"
    "s_xor_b32 s47, s73, 10240\n"
    "s_add_u32 m0, s47, s74\n"
    "s_add_u32 s47, s47, s72\n"
    "v_xor_b32 v32, 160, v24\n"
    "buffer_load_dwordx4 v32, s[8:11], s47 offen lds\n"
    "Lnb27_%=:\n"
    "s_xor_b32 s47, s73, 11264\n"
    "s_add_u32 m0, s47, s74\n"
    "s_add_u32 s47, s47, s72\n"
    "v_xor_b32 v33, 176, v24\n"
    "buffer_load_dwordx4 v33, s[8:11], s47 offen lds\n"
    "s_xor_b32 s47, s73, 12288\n"
    "s_add_u32 m0, s47, s74\n"
    "s_add_u32 s47, s47, s72\n"
    "v_xor_b32 v32, 192, v24\n"
    "buffer_load_dwordx4 v32, s[8:11], s47 offen lds\n"
    "s_xor_b32 s47, s73, 13312\n"
    "s_add_u32 m0, s47, s74\n"
    "s_add_u32 s47, s47, s72\n"
    "v_xor_b32 v33, 208, v24\n"
    "buffer_load_dwordx4 v33, s[8:11], s47 offen lds\n"
    "s_xor_b32 s47, s73, 14336\n"
    "s_add_u32 m0, s47, s74\n"
    "s_add_u32 s47, s47, s72\n"
    "v_xor_b32 v32, 224, v24\n"
    "buffer_load_dwordx4 v32, s[8:11], s47 offen lds\n"
    "s_xor_b32 s47, s73, 15360\n"
    "s_add_u32 m0, s47, s74\n"
    "s_add_u32 s47, s47, s72\n"
    "v_xor_b32 v33, 240, v24\n"
    "buffer_load_dwordx4 v33, s[8:11], s47 offen lds\n"
    "s_xor_b32 s47, s73, 16384\n"
    "s_add_u32 m0, s47, s74\n"
    "s_add_u32 s47, s47, s72\n"
    "v_xor_b32 v32, 0, v24\n"
    "buffer_load_dwordx4 v32, s[8:11], s47 offen lds\n"
    "s_xor_b32 s47, s73, 17408\n"
    "s_add_u32 m0, s47, s74\n"
    "s_add_u32 s47, s47, s72\n"
    "v_xor_b32 v33, 16, v24\n"
    "buffer_load_dwordx4 v33, s[8:11], s47 offen lds\n"
    "s_xor_b32 s47, s73, 18432\n"
    "s_add_u32 m0, s47, s74\n"
    "s_add_u32 s47, s47, s72\n"
    "v_xor_b32 v32, 32, v24\n"
    "buffer_load_dwordx4 v32, s[8:11], s47 offen lds\n"
    "s_xor_b32 s47, s73, 19456\n"
    "s_add_u32 m0, s47, s74\n"
    "s_add_u32 s47, s47, s72\n"
    "v_xor_b32 v33, 48, v24\n"
    "buffer_load_dwordx4 v33, s[8:11], s47 offen lds\n"
    "s_xor_b32 s47, s73, 20480\n"
    "s_add_u32 m0, s47, s74\n"
    "s_add_u32 s47, s47, s72\n"
    "v_xor_b32 v32, 64, v24\n"
    "buffer_load_dwordx4 v32, s[8:11], s47 offen lds\n"
    "s_xor_b32 s47, s73, 21504\n"
    "s_add_u32 m0, s47, s74\n"
    "s_add_u32 s47, s47, s72\n"
    "v_xor_b32 v33, 80, v24\n"
    "buffer_load_dwordx4 v33, s[8:11], s47 offen lds\n"
    "s_xor_b32 s47, s73, 22528\n"
    "s_add_u32 m0, s47, s74\n"
    "s_add_u32 s47, s47, s72\n"
    "v_xor_b32 v32, 96, v24\n"
    "buffer_load_dwordx4 v32, s[8:11], s47 offen lds\n"
    "s_xor_b32 s47, s73, 23552\n"
    "s_add_u32 m0, s47, s74\n"
    "s_add_u32 s47, s47, s72\n"
    "v_xor_b32 v33, 112, v24\n"
    "buffer_load_dwordx4 v33, s[8:11], s47 offen lds\n"
    "s_xor_b32 s47, s73, 24576\n"
    "s_add_u32 m0, s47, s74\n"
    "s_add_u32 s47, s47, s72\n"
    "v_xor_b32 v32, 128, v24\n"
    "buffer_load_dwordx4 v32, s[8:11], s47 offen lds\n"
    "s_xor_b32 s47, s73, 25600\n"
    "s_add_u32 m0, s47, s74\n"
    "s_add_u32 s47, s47, s72\n"
    "v_xor_b32 v33, 144, v24\n"
    "buffer_load_dwordx4 v33, s[8:11], s47 offen lds\n"
    "s_xor_b32 s47, s73, 26624\n"
    "s_add_u32 m0, s47, s74\n"
    "s_add_u32 s47, s47, s72\n"
    "v_xor_b32 v32, 160, v24\n"
    "buffer_load_dwordx4 v32, s[8:11], s47 offen lds\n"
    "s_xor_b32 s47, s73, 27648\n"
    "s_add_u32 m0, s47, s74\n"
    "s_add_u32 s47, s47, s72\n"
    "v_xor_b32 v33, 176, v24\n"
    "buffer_load_dwordx4 v33, s[8:11], s47 offen lds\n"
    "s_xor_b32 s47, s73, 28672\n"
    "s_add_u32 m0, s47, s74\n"
    "s_add_u32 s47, s47, s72\n"
    "v_xor_b32 v32, 192, v24\n"
    "buffer_load_dwordx4 v32, s[8:11], s47 offen lds\n"
    "s_xor_b32 s47, s73, 29696\n"
    "s_add_u32 m0, s47, s74\n"
    "s_add_u32 s47, s47, s72\n"
    "v_xor_b32 v33, 208, v24\n"
    "buffer_load_dwordx4 v33, s[8:11], s47 offen lds\n"
    "s_xor_b32 s47, s73, 30720\n"
    "s_add_u32 m0, s47, s74\n"
    "s_add_u32 s47, s47, s72\n"
    "v_xor_b32 v32, 224, v24\n"
    "buffer_load_dwordx4 v32, s[8:11], s47 offen lds\n"
    "s_xor_b32 s47, s73, 31744\n"
    "s_add_u32 m0, s47, s74\n"
    "s_add_u32 s47, s47, s72\n"
    "v_xor_b32 v33, 240, v24\n"
    "buffer_load_dwordx4 v33, s[8:11], s47 offen lds\n"
    "s_waitcnt vmcnt(21)\n"
    "s_barrier\n"
    "v_mov_b32 v48, s32\n"
    "v_mov_b32 v49, s33\n"
    "s_cmp_eq_u32 s37, 0\n"
    "s_cselect_b32 s45, s91, 0x40000000\n"
    "s_cselect_b32 s46, s92, 0x40000000\n"
    "s_mov_b64 exec, 0xffff\n"
    "s_add_u32 s47, s45, 0x10000\n"
    "buffer_store_dwordx2 v[48:49], v50, s[24:27], s47 offen sc1\n"
    "s_add_u32 s47, s46, 0x10000\n"
    "buffer_store_dwordx2 v[48:49], v50, s[24:27], s47 offen sc1\n"
    "s_mov_b64 exec, -1\n"
    "ds_read_b64_tr_b16 v[96:97], v94\n"
    "v_add_u32 v94, s75, v94\n"
    "ds_read_b64_tr_b16 v[98:99], v94\n"
    "v_add_u32 v94, s76, v94\n"
    "ds_read_b64_tr_b16 v[100:101], v94\n"
    "v_add_u32 v94, s75, v94\n"
    "ds_read_b64_tr_b16 v[102:103], v94\n"
    "v_add_u32 v94, s77, v94\n"
    "ds_read_b64_tr_b16 v[104:105], v94\n"
    "v_add_u32 v94, s75, v94\n"
    "ds_read_b64_tr_b16 v[106:107], v94\n"
    "v_add_u32 v94, s76, v94\n"
    "ds_read_b64_tr_b16 v[108:109], v94\n"
    "v_add_u32 v94, s75, v94\n"
    "ds_read_b64_tr_b16 v[110:111], v94\n"
    "v_add_u32 v94, s77, v94\n"
    "ds_read_b64_tr_b16 v[112:113], v94\n"
    "v_add_u32 v94, s75, v94\n"
    "ds_read_b64_tr_b16 v[114:115], v94\n"
    "v_add_u32 v94, s76, v94\n"
    "ds_read_b64_tr_b16 v[116:117], v94\n"
    "v_add_u32 v94, s75, v94\n"
    "ds_read_b64_tr_b16 v[118:119], v94\n"
    "v_add_u32 v94, s77, v94\n"
    "ds_read_b64_tr_b16 v[120:121], v94\n"
    "v_add_u32 v94, s75, v94\n"
    "ds_read_b64_tr_b16 v[122:123], v94\n"
    "v_add_u32 v94, s76, v94\n"
    "ds_read_b64_tr_b16 v[124:125], v94\n"
    "v_add_u32 v94, s75, v94\n"
    "ds_read_b64_tr_b16 v[126:127], v94\n"
    "s_waitcnt lgkmcnt(14)\n"
    "v_mfma_f32_32x32x16_bf16 v[64:79], v[20:23], v[96:99], 0\n"
    "buffer_store_dwordx4 v[96:99], v0, s[24:27], s42 offen sc1\n"
    "s_waitcnt lgkmcnt(12)\n"
    "v_mfma_f32_32x32x16_bf16 v[64:79], v[20:23], v[100:103], v[64:79]\n"
    "s_add_u32 s42, s42, s49\n"
    "buffer_store_dwordx4 v[100:103], v0, s[24:27], s42 offen sc1\n"
    "s_waitcnt lgkmcnt(10)\n"
    "v_mfma_f32_32x32x16_bf16 v[64:79], v[20:23], v[104:107], v[64:79]\n"
    "s_add_u32 s42, s42, s49\n"
    "buffer_store_dwordx4 v[104:107], v0, s[24:27], s42 offen sc1\n"
    "s_waitcnt lgkmcnt(8)\n"
    "v_mfma_f32_32x32x16_bf16 v[64:79], v[20:23], v[108:111], v[64:79]\n"
    "s_add_u32 s42, s42, s49\n"
    "buffer_store_dwordx4 v[108:111], v0, s[24:27], s42 offen sc1\n"
    "s_waitcnt lgkmcnt(6)\n"
    "v_mfma_f32_32x32x16_bf16 v[64:79], v[20:23], v[112:115], v[64:79]\n"
    "s_add_u32 s42, s42, s49\n"
    "buffer_store_dwordx4 v[112:115], v0, s[24:27], s42 offen sc1\n"
    "s_waitcnt lgkmcnt(4)\n"
    "v_mfma_f32_32x32x16_bf16 v[64:79], v[20:23], v[116:119], v[64:79]\n"
    "s_add_u32 s42, s42, s49\n"
    "buffer_store_dwordx4 v[116:119], v0, s[24:27], s42 offen sc1\n"
    "s_waitcnt lgkmcnt(2)\n"
    "v_mfma_f32_32x32x16_bf16 v[64:79], v[20:23], v[120:123], v[64:79]\n"
    "s_add_u32 s42, s42, s49\n"
    "buffer_store_dwordx4 v[120:123], v0, s[24:27], s42 offen sc1\n"
    "s_waitcnt lgkmcnt(0)\n"
    "v_mfma_f32_32x32x16_bf16 v[64:79], v[20:23], v[124:127], v[64:79]\n"
    "s_add_u32 s42, s42, s49\n"
    "buffer_store_dwordx4 v[124:127], v0, s[24:27], s42 offen sc1\n"
    "v_lshlrev_b32 v93, 2, v81\n"
    "s_nop 8\n"
    "v_cvt_pk_bf16_f32 v92, v64, v64\n"
    "v_lshlrev_b32 v92, 16, v92\n"
    "v_sub_f32 v92, v64, v92\n"
    "v_cvt_pk_bf16_f32 v92, v64, v92\n"
    "buffer_store_dword v92, v93, s[24:27], s48 offen sc1\n"
    "v_mov_b32 v46, 0\n"
    "v_mov_b32 v47, 0\n"
    "s_mov_b64 exec, s[16:17]\n"
    "buffer_atomic_add_x2 v[46:47], off, s[24:27], s91 offset:64 sc0 sc1\n"
    "s_mov_b64 exec, -1\n"
    "s_waitcnt vmcnt(11)\n"
    "ds_read_b128 v[128:131], v29\n"
    "ds_read_b128 v[160:163], v29 offset:256\n"
    "ds_read_b128 v[192:195], v29 offset:512\n"
    "ds_read_b128 v[224:227], v29 offset:768\n"
    "v_xor_b32 v33, 16, v29\n"
    "ds_read_b128 v[132:135], v33\n"
    "ds_read_b128 v[164:167], v33 offset:256\n"
    "ds_read_b128 v[196:199], v33 offset:512\n"
    "s_waitcnt lgkmcnt(6)\n"
    "v_dot2c_f32_bf16 v34, v128, v20\n"
    "ds_read_b128 v[228:231], v33 offset:768\n"
    "v_dot2c_f32_bf16 v35, v128, v128\n"
    "v_xor_b32 v32, 64, v29\n"
    "ds_read_b128 v[136:139], v32\n"
    "v_dot2c_f32_bf16 v34, v129, v20\n"
    "ds_read_b128 v[168:171], v32 offset:256\n"
    "v_dot2c_f32_bf16 v35, v129, v129\n"
    "ds_read_b128 v[200:203], v32 offset:512\n"
    "v_dot2c_f32_bf16 v34, v130, v20\n"
    "ds_read_b128 v[232:235], v32 offset:768\n"
    "v_dot2c_f32_bf16 v35, v130, v130\n"
    "v_xor_b32 v33, 80, v29\n"
    "ds_read_b128 v[140:143], v33\n"
    "v_dot2c_f32_bf16 v34, v131, v20\n"
    "ds_read_b128 v[172:175], v33 offset:256\n"
    "v_dot2c_f32_bf16 v35, v131, v131\n"
    "ds_read_b128 v[204:207], v33 offset:512\n"
    "s_waitcnt lgkmcnt(13)\n"
    "v_dot2c_f32_bf16 v34, v160, v20\n"
    "ds_read_b128 v[236:239], v33 offset:768\n"
    "v_dot2c_f32_bf16 v35, v160, v160\n"
    "v_xor_b32 v32, 128, v29\n"
    "ds_read_b128 v[144:147], v32\n"
    "v_dot2c_f32_bf16 v34, v161, v20\n"
    "ds_read_b128 v[176:179], v32 offset:256\n"
    "v_dot2c_f32_bf16 v35, v161, v161\n"
    "ds_read_b128 v[208:211], v32 offset:512\n"
    "v_dot2c_f32_bf16 v34, v162, v20\n"
    "ds_read_b128 v[240:243], v32 offset:768\n"
    "v_dot2c_f32_bf16 v35, v162, v162\n"
    "v_xor_b32 v33, 144, v29\n"
    "ds_read_b128 v[148:151], v33\n"
    "v_dot2c_f32_bf16 v34, v163, v20\n"
    "ds_read_b128 v[180:183], v33 offset:256\n"
    "v_dot2c_f32_bf16 v35, v163, v163\n"
    "ds_read_b128 v[212:215], v33 offset:512\n"
    "s_waitcnt lgkmcnt(15)\n"
    "v_dot2c_f32_bf16 v34, v192, v20\n"
    "ds_read_b128 v[244:247], v33 offset:768\n"
    "v_dot2c_f32_bf16 v35, v192, v192\n"
    "v_xor_b32 v32, 192, v29\n"
    "ds_read_b128 v[152:155], v32\n"
    "v_dot2c_f32_bf16 v34, v193, v20\n"
    "ds_read_b128 v[184:187], v32 offset:256\n"
    "v_dot2c_f32_bf16 v35, v193, v193\n"
    "ds_read_b128 v[216:219], v32 offset:512\n"
    "v_dot2c_f32_bf16 v34, v194, v20\n"
    "ds_read_b128 v[248:251], v32 offset:768\n"
    "v_dot2c_f32_bf16 v35, v194, v194\n"
    "v_xor_b32 v33, 208, v29\n"
    "ds_read_b128 v[156:159], v33\n"
    "v_dot2c_f32_bf16 v34, v195, v20\n"
    "ds_read_b128 v[188:191], v33 offset:256\n"
    "v_dot2c_f32_bf16 v35, v195, v195\n"
    "ds_read_b128 v[220:223], v33 offset:512\n"
    "s_waitcnt lgkmcnt(15)\n"
    "v_dot2c_f32_bf16 v34, v224, v20\n"
    "ds_read_b128 v[252:255], v33 offset:768\n"
    "v_dot2c_f32_bf16 v35, v224, v224\n"
    "v_dot2c_f32_bf16 v34, v225, v20\n"
    "v_dot2c_f32_bf16 v35, v225, v225\n"
    "v_dot2c_f32_bf16 v34, v226, v20\n"
    "v_dot2c_f32_bf16 v35, v226, v226\n"
    "v_dot2c_f32_bf16 v34, v227, v20\n"
    "v_dot2c_f32_bf16 v35, v227, v227\n"
    "s_add_u32 s44, s20, 12\n"
    "s_waitcnt vmcnt(0)\n"
    "v_cmp_ne_u32 vcc, s44, v46\n"
    "s_nop 0\n"
    "s_and_b64 vcc, vcc, s[16:17]\n"
    "s_cbranch_scc1 Lrd23_%=\n"
    "Lrk24_%=:\n"
    "s_waitcnt lgkmcnt(0)\n"
    "s_barrier\n"
    "s_add_u32 m0, s78, 0x0\n"
    "s_add_u32 s47, s66, 0x82000\n"
    "buffer_load_dwordx4 v0, s[24:27], s47 offen sc1 lds\n"
    "buffer_load_dwordx4 v0, s[24:27], s47 offen offset:1024 sc1 lds\n"
    "v_dot2c_f32_bf16 v34, v132, v20\n"
    "v_dot2c_f32_bf16 v35, v132, v132\n"
    "v_dot2c_f32_bf16 v34, v133, v20\n"
    "v_dot2c_f32_bf16 v35, v133, v133\n"
    "v_dot2c_f32_bf16 v34, v134, v20\n"
    "v_dot2c_f32_bf16 v35, v134, v134\n"
    "v_dot2c_f32_bf16 v34, v135, v20\n"
    "buffer_load_dwordx4 v0, s[24:27], s47 offen offset:2048 sc1 lds\n"
    "v_dot2c_f32_bf16 v35, v135, v135\n"
    "v_dot2c_f32_bf16 v34, v164, v20\n"
    "v_dot2c_f32_bf16 v35, v164, v164\n"
    "v_dot2c_f32_bf16 v34, v165, v20\n"
    "v_dot2c_f32_bf16 v35, v165, v165\n"
    "v_dot2c_f32_bf16 v34, v166, v20\n"
    "v_dot2c_f32_bf16 v35, v166, v166\n"
    "v_dot2c_f32_bf16 v34, v167, v20\n"
    "v_dot2c_f32_bf16 v35, v167, v167\n"
    "v_dot2c_f32_bf16 v34, v196, v20\n"
    "v_dot2c_f32_bf16 v35, v196, v196\n"
    "v_dot2c_f32_bf16 v34, v197, v20\n"
    "v_dot2c_f32_bf16 v35, v197, v197\n"
    "buffer_load_dwordx4 v0, s[24:27], s47 offen offset:3072 sc1 lds\n"
    "v_dot2c_f32_bf16 v34, v198, v20\n"
    "v_dot2c_f32_bf16 v35, v198, v198\n"
    "v_dot2c_f32_bf16 v34, v199, v20\n"
    "v_dot2c_f32_bf16 v35, v199, v199\n"
    "v_dot2c_f32_bf16 v34, v228, v20\n"
    "v_dot2c_f32_bf16 v35, v228, v228\n"
    "v_dot2c_f32_bf16 v34, v229, v20\n"
    "v_dot2c_f32_bf16 v35, v229, v229\n"
    "v_dot2c_f32_bf16 v34, v230, v20\n"
    "v_dot2c_f32_bf16 v35, v230, v230\n"
    "v_dot2c_f32_bf16 v34, v231, v20\n"
    "v_dot2c_f32_bf16 v35, v231, v231\n"
    "s_add_u32 m0, s78, 0x1000\n"
    "s_add_u32 s47, s67, 0x82000\n"
    "buffer_load_dwordx4 v0, s[24:27], s47 offen sc1 lds\n"
    "v_dot2c_f32_bf16 v34, v136, v20\n"
    "v_dot2c_f32_bf16 v35, v136, v136\n"
    "v_dot2c_f32_bf16 v34, v137, v20\n"
    "v_dot2c_f32_bf16 v35, v137, v137\n"
    "v_dot2c_f32_bf16 v34, v138, v20\n"
    "v_dot2c_f32_bf16 v35, v138, v138\n"
    "v_dot2c_f32_bf16 v34, v139, v20\n"
    "v_dot2c_f32_bf16 v35, v139, v139\n"
    "v_dot2c_f32_bf16 v34, v168, v20\n"
    "v_dot2c_f32_bf16 v35, v168, v168\n"
    "v_dot2c_f32_bf16 v34, v169, v20\n"
    "v_dot2c_f32_bf16 v35, v169, v169\n"
    "v_dot2c_f32_bf16 v34, v170, v20\n"
    "buffer_load_dwordx4 v0, s[24:27], s47 offen offset:1024 sc1 lds\n"
    "v_dot2c_f32_bf16 v35, v170, v170\n"
    "v_dot2c_f32_bf16 v34, v171, v20\n"
    "v_dot2c_f32_bf16 v35, v171, v171\n"
    "v_dot2c_f32_bf16 v34, v200, v20\n"
    "v_dot2c_f32_bf16 v35, v200, v200\n"
    "v_dot2c_f32_bf16 v34, v201, v20\n"
    "v_dot2c_f32_bf16 v35, v201, v201\n"
    "v_dot2c_f32_bf16 v34, v202, v20\n"
    "v_dot2c_f32_bf16 v35, v202, v202\n"
    "v_dot2c_f32_bf16 v34, v203, v20\n"
    "v_dot2c_f32_bf16 v35, v203, v203\n"
    "v_dot2c_f32_bf16 v34, v232, v20\n"
    "v_mov_b32 v32, 1\n"
    "v_mov_b32 v33, 0\n"
    "s_mov_b64 exec, s[14:15]\n"
    "buffer_atomic_add_x2 v[32:33], v18, s[24:27], s21 offen sc0 sc1\n"
    "s_mov_b64 exec, -1\n"
    "v_dot2c_f32_bf16 v35, v232, v232\n"
    "v_dot2c_f32_bf16 v34, v233, v20\n"
    "v_dot2c_f32_bf16 v35, v233, v233\n"
    "v_dot2c_f32_bf16 v34, v234, v20\n"
    "v_dot2c_f32_bf16 v35, v234, v234\n"
    "v_dot2c_f32_bf16 v34, v235, v20\n"
    "v_dot2c_f32_bf16 v35, v235, v235\n"
    "v_dot2c_f32_bf16 v34, v140, v20\n"
    "v_dot2c_f32_bf16 v35, v140, v140\n"
    "v_dot2c_f32_bf16 v34, v141, v20\n"
    "v_dot2c_f32_bf16 v35, v141, v141\n"
    "v_dot2c_f32_bf16 v34, v142, v20\n"
    "v_dot2c_f32_bf16 v35, v142, v142\n"
    "buffer_load_dwordx4 v0, s[24:27], s47 offen offset:2048 sc1 lds\n"
    "v_dot2c_f32_bf16 v34, v143, v20\n"
    "v_dot2c_f32_bf16 v35, v143, v143\n"
    "v_dot2c_f32_bf16 v34, v172, v20\n"
    "v_dot2c_f32_bf16 v35, v172, v172\n"
    "v_dot2c_f32_bf16 v34, v173, v20\n"
    "v_dot2c_f32_bf16 v35, v173, v173\n"
    "v_dot2c_f32_bf16 v34, v174, v20\n"
    "v_dot2c_f32_bf16 v35, v174, v174\n"
    "v_dot2c_f32_bf16 v34, v175, v20\n"
    "v_dot2c_f32_bf16 v35, v175, v175\n"
    "v_dot2c_f32_bf16 v34, v204, v20\n"
    "v_dot2c_f32_bf16 v35, v204, v204\n"
    "buffer_load_dwordx4 v0, s[24:27], s47 offen offset:3072 sc1 lds\n"
    "v_dot2c_f32_bf16 v34, v205, v20\n"
    "v_dot2c_f32_bf16 v35, v205, v205\n"
    "v_dot2c_f32_bf16 v34, v206, v20\n"
    "v_dot2c_f32_bf16 v35, v206, v206\n"
    "v_dot2c_f32_bf16 v34, v207, v20\n"
    "v_dot2c_f32_bf16 v35, v207, v207\n"
    "v_dot2c_f32_bf16 v34, v236, v20\n"
    "v_dot2c_f32_bf16 v35, v236, v236\n"
    "v_dot2c_f32_bf16 v34, v237, v20\n"
    "v_dot2c_f32_bf16 v35, v237, v237\n"
    "v_dot2c_f32_bf16 v34, v238, v20\n"
    "v_dot2c_f32_bf16 v35, v238, v238\n"
    "v_dot2c_f32_bf16 v34, v239, v20\n"
    "s_add_u32 m0, s86, 0x0\n"
    "s_nop 0\n"
    "buffer_load_dword v3, s[24:27], s47 offen sc1 lds\n"
    "v_dot2c_f32_bf16 v35, v239, v239\n"
    "v_dot2c_f32_bf16 v34, v144, v20\n"
    "v_dot2c_f32_bf16 v35, v144, v144\n"
    "v_dot2c_f32_bf16 v34, v145, v20\n"
    "v_dot2c_f32_bf16 v35, v145, v145\n"
    "v_dot2c_f32_bf16 v34, v146, v20\n"
    "v_dot2c_f32_bf16 v35, v146, v146\n"
    "v_dot2c_f32_bf16 v34, v147, v20\n"
    "v_dot2c_f32_bf16 v35, v147, v147\n"
    "v_dot2c_f32_bf16 v34, v176, v20\n"
    "v_dot2c_f32_bf16 v35, v176, v176\n"
    "v_dot2c_f32_bf16 v34, v177, v20\n"
    "s_add_u32 m0, s78, 0x10000\n"
    "s_add_u32 s47, s66, 0x104000\n"
    "buffer_load_dwordx4 v0, s[24:27], s47 offen sc1 lds\n"
    "v_dot2c_f32_bf16 v35, v177, v177\n"
    "v_dot2c_f32_bf16 v34, v178, v20\n"
    "v_dot2c_f32_bf16 v35, v178, v178\n"
    "v_dot2c_f32_bf16 v34, v179, v20\n"
    "v_dot2c_f32_bf16 v35, v179, v179\n"
    "v_dot2c_f32_bf16 v34, v208, v20\n"
    "v_dot2c_f32_bf16 v35, v208, v208\n"
    "v_dot2c_f32_bf16 v34, v209, v20\n"
    "v_dot2c_f32_bf16 v35, v209, v209\n"
    "v_dot2c_f32_bf16 v34, v210, v20\n"
    "v_dot2c_f32_bf16 v35, v210, v210\n"
    "v_dot2c_f32_bf16 v34, v211, v20\n"
    "v_dot2c_f32_bf16 v35, v211, v211\n"
    "buffer_load_dwordx4 v0, s[24:27], s47 offen offset:1024 sc1 lds\n"
    "v_dot2c_f32_bf16 v34, v240, v20\n"
    "v_dot2c_f32_bf16 v35, v240, v240\n"
    "v_dot2c_f32_bf16 v34, v241, v20\n"
    "v_dot2c_f32_bf16 v35, v241, v241\n"
    "v_dot2c_f32_bf16 v34, v242, v20\n"
    "v_dot2c_f32_bf16 v35, v242, v242\n"
    "v_dot2c_f32_bf16 v34, v243, v20\n"
    "v_dot2c_f32_bf16 v35, v243, v243\n"
    "v_dot2c_f32_bf16 v34, v148, v20\n"
    "v_dot2c_f32_bf16 v35, v148, v148\n"
    "v_dot2c_f32_bf16 v34, v149, v20\n"
    "v_dot2c_f32_bf16 v35, v149, v149\n"
    "buffer_load_dwordx4 v0, s[24:27], s47 offen offset:2048 sc1 lds\n"
    "v_dot2c_f32_bf16 v34, v150, v20\n"
    "v_dot2c_f32_bf16 v35, v150, v150\n"
    "v_dot2c_f32_bf16 v34, v151, v20\n"
    "v_dot2c_f32_bf16 v35, v151, v151\n"
    "v_dot2c_f32_bf16 v34, v180, v20\n"
    "v_dot2c_f32_bf16 v35, v180, v180\n"
    "v_dot2c_f32_bf16 v34, v181, v20\n"
    "v_dot2c_f32_bf16 v35, v181, v181\n"
    "v_dot2c_f32_bf16 v34, v182, v20\n"
    "v_dot2c_f32_bf16 v35, v182, v182\n"
    "v_dot2c_f32_bf16 v34, v183, v20\n"
    "v_dot2c_f32_bf16 v35, v183, v183\n"
    "v_dot2c_f32_bf16 v34, v212, v20\n"
    "buffer_load_dwordx4 v0, s[24:27], s47 offen offset:3072 sc1 lds\n"
    "v_dot2c_f32_bf16 v35, v212, v212\n"
    "v_dot2c_f32_bf16 v34, v213, v20\n"
    "v_dot2c_f32_bf16 v35, v213, v213\n"
    "v_dot2c_f32_bf16 v34, v214, v20\n"
    "v_dot2c_f32_bf16 v35, v214, v214\n"
    "v_dot2c_f32_bf16 v34, v215, v20\n"
    "v_dot2c_f32_bf16 v35, v215, v215\n"
    "v_dot2c_f32_bf16 v34, v244, v20\n"
    "v_dot2c_f32_bf16 v35, v244, v244\n"
    "v_dot2c_f32_bf16 v34, v245, v20\n"
    "v_dot2c_f32_bf16 v35, v245, v245\n"
    "v_dot2c_f32_bf16 v34, v246, v20\n"
    "s_add_u32 m0, s78, 0x11000\n"
    "s_add_u32 s47, s67, 0x104000\n"
    "buffer_load_dwordx4 v0, s[24:27], s47 offen sc1 lds\n"
    "v_dot2c_f32_bf16 v35, v246, v246\n"
    "v_dot2c_f32_bf16 v34, v247, v20\n"
    "v_dot2c_f32_bf16 v35, v247, v247\n"
    "v_dot2c_f32_bf16 v34, v152, v20\n"
    "v_dot2c_f32_bf16 v35, v152, v152\n"
    "v_dot2c_f32_bf16 v34, v153, v20\n"
    "v_dot2c_f32_bf16 v35, v153, v153\n"
    "v_dot2c_f32_bf16 v34, v154, v20\n"
    "v_dot2c_f32_bf16 v35, v154, v154\n"
    "v_dot2c_f32_bf16 v34, v155, v20\n"
    "v_dot2c_f32_bf16 v35, v155, v155\n"
    "v_dot2c_f32_bf16 v34, v184, v20\n"
    "v_dot2c_f32_bf16 v35, v184, v184\n"
    "buffer_load_dwordx4 v0, s[24:27], s47 offen offset:1024 sc1 lds\n"
    "v_dot2c_f32_bf16 v34, v185, v20\n"
    "v_dot2c_f32_bf16 v35, v185, v185\n"
    "v_dot2c_f32_bf16 v34, v186, v20\n"
    "v_dot2c_f32_bf16 v35, v186, v186\n"
    "v_dot2c_f32_bf16 v34, v187, v20\n"
    "v_dot2c_f32_bf16 v35, v187, v187\n"
    "v_dot2c_f32_bf16 v34, v216, v20\n"
    "v_dot2c_f32_bf16 v35, v216, v216\n"
    "v_dot2c_f32_bf16 v34, v217, v20\n"
    "v_dot2c_f32_bf16 v35, v217, v217\n"
    "v_dot2c_f32_bf16 v34, v218, v20\n"
    "v_dot2c_f32_bf16 v35, v218, v218\n"
    "buffer_load_dwordx4 v0, s[24:27], s47 offen offset:2048 sc1 lds\n"
    "v_dot2c_f32_bf16 v34, v219, v20\n"
    "v_dot2c_f32_bf16 v35, v219, v219\n"
    "v_dot2c_f32_bf16 v34, v248, v20\n"
    "v_dot2c_f32_bf16 v35, v248, v248\n"
    "v_dot2c_f32_bf16 v34, v249, v20\n"
    "v_dot2c_f32_bf16 v35, v249, v249\n"
    "v_dot2c_f32_bf16 v34, v250, v20\n"
    "v_dot2c_f32_bf16 v35, v250, v250\n"
    "v_dot2c_f32_bf16 v34, v251, v20\n"
    "v_dot2c_f32_bf16 v35, v251, v251\n"
    "v_dot2c_f32_bf16 v34, v156, v20\n"
    "v_dot2c_f32_bf16 v35, v156, v156\n"
    "v_dot2c_f32_bf16 v34, v157, v20\n"
    "buffer_load_dwordx4 v0, s[24:27], s47 offen offset:3072 sc1 lds\n"
    "v_dot2c_f32_bf16 v35, v157, v157\n"
    "v_dot2c_f32_bf16 v34, v158, v20\n"
    "v_dot2c_f32_bf16 v35, v158, v158\n"
    "v_dot2c_f32_bf16 v34, v159, v20\n"
    "v_dot2c_f32_bf16 v35, v159, v159\n"
    "v_dot2c_f32_bf16 v34, v188, v20\n"
    "v_dot2c_f32_bf16 v35, v188, v188\n"
    "v_dot2c_f32_bf16 v34, v189, v20\n"
    "v_dot2c_f32_bf16 v35, v189, v189\n"
    "v_dot2c_f32_bf16 v34, v190, v20\n"
    "v_dot2c_f32_bf16 v35, v190, v190\n"
    "v_dot2c_f32_bf16 v34, v191, v20\n"
    "v_dot2c_f32_bf16 v35, v191, v191\n"
    "s_add_u32 m0, s86, 0x400\n"
    "s_nop 0\n"
    "buffer_load_dword v3, s[24:27], s47 offen sc1 lds\n"
    "v_dot2c_f32_bf16 v34, v220, v20\n"
    "v_dot2c_f32_bf16 v35, v220, v220\n"
    "v_dot2c_f32_bf16 v34, v221, v20\n"
    "v_dot2c_f32_bf16 v35, v221, v221\n"
    "v_dot2c_f32_bf16 v34, v222, v20\n"
    "v_dot2c_f32_bf16 v35, v222, v222\n"
    "v_dot2c_f32_bf16 v34, v223, v20\n"
    "v_dot2c_f32_bf16 v35, v223, v223\n"
    "v_dot2c_f32_bf16 v34, v252, v20\n"
    "v_dot2c_f32_bf16 v35, v252, v252\n"
    "v_dot2c_f32_bf16 v34, v253, v20\n"
    "v_dot2c_f32_bf16 v35, v253, v253\n"
    "s_mov_b32 m0, s83\n"
    "s_lshl_b32 s45, s39, 10\n"
    "v_add_u32 v95, s45, v0\n"
    "global_load_lds_dwordx4 v95, s[12:13]\n"
    "v_mov_b32 v88, 0\n"
    "v_and_b32 v89, 60, v2\n"
    "v_add_u32 v89, 0x27aa0, v89\n"
    "ds_write_b32 v89, v88\n"
    "v_dot2c_f32_bf16 v34, v254, v20\n"
    "v_dot2c_f32_bf16 v35, v254, v254\n"
    "v_dot2c_f32_bf16 v34, v255, v20\n"
    "v_dot2c_f32_bf16 v35, v255, v255\n"
    "s_waitcnt vmcnt(10)\n"
    "Lj15_%=:\n"
    "s_waitcnt lgkmcnt(0)\n"
    "s_barrier\n"
    "ds_read_b128 v[40:43], v0\n"
    "ds_read_b128 v[44:47], v0 offset:1024\n"
    "ds_read_b128 v[48:51], v0 offset:2048\n"
    "ds_read_b128 v[52:55], v0 offset:3072\n"
    "ds_read_b128 v[56:59], v0 offset:4096\n"
    "s_waitcnt lgkmcnt(3)\n"
    "v_mfma_f32_32x32x16_bf16 v[64:79], v[40:43], v[128:131], 0\n"
    "ds_read_b128 v[60:63], v0 offset:5120\n"
    "v_mfma_f32_32x32x16_bf16 v[64:79], v[44:47], v[132:135], v[64:79]\n"
    "ds_read_b128 v[40:43], v0 offset:6144\n"
    "v_mov_b32 v28, v34\n"
    "v_mov_b32 v29, v35\n"
    "s_nop 0\n"
    "v_permlane32_swap_b32 v28, v34\n"
    "v_permlane32_swap_b32 v29, v35\n"
    "s_waitcnt lgkmcnt(3)\n"
    "v_mfma_f32_32x32x16_bf16 v[64:79], v[48:51], v[136:139], v[64:79]\n"
    "ds_read_b128 v[44:47], v0 offset:7168\n"
    "v_add_f32 v28, v28, v34\n"
    "v_add_f32 v29, v29, v35\n"
    "v_mul_f32 v28, 0xbb000000, v28\n"
    "v_mul_f32 v29, 0x3b000000, v29\n"
    "v_cvt_pk_bf16_f32 v30, v28, v28\n"
    "v_mfma_f32_32x32x16_bf16 v[64:79], v[52:55], v[140:143], v[64:79]\n"
    "ds_read_b128 v[48:51], v0 offset:8192\n"
    "v_fma_f32 v29, -v28, v28, v29\n"
    "v_lshlrev_b32 v31, 16, v30\n"
    "v_max_f32 v29, 0, v29\n"
    "v_sub_f32 v31, v28, v31\n"
    "s_waitcnt lgkmcnt(3)\n"
    "v_mfma_f32_32x32x16_bf16 v[64:79], v[56:59], v[144:147], v[64:79]\n"
    "ds_read_b128 v[52:55], v0 offset:9216\n"
    "v_add_f32 v29, 0x3727c5ac, v29\n"
    "v_cvt_pk_bf16_f32 v31, v31, v31\n"
    "v_rsq_f32 v16, v29\n"
    "s_nop 0\n"
    "v_permlane32_swap_b32 v30, v31\n"
    "v_mfma_f32_32x32x16_bf16 v[64:79], v[60:63], v[148:151], v[64:79]\n"
    "ds_read_b128 v[56:59], v0 offset:10240\n"
    "v_mul_f32 v17, 0x3fb8aa3b, v16\n"
    "v_mov_b32 v20, v30\n"
    "v_mov_b32 v21, v30\n"
    "v_mov_b32 v22, v30\n"
    "s_waitcnt lgkmcnt(3)\n"
    "v_mfma_f32_32x32x16_bf16 v[64:79], v[40:43], v[152:155], v[64:79]\n"
    "s_cmp_eq_u32 s37, 1\n"
    "s_cselect_b32 s44, s88, s90\n"
    "s_cmp_eq_u32 s37, 0\n"
    "s_cselect_b32 s44, s89, s44\n"
    "s_cselect_b32 s49, 7, 3\n"
    "v_and_b32 v34, 0xf0, v0\n"
    "v_lshlrev_b32 v34, s49, v34\n"
    "s_add_u32 s44, s44, 0x10000\n"
    "buffer_load_dwordx2 v[34:35], v34, s[24:27], s44 offen sc1\n"
    "ds_read_b128 v[60:63], v0 offset:11264\n"
    "v_mov_b32 v23, v30\n"
    "v_mfma_f32_32x32x16_bf16 v[64:79], v[44:47], v[156:159], v[64:79]\n"
    "ds_read_b128 v[40:43], v0 offset:12288\n"
    "v_and_b32 v94, 1, v80\n"
    "v_cmp_eq_u32 vcc, v94, v82\n"
    "v_and_b32 v95, 31, v84\n"
    "v_lshl_add_u32 v95, v85, 5, v95\n"
    "s_waitcnt lgkmcnt(3)\n"
    "v_mfma_f32_32x32x16_bf16 v[64:79], v[48:51], v[160:163], v[64:79]\n"
    "ds_read_b128 v[44:47], v0 offset:13312\n"
    "v_lshl_add_u32 v95, v82, 9, v95\n"
    "v_add_u32 v95, s83, v95\n"
    "v_mov_b32 v88, 0x27aa0\n"
    "s_waitcnt vmcnt(1)\n"
    "v_add_u32 v94, 0, v95\n"
    "v_mfma_f32_32x32x16_bf16 v[64:79], v[52:55], v[164:167], v[64:79]\n"
    "ds_read_b128 v[48:51], v0 offset:14336\n"
    "v_cndmask_b32 v94, v88, v94, vcc\n"
    "ds_read_b64_tr_b16 a[240:241], v94\n"
    "v_add_u32 v94, 128, v95\n"
    "v_cndmask_b32 v94, v88, v94, vcc\n"
    "s_waitcnt lgkmcnt(4)\n"
    "v_mfma_f32_32x32x16_bf16 v[64:79], v[56:59], v[168:171], v[64:79]\n"
    "ds_read_b128 v[52:55], v0 offset:15360\n"
    "ds_read_b64_tr_b16 a[242:243], v94\n"
    "v_add_u32 v94, 256, v95\n"
    "v_cndmask_b32 v94, v88, v94, vcc\n"
    "ds_read_b64_tr_b16 a[244:245], v94\n"
    "v_add_u32 v94, 384, v95\n"
    "v_mfma_f32_32x32x16_bf16 v[64:79], v[60:63], v[172:175], v[64:79]\n"
    "ds_read_b128 v[56:59], v0 offset:16384\n"
    "v_cndmask_b32 v94, v88, v94, vcc\n"
    "ds_read_b64_tr_b16 a[246:247], v94\n"
    "s_getpc_b64 s[28:29]\n"
    "Lpc13_%=:\n"
    "s_and_b32 s29, s29, 0xffff\n"
    "s_waitcnt lgkmcnt(7)\n"
    "v_mfma_f32_32x32x16_bf16 v[64:79], v[40:43], v[176:179], v[64:79]\n"
    "v_mov_b32 v24, 0\n"
    "v_mov_b32 v25, 0\n"
    "s_mov_b64 exec, s[16:17]\n"
    "buffer_atomic_add_x2 v[24:25], off, s[24:27], s91 offset:72 sc0 sc1\n"
    "s_mov_b64 exec, -1\n"
    "ds_read_b128 v[60:63], v0 offset:17408\n"
    "v_mfma_f32_32x32x16_bf16 v[64:79], v[44:47], v[180:183], v[64:79]\n"
    "s_add_u32 m0, s78, 0x8000\n"
    "s_add_u32 s47, s66, 0x0\n"
    "buffer_load_dwordx4 v0, s[24:27], s47 offen sc1 lds\n"
    "ds_read_b128 v[40:43], v0 offset:18432\n"
    "s_waitcnt lgkmcnt(6)\n"
    "v_mfma_f32_32x32x16_bf16 v[64:79], v[48:51], v[184:187], v[64:79]\n"
    "v_mov_b32 v26, 0\n"
    "v_mov_b32 v27, 0\n"
    "s_mov_b64 exec, s[16:17]\n"
    "buffer_atomic_add_x2 v[26:27], off, s[24:27], s92 offset:64 sc0 sc1\n"
    "s_mov_b64 exec, -1\n"
    "ds_read_b128 v[44:47], v0 offset:19456\n"
    "v_mfma_f32_32x32x16_bf16 v[64:79], v[52:55], v[188:191], v[64:79]\n"
    "buffer_load_dwordx4 v0, s[24:27], s47 offen offset:1024 sc1 lds\n"
    "ds_read_b128 v[48:51], v0 offset:20480\n"
    "s_mov_b32 s30, Lend_%= - Lpc13_%=\n"
    "s_lshl2_add_u32 s87, s87, s37\n"
    "s_waitcnt lgkmcnt(3)\n"
    "v_mfma_f32_32x32x16_bf16 v[64:79], v[56:59], v[192:195], v[64:79]\n"
    "ds_read_b128 v[52:55], v0 offset:21504\n"
    "s_lshl_b32 s87, s87, 9\n"
    "s_add_u32 s45, s38, 0\n"
    "s_and_b32 s45, s45, 3\n"
    "s_lshl_b32 s74, s45, 11\n"
    "s_add_u32 s45, s38, 1\n"
    "v_mfma_f32_32x32x16_bf16 v[64:79], v[60:63], v[196:199], v[64:79]\n"
    "buffer_load_dwordx4 v0, s[24:27], s47 offen offset:2048 sc1 lds\n"
    "ds_read_b128 v[56:59], v0 offset:22528\n"
    "s_and_b32 s45, s45, 3\n"
    "s_lshl_b32 s75, s45, 11\n"
    "s_waitcnt lgkmcnt(3)\n"
    "v_mfma_f32_32x32x16_bf16 v[64:79], v[40:43], v[200:203], v[64:79]\n"
    "ds_read_b128 v[60:63], v0 offset:23552\n"
    "s_add_u32 s45, s38, 2\n"
    "s_and_b32 s45, s45, 3\n"
    "s_lshl_b32 s76, s45, 11\n"
    "s_add_u32 s45, s38, 3\n"
    "s_and_b32 s45, s45, 3\n"
    "v_mfma_f32_32x32x16_bf16 v[64:79], v[44:47], v[204:207], v[64:79]\n"
    "buffer_load_dwordx4 v0, s[24:27], s47 offen offset:3072 sc1 lds\n"
    "ds_read_b128 v[40:43], v0 offset:24576\n"
    "s_lshl_b32 s77, s45, 11\n"
    "s_mul_i32 s45, s37, 22\n"
    "s_waitcnt lgkmcnt(3)\n"
    "v_mfma_f32_32x32x16_bf16 v[64:79], v[48:51], v[208:211], v[64:79]\n"
    "ds_read_b128 v[44:47], v0 offset:25600\n"
    "s_cmp_eq_u32 s37, 3\n"
    "s_subb_u32 s45, s45, 0\n"
    "s_lshl_b32 s45, s45, 10\n"
    "s_add_u32 s45, s45, 0x2200000\n"
    "s_and_b32 s46, s37, 1\n"
    "v_mfma_f32_32x32x16_bf16 v[64:79], v[52:55], v[212:215], v[64:79]\n"
    "s_add_u32 m0, s78, 0x9000\n"
    "s_add_u32 s47, s67, 0x0\n"
    "buffer_load_dwordx4 v0, s[24:27], s47 offen sc1 lds\n"
    "ds_read_b128 v[48:51], v0 offset:26624\n"
    "s_waitcnt lgkmcnt(3)\n"
    "v_mfma_f32_32x32x16_bf16 v[64:79], v[56:59], v[216:219], v[64:79]\n"
    "ds_read_b128 v[52:55], v0 offset:27648\n"
    "s_lshl1_add_u32 s46, s41, s46\n"
    "s_mul_i32 s84, s46, 0x16000\n"
    "s_add_u32 s84, s84, s45\n"
    "s_xor_b32 s43, s46, 1\n"
    "s_mul_i32 s85, s43, 0x16000\n"
    "v_mfma_f32_32x32x16_bf16 v[64:79], v[60:63], v[220:223], v[64:79]\n"
    "ds_read_b128 v[56:59], v0 offset:28672\n"
    "v_and_b32 v32, 0xffffff00, v32\n"
    "v_xor_b32 v32, s20, v32\n"
    "v_xor_b32 v33, s33, v33\n"
    "v_or_b32 v32, v32, v33\n"
    "v_cmp_ne_u32 vcc, 0, v32\n"
    "s_waitcnt lgkmcnt(3)\n"
    "v_mfma_f32_32x32x16_bf16 v[64:79], v[40:43], v[224:227], v[64:79]\n"
    "s_and_b64 s[14:15], s[14:15], vcc\n"
    "s_cbranch_scc1 Lrep25_%=\n"
    "Lvok26_%=:\n"
    "v_lshlrev_b32 v15, 10, v81\n"
    "v_lshl_add_u32 v15, v82, 5, v15\n"
    "buffer_load_dwordx4 a[248:251], v15, s[8:11], s34 offen\n"
    "ds_read_b128 v[60:63], v0 offset:29696\n"
    "v_mfma_f32_32x32x16_bf16 v[64:79], v[44:47], v[228:231], v[64:79]\n"
    "buffer_load_dwordx4 v0, s[24:27], s47 offen offset:1024 sc1 lds\n"
    "ds_read_b128 v[40:43], v0 offset:30720\n"
    "s_add_u32 s85, s85, s45\n"
    "s_lshl_b32 s46, s46, 15\n"
    "s_waitcnt lgkmcnt(3)\n"
    "v_mfma_f32_32x32x16_bf16 v[64:79], v[48:51], v[232:235], v[64:79]\n"
    "s_barrier\n"
    "buffer_load_dwordx4 a[252:255], v15, s[8:11], s34 offen offset:16\n"
    "ds_read_b128 v[44:47], v0 offset:31744\n"
    "s_lshl_b32 s45, s37, 10\n"
    "v_mfma_f32_32x32x16_bf16 v[64:79], v[52:55], v[236:239], v[64:79]\n"
    "buffer_load_dwordx4 v0, s[24:27], s47 offen offset:2048 sc1 lds\n"
    "ds_read_b128 v[48:51], v4 offset:0\n"
    "s_add_u32 s46, s46, s45\n"
    "s_add_u32 s72, s46, 0x2400000\n"
    "s_waitcnt lgkmcnt(3)\n"
    "v_mfma_f32_32x32x16_bf16 v[64:79], v[56:59], v[240:243], v[64:79]\n"
    "ds_read_b128 v[52:55], v1\n"
    "s_cmp_eq_u32 s68, s70\n"
    "s_cselect_b32 s48, 24, 20\n"
    "s_add_u32 s44, s20, s48\n"
    "s_waitcnt vmcnt(9)\n"
    "v_cmp_ne_u32 vcc, s44, v24\n"
    "s_xor_b32 s73, s72, 0x8000\n"
    "v_mfma_f32_32x32x16_bf16 v[64:79], v[60:63], v[244:247], v[64:79]\n"
    "s_and_b64 vcc, vcc, s[16:17]\n"
    "s_cbranch_scc1 Lrd28_%=\n"
    "Lrk29_%=:\n"
    "ds_read_b128 v[56:59], v1 offset:1024\n"
    "s_and_b32 s94, s38, 3\n"
    "s_lshl_b32 s94, s94, 7\n"
    "s_lshl_b32 s93, s41, 7\n"
    "s_waitcnt lgkmcnt(3)\n"
    "s_add_u32 s44, s20, 30\n"
    "s_waitcnt vmcnt(8)\n"
    "v_cmp_ne_u32 vcc, s44, v26\n"
    "v_mfma_f32_32x32x16_bf16 v[64:79], v[40:43], v[248:251], v[64:79]\n"
    "s_and_b64 vcc, vcc, s[16:17]\n"
    "s_cbranch_scc1 Lrd30_%=\n"
    "Lrk31_%=:\n"
    "s_barrier\n"
    "buffer_load_dwordx4 v0, s[24:27], s47 offen offset:3072 sc1 lds\n"
    "ds_read_b128 v[60:63], v1 offset:2048\n"
    "v_mfma_f32_32x32x16_bf16 v[64:79], v[44:47], v[252:255], v[64:79]\n"
    "v_lshlrev_b32 v18, 1, v2\n"
    "buffer_store_dwordx2 v[36:37], v18, s[24:27], s54 offen sc1\n"
    "ds_read_b128 v[40:43], v1 offset:3072\n"
    "s_waitcnt lgkmcnt(3)\n"
    "v_mfma_f32_32x32x16_bf16 v[64:79], v[48:51], v[20:23], v[64:79]\n"
    "s_add_u32 m0, s86, 0x200\n"
    "s_nop 0\n"
    "buffer_load_dword v3, s[24:27], s47 offen sc1 lds\n"
    "ds_read_b128 v[44:47], v1 offset:4096\n"
    "v_mfma_f32_32x32x16_bf16 v[80:95], v[52:55], v[128:131], 0\n"
    "buffer_load_dwordx4 a[176:179], v0, s[24:27], s72 offen sc1\n"
    "ds_read_b128 v[48:51], v1 offset:5120\n"
    "s_waitcnt lgkmcnt(3)\n"
    "v_mfma_f32_32x32x16_bf16 v[80:95], v[56:59], v[132:135], v[80:95]\n"
    "buffer_store_dwordx4 v[36:39], v14, s[24:27], s55 offen sc1\n"
    "ds_read_b128 v[52:55], v1 offset:6144\n"
    "s_nop 0\n"
    "v_cvt_pk_bf16_f32 v112, v64, v65\n"
    "v_cvt_pk_bf16_f32 v113, v66, v67\n"
    "v_cvt_pk_bf16_f32 v114, v68, v69\n"
    "v_cvt_pk_bf16_f32 v115, v70, v71\n"
    "v_cvt_pk_bf16_f32 v116, v72, v73\n"
    "v_mfma_f32_32x32x16_bf16 v[80:95], v[60:63], v[136:139], v[80:95]\n"
    "buffer_load_dwordx4 a[208:211], v0, s[24:27], s73 offen sc1\n"
    "ds_read_b128 v[56:59], v1 offset:7168\n"
    "v_cvt_pk_bf16_f32 v117, v74, v75\n"
    "v_cvt_pk_bf16_f32 v118, v76, v77\n"
    "v_cvt_pk_bf16_f32 v119, v78, v79\n"
    "s_waitcnt lgkmcnt(3)\n"
    "v_mfma_f32_32x32x16_bf16 v[80:95], v[40:43], v[140:143], v[80:95]\n"
    "buffer_store_dwordx4 v[36:39], v0, s[24:27], s57 offen sc1\n"
    "ds_read_b128 v[60:63], v1 offset:8192\n"
    "v_mfma_f32_32x32x16_bf16 v[96:111], a[240:243], v[112:115], 0\n"
    "v_mfma_f32_32x32x16_bf16 v[96:111], a[244:247], v[116:119], v[96:111]\n"
    "v_mfma_f32_32x32x16_bf16 v[80:95], v[44:47], v[144:147], v[80:95]\n"
    "s_add_u32 s46, s72, 0x1000\n"
    "buffer_load_dwordx4 a[180:183], v0, s[24:27], s46 offen sc1\n"
    "ds_read_b128 v[40:43], v1 offset:9216\n"
    "s_waitcnt lgkmcnt(3)\n"
    "v_mfma_f32_32x32x16_bf16 v[80:95], v[48:51], v[148:151], v[80:95]\n"
    "s_add_u32 s44, s57, 0x1000\n"
    "buffer_store_dwordx4 v[36:39], v0, s[24:27], s44 offen sc1\n"
    "ds_read_b128 v[44:47], v1 offset:10240\n"
    "v_mfma_f32_32x32x16_bf16 v[80:95], v[52:55], v[152:155], v[80:95]\n"
    "s_add_u32 s46, s73, 0x1000\n"
    "buffer_load_dwordx4 a[212:215], v0, s[24:27], s46 offen sc1\n"
    "ds_read_b128 v[48:51], v1 offset:11264\n"
    "v_max3_f32 v24, v96, v97, v98\n"
    "v_min3_f32 v25, v96, v97, v98\n"
    "v_max3_f32 v24, v24, v99, v100\n"
    "s_waitcnt lgkmcnt(3)\n"
    "v_mfma_f32_32x32x16_bf16 v[80:95], v[56:59], v[156:159], v[80:95]\n"
    "ds_read_b128 v[52:55], v1 offset:12288\n"
    "v_min3_f32 v25, v25, v99, v100\n"
    "v_max3_f32 v24, v24, v101, v102\n"
    "v_min3_f32 v25, v25, v101, v102\n"
    "v_max3_f32 v24, v24, v103, v104\n"
    "v_min3_f32 v25, v25, v103, v104\n"
    "v_max3_f32 v24, v24, v105, v106\n"
    "v_min3_f32 v25, v25, v105, v106\n"
    "v_mfma_f32_32x32x16_bf16 v[80:95], v[60:63], v[160:163], v[80:95]\n"
    "s_add_u32 s46, s72, 0x2000\n"
    "buffer_load_dwordx4 a[184:187], v0, s[24:27], s46 offen sc1\n"
    "ds_read_b128 v[56:59], v1 offset:13312\n"
    "v_max3_f32 v24, v24, v107, v108\n"
    "v_min3_f32 v25, v25, v107, v108\n"
    "v_max3_f32 v24, v24, v109, v110\n"
    "s_waitcnt lgkmcnt(3)\n"
    "v_mfma_f32_32x32x16_bf16 v[80:95], v[40:43], v[164:167], v[80:95]\n"
    "ds_read_b128 v[60:63], v1 offset:14336\n"
    "v_min3_f32 v25, v25, v109, v110\n"
    "v_max_f32 v24, v24, v111\n"
    "v_min_f32 v25, v25, v111\n"
    "v_mul_f32 v26, v17, v24\n"
    "v_mul_f32 v28, v17, v25\n"
    "v_fma_f32 v64, v96, v17, -v26\n"
    "v_fma_f32 v65, v97, v17, -v26\n"
    "v_mfma_f32_32x32x16_bf16 v[80:95], v[44:47], v[168:171], v[80:95]\n"
    "s_add_u32 s46, s73, 0x2000\n"
    "buffer_load_dwordx4 a[216:219], v0, s[24:27], s46 offen sc1\n"
    "ds_read_b128 v[40:43], v1 offset:15360\n"
    "v_exp_f32 v64, v64\n"
    "v_fma_f32 v66, v98, v17, -v26\n"
    "s_waitcnt lgkmcnt(3)\n"
    "v_mfma_f32_32x32x16_bf16 v[80:95], v[48:51], v[172:175], v[80:95]\n"
    "ds_read_b128 v[44:47], v1 offset:16384\n"
    "v_exp_f32 v65, v65\n"
    "v_fma_f32 v67, v99, v17, -v26\n"
    "v_exp_f32 v66, v66\n"
    "v_fma_f32 v68, v100, v17, -v26\n"
    "v_mfma_f32_32x32x16_bf16 v[80:95], v[52:55], v[176:179], v[80:95]\n"
    "s_add_u32 s46, s72, 0x3000\n"
    "buffer_load_dwordx4 a[188:191], v0, s[24:27], s46 offen sc1\n"
    "ds_read_b128 v[48:51], v1 offset:17408\n"
    "v_exp_f32 v67, v67\n"
    "v_fma_f32 v69, v101, v17, -v26\n"
    "s_waitcnt lgkmcnt(3)\n"
    "v_mfma_f32_32x32x16_bf16 v[80:95], v[56:59], v[180:183], v[80:95]\n"
    "ds_read_b128 v[52:55], v1 offset:18432\n"
    "v_exp_f32 v68, v68\n"
    "v_fma_f32 v70, v102, v17, -v26\n"
    "v_exp_f32 v69, v69\n"
    "v_fma_f32 v71, v103, v17, -v26\n"
    "v_mfma_f32_32x32x16_bf16 v[80:95], v[60:63], v[184:187], v[80:95]\n"
    "s_add_u32 s46, s73, 0x3000\n"
    "buffer_load_dwordx4 a[220:223], v0, s[24:27], s46 offen sc1\n"
    "ds_read_b128 v[56:59], v1 offset:19456\n"
    "v_exp_f32 v70, v70\n"
    "v_fma_f32 v72, v104, v17, -v26\n"
    "s_waitcnt lgkmcnt(3)\n"
    "v_mfma_f32_32x32x16_bf16 v[80:95], v[40:43], v[188:191], v[80:95]\n"
    "ds_read_b128 v[60:63], v1 offset:20480\n"
    "v_exp_f32 v71, v71\n"
    "v_fma_f32 v73, v105, v17, -v26\n"
    "v_exp_f32 v72, v72\n"
    "v_fma_f32 v74, v106, v17, -v26\n"
    "v_mfma_f32_32x32x16_bf16 v[80:95], v[44:47], v[192:195], v[80:95]\n"
    "s_add_u32 s46, s72, 0x4000\n"
    "buffer_load_dwordx4 a[192:195], v0, s[24:27], s46 offen sc1\n"
    "ds_read_b128 v[40:43], v1 offset:21504\n"
    "v_exp_f32 v73, v73\n"
    "v_fma_f32 v75, v107, v17, -v26\n"
    "s_waitcnt lgkmcnt(3)\n"
    "v_mfma_f32_32x32x16_bf16 v[80:95], v[48:51], v[196:199], v[80:95]\n"
    "ds_read_b128 v[44:47], v1 offset:22528\n"
    "v_exp_f32 v74, v74\n"
    "v_fma_f32 v76, v108, v17, -v26\n"
    "v_exp_f32 v75, v75\n"
    "v_fma_f32 v77, v109, v17, -v26\n"
    "v_mfma_f32_32x32x16_bf16 v[80:95], v[52:55], v[200:203], v[80:95]\n"
    "s_add_u32 s46, s73, 0x4000\n"
    "buffer_load_dwordx4 a[224:227], v0, s[24:27], s46 offen sc1\n"
    "ds_read_b128 v[48:51], v1 offset:23552\n"
    "v_exp_f32 v76, v76\n"
    "v_fma_f32 v78, v110, v17, -v26\n"
    "s_waitcnt lgkmcnt(3)\n"
    "v_mfma_f32_32x32x16_bf16 v[80:95], v[56:59], v[204:207], v[80:95]\n"
    "ds_read_b128 v[52:55], v1 offset:24576\n"
    "v_exp_f32 v77, v77\n"
    "v_fma_f32 v79, v111, v17, -v26\n"
    "v_exp_f32 v78, v78\n"
    "v_exp_f32 v79, v79\n"
    "v_mfma_f32_32x32x16_bf16 v[80:95], v[60:63], v[208:211], v[80:95]\n"
    "s_add_u32 s46, s72, 0x5000\n"
    "buffer_load_dwordx4 a[196:199], v0, s[24:27], s46 offen sc1\n"
    "ds_read_b128 v[56:59], v1 offset:25600\n"
    "v_fma_f32 v112, -v96, v17, v28\n"
    "v_fma_f32 v113, -v97, v17, v28\n"
    "s_waitcnt lgkmcnt(3)\n"
    "v_mfma_f32_32x32x16_bf16 v[80:95], v[40:43], v[212:215], v[80:95]\n"
    "ds_read_b128 v[60:63], v1 offset:26624\n"
    "v_exp_f32 v112, v112\n"
    "v_fma_f32 v114, -v98, v17, v28\n"
    "v_exp_f32 v113, v113\n"
    "v_fma_f32 v115, -v99, v17, v28\n"
    "v_mfma_f32_32x32x16_bf16 v[80:95], v[44:47], v[216:219], v[80:95]\n"
    "s_add_u32 s46, s73, 0x5000\n"
    "buffer_load_dwordx4 a[228:231], v0, s[24:27], s46 offen sc1\n"
    "ds_read_b128 v[40:43], v1 offset:27648\n"
    "v_exp_f32 v114, v114\n"
    "v_fma_f32 v116, -v100, v17, v28\n"
    "s_waitcnt lgkmcnt(3)\n"
    "v_mfma_f32_32x32x16_bf16 v[80:95], v[48:51], v[220:223], v[80:95]\n"
    "ds_read_b128 v[44:47], v1 offset:28672\n"
    "v_exp_f32 v115, v115\n"
    "v_fma_f32 v117, -v101, v17, v28\n"
    "v_exp_f32 v116, v116\n"
    "v_fma_f32 v118, -v102, v17, v28\n"
    "v_mfma_f32_32x32x16_bf16 v[80:95], v[52:55], v[224:227], v[80:95]\n"
    "s_add_u32 s46, s72, 0x6000\n"
    "buffer_load_dwordx4 a[200:203], v0, s[24:27], s46 offen sc1\n"
    "ds_read_b128 v[48:51], v1 offset:29696\n"
    "v_exp_f32 v117, v117\n"
    "v_fma_f32 v119, -v103, v17, v28\n"
    "s_waitcnt lgkmcnt(3)\n"
    "v_mfma_f32_32x32x16_bf16 v[80:95], v[56:59], v[228:231], v[80:95]\n"
    "ds_read_b128 v[52:55], v1 offset:30720\n"
    "v_exp_f32 v118, v118\n"
    "v_fma_f32 v120, -v104, v17, v28\n"
    "v_exp_f32 v119, v119\n"
    "v_fma_f32 v121, -v105, v17, v28\n"
    "v_mfma_f32_32x32x16_bf16 v[80:95], v[60:63], v[232:235], v[80:95]\n"
    "s_add_u32 s46, s73, 0x6000\n"
    "buffer_load_dwordx4 a[232:235], v0, s[24:27], s46 offen sc1\n"
    "ds_read_b128 v[56:59], v1 offset:31744\n"
    "v_exp_f32 v120, v120\n"
    "v_fma_f32 v122, -v106, v17, v28\n"
    "s_waitcnt lgkmcnt(3)\n"
    "v_mfma_f32_32x32x16_bf16 v[80:95], v[40:43], v[236:239], v[80:95]\n"
    "ds_read_b128 v[60:63], v4 offset:1024\n"
    "v_exp_f32 v121, v121\n"
    "v_fma_f32 v123, -v107, v17, v28\n"
    "v_exp_f32 v122, v122\n"
    "v_fma_f32 v124, -v108, v17, v28\n"
    "v_mfma_f32_32x32x16_bf16 v[80:95], v[44:47], v[240:243], v[80:95]\n"
    "v_exp_f32 v123, v123\n"
    "v_fma_f32 v125, -v109, v17, v28\n"
    "v_exp_f32 v124, v124\n"
    "v_fma_f32 v126, -v110, v17, v28\n"
    "s_waitcnt lgkmcnt(2)\n"
    "v_mfma_f32_32x32x16_bf16 v[80:95], v[48:51], v[244:247], v[80:95]\n"
    "v_exp_f32 v125, v125\n"
    "v_fma_f32 v127, -v111, v17, v28\n"
    "v_exp_f32 v126, v126\n"
    "v_exp_f32 v127, v127\n"
    "v_cmp_ne_u64 vcc, s[32:33], v[34:35]\n"
    "v_mfma_f32_32x32x16_bf16 v[80:95], v[52:55], v[248:251], v[80:95]\n"
    "s_cbranch_vccnz Lp111_%=\n"
    "Lo112_%=:\n"
    "s_waitcnt lgkmcnt(0)\n"
    "v_mfma_f32_32x32x16_bf16 v[80:95], v[56:59], v[252:255], v[80:95]\n"
    "v_mfma_f32_32x32x16_bf16 v[80:95], v[60:63], v[20:23], v[80:95]\n"
    "v_and_b32 v15, 0x180, v0\n"
    "s_mov_b32 m0, 162528\n"
    "s_nop 0\n"
    "buffer_load_dword v15, s[28:31], s87 offen lds\n"
    "s_add_u32 s44, s60, 0x2000\n"
    "buffer_store_dwordx4 v[36:39], v0, s[24:27], s44 offen sc1\n"
    "s_nop 5\n"
    "v_pk_mul_f32 v[80:81], v[80:81], v[16:17] op_sel_hi:[1,0]\n"
    "v_pk_mul_f32 v[82:83], v[82:83], v[16:17] op_sel_hi:[1,0]\n"
    "v_pk_mul_f32 v[84:85], v[84:85], v[16:17] op_sel_hi:[1,0]\n"
    "v_pk_mul_f32 v[86:87], v[86:87], v[16:17] op_sel_hi:[1,0]\n"
    "v_pk_mul_f32 v[88:89], v[88:89], v[16:17] op_sel_hi:[1,0]\n"
    "v_pk_mul_f32 v[90:91], v[90:91], v[16:17] op_sel_hi:[1,0]\n"
    "v_pk_mul_f32 v[92:93], v[92:93], v[16:17] op_sel_hi:[1,0]\n"
    "s_add_u32 s46, s72, 0x7000\n"
    "buffer_load_dwordx4 a[204:207], v0, s[24:27], s46 offen sc1\n"
    "v_pk_mul_f32 v[94:95], v[94:95], v[16:17] op_sel_hi:[1,0]\n"
    "v_cvt_pk_bf16_f32 v80, v80, v81\n"
    "v_cvt_pk_bf16_f32 v81, v82, v83\n"
    "v_cvt_pk_bf16_f32 v82, v84, v85\n"
    "v_cvt_pk_bf16_f32 v83, v86, v87\n"
    "v_cvt_pk_bf16_f32 v84, v88, v89\n"
    "s_add_u32 s46, s73, 0x7000\n"
    "buffer_load_dwordx4 a[236:239], v0, s[24:27], s46 offen sc1\n"
    "v_cvt_pk_bf16_f32 v85, v90, v91\n"
    "v_cvt_pk_bf16_f32 v86, v92, v93\n"
    "v_cvt_pk_bf16_f32 v87, v94, v95\n"
    "ds_write_b128 v6, v[80:83] offset:128\n"
    "ds_write_b128 v6, v[84:87] offset:144\n"
    "v_pk_add_f32 v[24:25], v[64:65], v[66:67]\n"
    "buffer_load_dwordx4 a[0:3], v0, s[24:27], s84 offen sc1\n"
    "v_pk_add_f32 v[26:27], v[68:69], v[70:71]\n"
    "v_pk_add_f32 v[24:25], v[24:25], v[72:73]\n"
    "v_pk_add_f32 v[26:27], v[26:27], v[74:75]\n"
    "v_pk_add_f32 v[24:25], v[24:25], v[76:77]\n"
    "v_pk_add_f32 v[26:27], v[26:27], v[78:79]\n"
    "v_pk_add_f32 v[24:25], v[24:25], v[26:27]\n"
    "buffer_load_dwordx4 a[4:7], v0, s[24:27], s84 offen offset:1024 sc1\n"
    "v_add_f32 v24, v24, v25\n"
    "v_rcp_f32 v24, v24\n"
    "s_nop 0\n"
    "v_pk_mul_f32 v[64:65], v[64:65], v[24:25] op_sel_hi:[1,0]\n"
    "v_pk_mul_f32 v[66:67], v[66:67], v[24:25] op_sel_hi:[1,0]\n"
    "v_pk_mul_f32 v[68:69], v[68:69], v[24:25] op_sel_hi:[1,0]\n"
    "v_pk_mul_f32 v[70:71], v[70:71], v[24:25] op_sel_hi:[1,0]\n"
    "v_cvt_pk_bf16_f32 v96, v64, v65\n"
    "s_add_u32 s44, s60, 0x3000\n"
    "buffer_store_dwordx4 v[36:39], v0, s[24:27], s44 offen sc1\n"
    "v_cvt_pk_bf16_f32 v97, v66, v67\n"
    "v_cvt_pk_bf16_f32 v98, v68, v69\n"
    "v_cvt_pk_bf16_f32 v99, v70, v71\n"
    "ds_write_b128 v5, v[96:99]\n"
    "v_pk_mul_f32 v[72:73], v[72:73], v[24:25] op_sel_hi:[1,0]\n"
    "v_pk_mul_f32 v[74:75], v[74:75], v[24:25] op_sel_hi:[1,0]\n"
    "buffer_load_dwordx4 a[8:11], v0, s[24:27], s84 offen offset:2048 sc1\n"
    "v_pk_mul_f32 v[76:77], v[76:77], v[24:25] op_sel_hi:[1,0]\n"
    "v_pk_mul_f32 v[78:79], v[78:79], v[24:25] op_sel_hi:[1,0]\n"
    "v_cvt_pk_bf16_f32 v100, v72, v73\n"
    "v_cvt_pk_bf16_f32 v101, v74, v75\n"
    "v_cvt_pk_bf16_f32 v102, v76, v77\n"
    "v_cvt_pk_bf16_f32 v103, v78, v79\n"
    "buffer_load_dwordx4 a[12:15], v0, s[24:27], s84 offen offset:3072 sc1\n"
    "ds_write_b128 v5, v[100:103] offset:16\n"
    "v_pk_add_f32 v[30:31], v[112:113], v[114:115]\n"
    "v_pk_add_f32 v[26:27], v[116:117], v[118:119]\n"
    "v_pk_add_f32 v[30:31], v[30:31], v[120:121]\n"
    "v_pk_add_f32 v[26:27], v[26:27], v[122:123]\n"
    "v_pk_add_f32 v[30:31], v[30:31], v[124:125]\n"
    "buffer_load_dwordx4 a[88:91], v0, s[24:27], s85 offen sc1\n"
    "v_pk_add_f32 v[26:27], v[26:27], v[126:127]\n"
    "v_pk_add_f32 v[30:31], v[30:31], v[26:27]\n"
    "v_add_f32 v30, v30, v31\n"
    "v_rcp_f32 v30, v30\n"
    "s_nop 0\n"
    "v_pk_mul_f32 v[112:113], v[112:113], v[30:31] op_sel_hi:[1,0]\n"
    "v_pk_mul_f32 v[114:115], v[114:115], v[30:31] op_sel_hi:[1,0]\n"
    "buffer_load_dwordx4 a[92:95], v0, s[24:27], s85 offen offset:1024 sc1\n"
    "v_pk_mul_f32 v[116:117], v[116:117], v[30:31] op_sel_hi:[1,0]\n"
    "v_pk_mul_f32 v[118:119], v[118:119], v[30:31] op_sel_hi:[1,0]\n"
    "v_cvt_pk_bf16_f32 v104, v112, v113\n"
    "v_cvt_pk_bf16_f32 v105, v114, v115\n"
    "v_cvt_pk_bf16_f32 v106, v116, v117\n"
    "v_cvt_pk_bf16_f32 v107, v118, v119\n"
    "ds_write_b128 v5, v[104:107] offset:32\n"
    "buffer_load_dwordx4 a[96:99], v0, s[24:27], s85 offen offset:2048 sc1\n"
    "v_pk_mul_f32 v[120:121], v[120:121], v[30:31] op_sel_hi:[1,0]\n"
    "v_pk_mul_f32 v[122:123], v[122:123], v[30:31] op_sel_hi:[1,0]\n"
    "v_pk_mul_f32 v[124:125], v[124:125], v[30:31] op_sel_hi:[1,0]\n"
    "v_pk_mul_f32 v[126:127], v[126:127], v[30:31] op_sel_hi:[1,0]\n"
    "v_cvt_pk_bf16_f32 v108, v120, v121\n"
    "v_cvt_pk_bf16_f32 v109, v122, v123\n"
    "buffer_load_dwordx4 a[100:103], v0, s[24:27], s85 offen offset:3072 sc1\n"
    "v_cvt_pk_bf16_f32 v110, v124, v125\n"
    "v_cvt_pk_bf16_f32 v111, v126, v127\n"
    "ds_write_b128 v5, v[108:111] offset:48\n"
    "v_mov_b32 v24, 0x3f803f80\n"
    "v_mov_b32 v25, 0x3f803f80\n"
    "v_mov_b32 v26, 0x3f803f80\n"
    "v_mov_b32 v27, 0x3f803f80\n"
    "s_waitcnt vmcnt(28)\n"
    "s_waitcnt lgkmcnt(0)\n"
    "s_barrier\n"
    "ds_read_b64_tr_b16 v[64:65], v7\n"
    "ds_read_b64_tr_b16 v[66:67], v7 offset:832\n"
    "ds_read_b64_tr_b16 v[68:69], v8\n"
    "ds_read_b64_tr_b16 v[70:71], v8 offset:832\n"
    "ds_read_b64_tr_b16 v[72:73], v7 offset:6656\n"
    "ds_read_b64_tr_b16 v[74:75], v7 offset:7488\n"
    "ds_read_b64_tr_b16 v[76:77], v8 offset:6656\n"
    "ds_read_b64_tr_b16 v[78:79], v8 offset:7488\n"
    "ds_read_b64_tr_b16 v[80:81], v7 offset:13312\n"
    "ds_read_b64_tr_b16 v[82:83], v7 offset:14144\n"
    "ds_read_b64_tr_b16 v[84:85], v8 offset:13312\n"
    "ds_read_b64_tr_b16 v[86:87], v8 offset:14144\n"
    "ds_read_b64_tr_b16 v[88:89], v7 offset:19968\n"
    "ds_read_b64_tr_b16 v[90:91], v7 offset:20800\n"
    "ds_read_b64_tr_b16 v[92:93], v8 offset:19968\n"
    "ds_read_b64_tr_b16 v[94:95], v8 offset:20800\n"
    "v_lshlrev_b32 v33, 7, v0\n"
    "v_mov_b32 v34, 1\n"
    "s_mov_b64 exec, s[18:19]\n"
    "buffer_atomic_add v34, v33, s[24:27], s89 offen sc1\n"
    "s_mov_b64 exec, -1\n"
    "s_waitcnt lgkmcnt(12)\n"
    "v_mfma_f32_16x16x32_bf16 v[112:115], v[64:67], v[68:71], 0\n"
    "v_mfma_f32_16x16x32_bf16 v[116:119], v[64:67], v[24:27], 0\n"
    "s_waitcnt lgkmcnt(8)\n"
    "v_mfma_f32_16x16x32_bf16 v[112:115], v[72:75], v[76:79], v[112:115]\n"
    "v_mfma_f32_16x16x32_bf16 v[116:119], v[72:75], v[24:27], v[116:119]\n"
    "s_waitcnt lgkmcnt(4)\n"
    "v_mfma_f32_16x16x32_bf16 v[112:115], v[80:83], v[84:87], v[112:115]\n"
    "v_mfma_f32_16x16x32_bf16 v[116:119], v[80:83], v[24:27], v[116:119]\n"
    "s_waitcnt lgkmcnt(0)\n"
    "v_mfma_f32_16x16x32_bf16 v[112:115], v[88:91], v[92:95], v[112:115]\n"
    "v_mfma_f32_16x16x32_bf16 v[116:119], v[88:91], v[24:27], v[116:119]\n"
    "ds_read_b128 v[40:43], v0 offset:32768\n"
    "ds_read_b128 v[44:47], v0 offset:33792\n"
    "ds_read_b128 v[48:51], v0 offset:34816\n"
    "ds_read_b128 v[52:55], v0 offset:35840\n"
    "ds_read_b128 v[56:59], v0 offset:36864\n"
    "s_nop 1\n"
    "v_cvt_pk_bf16_f32 v112, v112, v113\n"
    "v_cvt_pk_bf16_f32 v113, v114, v115\n"
    "buffer_store_dwordx2 v[112:113], v18, s[24:27], s54 offen sc1\n"
    "buffer_store_dwordx4 v[116:119], v14, s[24:27], s55 offen sc1\n"
    "s_waitcnt lgkmcnt(3)\n"
    "v_mfma_f32_32x32x16_bf16 v[96:111], v[40:43], v[128:131], 0\n"
    "ds_read_b128 v[60:63], v0 offset:37888\n"
    "v_and_b32 v32, 0x3c0, v0\n"
    "v_add_u32 v32, v32, v0\n"
    "v_mfma_f32_32x32x16_bf16 v[96:111], v[44:47], v[132:135], v[96:111]\n"
    "s_add_u32 m0, s78, 0x0\n"
    "s_add_u32 s47, s68, 0x41000\n"
    "buffer_load_dwordx4 v0, s[24:27], s47 offen sc1 lds\n"
    "ds_read_b128 v[40:43], v0 offset:38912\n"
    "s_waitcnt lgkmcnt(3)\n"
    "v_mfma_f32_32x32x16_bf16 v[96:111], v[48:51], v[136:139], v[96:111]\n"
    "ds_read_b128 v[44:47], v0 offset:39936\n"
    "v_mfma_f32_32x32x16_bf16 v[96:111], v[52:55], v[140:143], v[96:111]\n"
    "buffer_load_dwordx4 v0, s[24:27], s47 offen offset:1024 sc1 lds\n"
    "ds_read_b128 v[48:51], v0 offset:40960\n"
    "s_waitcnt lgkmcnt(3)\n"
    "v_mfma_f32_32x32x16_bf16 v[96:111], v[56:59], v[144:147], v[96:111]\n"
    "ds_read_b128 v[52:55], v0 offset:41984\n"
    "v_mfma_f32_32x32x16_bf16 v[96:111], v[60:63], v[148:151], v[96:111]\n"
    "buffer_load_dwordx4 v0, s[24:27], s47 offen offset:2048 sc1 lds\n"
    "ds_read_b128 v[56:59], v0 offset:43008\n"
    "s_waitcnt lgkmcnt(3)\n"
    "v_mfma_f32_32x32x16_bf16 v[96:111], v[40:43], v[152:155], v[96:111]\n"
    "ds_read_b128 v[60:63], v0 offset:44032\n"
    "v_mfma_f32_32x32x16_bf16 v[96:111], v[44:47], v[156:159], v[96:111]\n"
    "buffer_load_dwordx4 v0, s[24:27], s47 offen offset:3072 sc1 lds\n"
    "ds_read_b128 v[40:43], v0 offset:45056\n"
    "s_waitcnt lgkmcnt(3)\n"
    "v_mfma_f32_32x32x16_bf16 v[96:111], v[48:51], v[160:163], v[96:111]\n"
    "ds_read_b128 v[44:47], v0 offset:46080\n"
    "v_mfma_f32_32x32x16_bf16 v[96:111], v[52:55], v[164:167], v[96:111]\n"
    "s_add_u32 m0, s78, 0x1000\n"
    "s_add_u32 s47, s69, 0x41000\n"
    "buffer_load_dwordx4 v0, s[24:27], s47 offen sc1 lds\n"
    "ds_read_b128 v[48:51], v0 offset:47104\n"
    "s_waitcnt lgkmcnt(3)\n"
    "v_mfma_f32_32x32x16_bf16 v[96:111], v[56:59], v[168:171], v[96:111]\n"
    "ds_read_b128 v[52:55], v0 offset:48128\n"
    "v_mfma_f32_32x32x16_bf16 v[96:111], v[60:63], v[172:175], v[96:111]\n"
    "buffer_load_dwordx4 v0, s[24:27], s47 offen offset:1024 sc1 lds\n"
    "ds_read_b128 v[56:59], v0 offset:49152\n"
    "s_waitcnt lgkmcnt(3)\n"
    "v_mfma_f32_32x32x16_bf16 v[96:111], v[40:43], v[176:179], v[96:111]\n"
    "ds_read_b128 v[60:63], v0 offset:50176\n"
    "v_and_b32 v168, 0x300, v0\n"
    "v_add_u32 v168, v168, v0\n"
    "v_bfe_u32 v169, v0, 5, 3\n"
    "v_and_b32 v170, 16, v0\n"
    "v_mov_b32 v171, 0x3f80\n"
    "v_mfma_f32_32x32x16_bf16 v[96:111], v[44:47], v[180:183], v[96:111]\n"
    "buffer_load_dwordx4 v0, s[24:27], s47 offen offset:2048 sc1 lds\n"
    "ds_read_b128 v[40:43], v0 offset:51200\n"
    "v_lshlrev_b32 v170, v170, v171\n"
    "s_waitcnt lgkmcnt(3)\n"
    "v_mfma_f32_32x32x16_bf16 v[96:111], v[48:51], v[184:187], v[96:111]\n"
    "ds_read_b128 v[44:47], v0 offset:52224\n"
    "v_cmp_eq_u32 vcc, 0, v169\n"
    "s_nop 1\n"
    "v_cndmask_b32 v160, 0, v170, vcc\n"
    "v_mfma_f32_32x32x16_bf16 v[96:111], v[52:55], v[188:191], v[96:111]\n"
    "buffer_load_dwordx4 v0, s[24:27], s47 offen offset:3072 sc1 lds\n"
    "ds_read_b128 v[48:51], v0 offset:53248\n"
    "s_waitcnt lgkmcnt(3)\n"
    "v_mfma_f32_32x32x16_bf16 v[96:111], v[56:59], v[192:195], v[96:111]\n"
    "v_mov_b32 v33, 0\n"
    "s_mov_b64 exec, s[16:17]\n"
    "buffer_atomic_add v33, off, s[24:27], s91 sc0 sc1\n"
    "s_mov_b64 exec, -1\n"
    "ds_read_b128 v[52:55], v0 offset:54272\n"
    "v_mfma_f32_32x32x16_bf16 v[96:111], v[60:63], v[196:199], v[96:111]\n"
    "s_add_u32 m0, s86, 0x0\n"
    "s_nop 0\n"
    "buffer_load_dword v3, s[24:27], s47 offen sc1 lds\n"
    "ds_read_b128 v[56:59], v0 offset:55296\n"
    "s_waitcnt lgkmcnt(3)\n"
    "v_mfma_f32_32x32x16_bf16 v[96:111], v[40:43], v[200:203], v[96:111]\n"
    "ds_read_b128 v[60:63], v0 offset:56320\n"
    "v_cmp_eq_u32 vcc, 1, v169\n"
    "s_nop 1\n"
    "v_cndmask_b32 v161, 0, v170, vcc\n"
    "v_mfma_f32_32x32x16_bf16 v[96:111], v[44:47], v[204:207], v[96:111]\n"
    "s_add_u32 s46, s84, 0x1000\n"
    "buffer_load_dwordx4 a[16:19], v0, s[24:27], s46 offen sc1\n"
    "ds_read_b128 v[40:43], v0 offset:57344\n"
    "s_waitcnt lgkmcnt(3)\n"
    "v_mfma_f32_32x32x16_bf16 v[96:111], v[48:51], v[208:211], v[96:111]\n"
    "ds_read_b128 v[44:47], v0 offset:58368\n"
    "v_cmp_eq_u32 vcc, 2, v169\n"
    "s_nop 1\n"
    "v_cndmask_b32 v162, 0, v170, vcc\n"
    "v_mfma_f32_32x32x16_bf16 v[96:111], v[52:55], v[212:215], v[96:111]\n"
    "buffer_load_dwordx4 a[20:23], v0, s[24:27], s46 offen offset:1024 sc1\n"
    "ds_read_b128 v[48:51], v0 offset:59392\n"
    "s_waitcnt lgkmcnt(3)\n"
    "v_mfma_f32_32x32x16_bf16 v[96:111], v[56:59], v[216:219], v[96:111]\n"
    "ds_read_b128 v[52:55], v0 offset:60416\n"
    "v_cmp_eq_u32 vcc, 3, v169\n"
    "s_nop 1\n"
    "v_cndmask_b32 v163, 0, v170, vcc\n"
    "v_mfma_f32_32x32x16_bf16 v[96:111], v[60:63], v[220:223], v[96:111]\n"
    "buffer_load_dwordx4 a[24:27], v0, s[24:27], s46 offen offset:2048 sc1\n"
    "ds_read_b128 v[56:59], v0 offset:61440\n"
    "s_waitcnt lgkmcnt(3)\n"
    "v_mfma_f32_32x32x16_bf16 v[96:111], v[40:43], v[224:227], v[96:111]\n"
    "v_mov_b32 v34, 0\n"
    "s_mov_b64 exec, s[16:17]\n"
    "buffer_atomic_add v34, off, s[24:27], s91 sc0 sc1\n"
    "s_mov_b64 exec, -1\n"
    "ds_read_b128 v[60:63], v0 offset:62464\n"
    "v_mfma_f32_32x32x16_bf16 v[96:111], v[44:47], v[228:231], v[96:111]\n"
    "buffer_load_dwordx4 a[28:31], v0, s[24:27], s46 offen offset:3072 sc1\n"
    "ds_read_b128 v[40:43], v0 offset:63488\n"
    "s_waitcnt lgkmcnt(3)\n"
    "v_mfma_f32_32x32x16_bf16 v[96:111], v[48:51], v[232:235], v[96:111]\n"
    "ds_read_b128 v[44:47], v0 offset:64512\n"
    "v_cmp_eq_u32 vcc, 4, v169\n"
    "s_nop 1\n"
    "v_cndmask_b32 v164, 0, v170, vcc\n"
    "v_mfma_f32_32x32x16_bf16 v[96:111], v[52:55], v[236:239], v[96:111]\n"
    "s_add_u32 s46, s85, 0x1000\n"
    "buffer_load_dwordx4 a[104:107], v0, s[24:27], s46 offen sc1\n"
    "ds_read_b128 v[48:51], v4 offset:512\n"
    "s_waitcnt lgkmcnt(3)\n"
    "v_mfma_f32_32x32x16_bf16 v[96:111], v[56:59], v[240:243], v[96:111]\n"
    "v_cmp_eq_u32 vcc, 5, v169\n"
    "s_nop 1\n"
    "v_cndmask_b32 v165, 0, v170, vcc\n"
    "v_cmp_eq_u32 vcc, 6, v169\n"
    "s_nop 1\n"
    "v_cndmask_b32 v166, 0, v170, vcc\n"
    "v_mfma_f32_32x32x16_bf16 v[96:111], v[60:63], v[244:247], v[96:111]\n"
    "buffer_load_dwordx4 a[108:111], v0, s[24:27], s46 offen offset:1024 sc1\n"
    "v_cmp_eq_u32 vcc, 7, v169\n"
    "s_nop 1\n"
    "v_cndmask_b32 v167, 0, v170, vcc\n"
    "s_waitcnt lgkmcnt(1)\n"
    "v_mfma_f32_32x32x16_bf16 v[96:111], v[40:43], v[248:251], v[96:111]\n"
    "v_mfma_f32_32x32x16_bf16 v[96:111], v[44:47], v[252:255], v[96:111]\n"
    "s_waitcnt lgkmcnt(0)\n"
    "v_mfma_f32_32x32x16_bf16 v[96:111], v[48:51], v[20:23], v[96:111]\n"
    "s_cmp_lg_u32 s37, 0\n"
    "s_cbranch_scc1 Lhs34_%=\n"
    "s_mov_b64 exec, 1\n"
    "v_mov_b32 v35, 0\n"
    "buffer_atomic_add v35, off, s[24:27], s91 sc0 sc1\n"
    "s_waitcnt vmcnt(9)\n"
    "v_cmp_eq_u32 vcc, 16, v33\n"
    "s_cbranch_vccnz Lhd33_%=\n"
    "v_mov_b32 v33, 0\n"
    "buffer_atomic_add v33, off, s[24:27], s91 sc0 sc1\n"
    "s_waitcnt vmcnt(5)\n"
    "v_cmp_eq_u32 vcc, 16, v34\n"
    "s_cbranch_vccnz Lhd33_%=\n"
    "v_mov_b32 v34, 0\n"
    "buffer_atomic_add v34, off, s[24:27], s91 sc0 sc1\n"
    "Lhp32_%=:\n"
    "s_waitcnt vmcnt(2)\n"
    "v_cmp_eq_u32 vcc, 16, v35\n"
    "s_cbranch_vccnz Lhd33_%=\n"
    "v_mov_b32 v35, 0\n"
    "buffer_atomic_add v35, off, s[24:27], s91 sc0 sc1\n"
    "s_waitcnt vmcnt(2)\n"
    "v_cmp_eq_u32 vcc, 16, v33\n"
    "s_cbranch_vccnz Lhd33_%=\n"
    "v_mov_b32 v33, 0\n"
    "buffer_atomic_add v33, off, s[24:27], s91 sc0 sc1\n"
    "s_waitcnt vmcnt(2)\n"
    "v_cmp_eq_u32 vcc, 16, v34\n"
    "s_cbranch_vccnz Lhd33_%=\n"
    "v_mov_b32 v34, 0\n"
    "buffer_atomic_add v34, off, s[24:27], s91 sc0 sc1\n"
    "s_sub_u32 s50, s50, 1\n"
    "s_cbranch_scc0 Lhp32_%=\n"
    "s_branch Lfail_%=\n"
    "Lhd33_%=:\n"
    "s_mov_b64 exec, -1\n"
    "Lhs34_%=:\n"
    "s_barrier\n"
    "s_add_u32 s42, s56, 0x2000\n"
    "buffer_load_dwordx4 v[192:195], v32, s[24:27], s42 offen sc1\n"
    "s_add_u32 s42, s56, s74\n"
    "buffer_load_dwordx4 v[128:131], v168, s[24:27], s42 offen sc1\n"
    "buffer_load_dwordx4 v[132:135], v168, s[24:27], s42 offen offset:256 sc1\n"
    "s_nop 3\n"
    "v_cvt_pk_bf16_f32 v40, v96, v97\n"
    "v_cvt_pk_bf16_f32 v41, v98, v99\n"
    "v_cvt_pk_bf16_f32 v42, v100, v101\n"
    "v_cvt_pk_bf16_f32 v43, v102, v103\n"
    "v_cvt_pk_bf16_f32 v44, v104, v105\n"
    "v_cvt_pk_bf16_f32 v45, v106, v107\n"
    "v_cvt_pk_bf16_f32 v46, v108, v109\n"
    "v_cvt_pk_bf16_f32 v47, v110, v111\n"
    "v_mfma_f32_32x32x16_bf16 v[112:127], a[240:243], v[40:43], 0\n"
    "s_nop 0\n"
    "v_mfma_f32_32x32x16_bf16 v[112:127], a[244:247], v[44:47], v[112:127]\n"
    "s_add_u32 s42, s56, s75\n"
    "buffer_load_dwordx4 v[136:139], v168, s[24:27], s42 offen sc1\n"
    "buffer_load_dwordx4 v[140:143], v168, s[24:27], s42 offen offset:256 sc1\n"
    "s_nop 8\n"
    "v_max3_f32 v24, v112, v113, v114\n"
    "v_min3_f32 v25, v112, v113, v114\n"
    "s_add_u32 s42, s56, s76\n"
    "buffer_load_dwordx4 v[144:147], v168, s[24:27], s42 offen sc1\n"
    "v_max3_f32 v24, v24, v115, v116\n"
    "v_min3_f32 v25, v25, v115, v116\n"
    "v_max3_f32 v24, v24, v117, v118\n"
    "v_min3_f32 v25, v25, v117, v118\n"
    "v_max3_f32 v24, v24, v119, v120\n"
    "v_min3_f32 v25, v25, v119, v120\n"
    "v_max3_f32 v24, v24, v121, v122\n"
    "v_min3_f32 v25, v25, v121, v122\n"
    "v_max3_f32 v24, v24, v123, v124\n"
    "buffer_load_dwordx4 v[148:151], v168, s[24:27], s42 offen offset:256 sc1\n"
    "v_min3_f32 v25, v25, v123, v124\n"
    "v_max3_f32 v24, v24, v125, v126\n"
    "v_min3_f32 v25, v25, v125, v126\n"
    "v_max_f32 v24, v24, v127\n"
    "v_min_f32 v25, v25, v127\n"
    "v_mul_f32 v26, v17, v24\n"
    "v_mul_f32 v28, v17, v25\n"
    "v_pk_fma_f32 v[64:65], v[112:113], v[16:17], v[26:27] op_sel:[0,1,0] op_sel_hi:[1,1,0] neg_lo:[0,0,1] neg_hi:[0,0,1]\n"
    "v_pk_fma_f32 v[80:81], v[112:113], v[16:17], v[28:29] op_sel:[0,1,0] op_sel_hi:[1,1,0] neg_lo:[1,0,0] neg_hi:[1,0,0]\n"
    "s_add_u32 s42, s56, s77\n"
    "buffer_load_dwordx4 v[152:155], v168, s[24:27], s42 offen sc1\n"
    "v_pk_fma_f32 v[66:67], v[114:115], v[16:17], v[26:27] op_sel:[0,1,0] op_sel_hi:[1,1,0] neg_lo:[0,0,1] neg_hi:[0,0,1]\n"
    "v_pk_fma_f32 v[82:83], v[114:115], v[16:17], v[28:29] op_sel:[0,1,0] op_sel_hi:[1,1,0] neg_lo:[1,0,0] neg_hi:[1,0,0]\n"
    "v_pk_fma_f32 v[68:69], v[116:117], v[16:17], v[26:27] op_sel:[0,1,0] op_sel_hi:[1,1,0] neg_lo:[0,0,1] neg_hi:[0,0,1]\n"
    "v_pk_fma_f32 v[84:85], v[116:117], v[16:17], v[28:29] op_sel:[0,1,0] op_sel_hi:[1,1,0] neg_lo:[1,0,0] neg_hi:[1,0,0]\n"
    "v_pk_fma_f32 v[70:71], v[118:119], v[16:17], v[26:27] op_sel:[0,1,0] op_sel_hi:[1,1,0] neg_lo:[0,0,1] neg_hi:[0,0,1]\n"
    "v_pk_fma_f32 v[86:87], v[118:119], v[16:17], v[28:29] op_sel:[0,1,0] op_sel_hi:[1,1,0] neg_lo:[1,0,0] neg_hi:[1,0,0]\n"
    "v_pk_fma_f32 v[72:73], v[120:121], v[16:17], v[26:27] op_sel:[0,1,0] op_sel_hi:[1,1,0] neg_lo:[0,0,1] neg_hi:[0,0,1]\n"
    "v_pk_fma_f32 v[88:89], v[120:121], v[16:17], v[28:29] op_sel:[0,1,0] op_sel_hi:[1,1,0] neg_lo:[1,0,0] neg_hi:[1,0,0]\n"
    "buffer_load_dwordx4 v[156:159], v168, s[24:27], s42 offen offset:256 sc1\n"
    "v_pk_fma_f32 v[74:75], v[122:123], v[16:17], v[26:27] op_sel:[0,1,0] op_sel_hi:[1,1,0] neg_lo:[0,0,1] neg_hi:[0,0,1]\n"
    "v_pk_fma_f32 v[90:91], v[122:123], v[16:17], v[28:29] op_sel:[0,1,0] op_sel_hi:[1,1,0] neg_lo:[1,0,0] neg_hi:[1,0,0]\n"
    "v_pk_fma_f32 v[76:77], v[124:125], v[16:17], v[26:27] op_sel:[0,1,0] op_sel_hi:[1,1,0] neg_lo:[0,0,1] neg_hi:[0,0,1]\n"
    "v_pk_fma_f32 v[92:93], v[124:125], v[16:17], v[28:29] op_sel:[0,1,0] op_sel_hi:[1,1,0] neg_lo:[1,0,0] neg_hi:[1,0,0]\n"
    "v_pk_fma_f32 v[78:79], v[126:127], v[16:17], v[26:27] op_sel:[0,1,0] op_sel_hi:[1,1,0] neg_lo:[0,0,1] neg_hi:[0,0,1]\n"
    "v_pk_fma_f32 v[94:95], v[126:127], v[16:17], v[28:29] op_sel:[0,1,0] op_sel_hi:[1,1,0] neg_lo:[1,0,0] neg_hi:[1,0,0]\n"
    "v_exp_f32 v64, v64\n"
    "v_exp_f32 v65, v65\n"
    "v_exp_f32 v66, v66\n"
    "v_exp_f32 v67, v67\n"
    "v_exp_f32 v68, v68\n"
    "v_exp_f32 v69, v69\n"
    "v_exp_f32 v70, v70\n"
    "v_exp_f32 v71, v71\n"
    "v_exp_f32 v72, v72\n"
    "v_exp_f32 v73, v73\n"
    "v_exp_f32 v74, v74\n"
    "v_exp_f32 v75, v75\n"
    "s_add_u32 m0, s78, 0x8000\n"
    "s_add_u32 s47, s68, 0x0\n"
    "buffer_load_dwordx4 v0, s[24:27], s47 offen sc1 lds\n"
    "v_exp_f32 v76, v76\n"
    "v_exp_f32 v77, v77\n"
    "v_exp_f32 v78, v78\n"
    "v_exp_f32 v79, v79\n"
    "v_pk_add_f32 v[24:25], v[64:65], v[66:67]\n"
    "v_pk_add_f32 v[26:27], v[68:69], v[70:71]\n"
    "v_pk_add_f32 v[24:25], v[24:25], v[72:73]\n"
    "v_pk_add_f32 v[26:27], v[26:27], v[74:75]\n"
    "v_pk_add_f32 v[24:25], v[24:25], v[76:77]\n"
    "v_pk_add_f32 v[26:27], v[26:27], v[78:79]\n"
    "v_pk_add_f32 v[24:25], v[24:25], v[26:27]\n"
    "v_add_f32 v24, v24, v25\n"
    "v_rcp_f32 v24, v24\n"
    "s_nop 0\n"
    "v_pk_mul_f32 v[64:65], v[64:65], v[24:25] op_sel_hi:[1,0]\n"
    "buffer_load_dwordx4 v0, s[24:27], s47 offen offset:1024 sc1 lds\n"
    "v_pk_mul_f32 v[66:67], v[66:67], v[24:25] op_sel_hi:[1,0]\n"
    "v_pk_mul_f32 v[68:69], v[68:69], v[24:25] op_sel_hi:[1,0]\n"
    "v_pk_mul_f32 v[70:71], v[70:71], v[24:25] op_sel_hi:[1,0]\n"
    "v_cvt_pk_bf16_f32 v112, v64, v65\n"
    "v_cvt_pk_bf16_f32 v113, v66, v67\n"
    "v_cvt_pk_bf16_f32 v114, v68, v69\n"
    "v_cvt_pk_bf16_f32 v115, v70, v71\n"
    "v_pk_mul_f32 v[72:73], v[72:73], v[24:25] op_sel_hi:[1,0]\n"
    "v_pk_mul_f32 v[74:75], v[74:75], v[24:25] op_sel_hi:[1,0]\n"
    "v_pk_mul_f32 v[76:77], v[76:77], v[24:25] op_sel_hi:[1,0]\n"
    "v_pk_mul_f32 v[78:79], v[78:79], v[24:25] op_sel_hi:[1,0]\n"
    "v_cvt_pk_bf16_f32 v116, v72, v73\n"
    "v_cvt_pk_bf16_f32 v117, v74, v75\n"
    "v_cvt_pk_bf16_f32 v118, v76, v77\n"
    "buffer_load_dwordx4 v0, s[24:27], s47 offen offset:2048 sc1 lds\n"
    "v_cvt_pk_bf16_f32 v119, v78, v79\n"
    "v_exp_f32 v80, v80\n"
    "v_exp_f32 v81, v81\n"
    "v_exp_f32 v82, v82\n"
    "v_exp_f32 v83, v83\n"
    "v_exp_f32 v84, v84\n"
    "v_exp_f32 v85, v85\n"
    "v_exp_f32 v86, v86\n"
    "v_exp_f32 v87, v87\n"
    "v_exp_f32 v88, v88\n"
    "v_exp_f32 v89, v89\n"
    "v_exp_f32 v90, v90\n"
    "v_exp_f32 v91, v91\n"
    "v_exp_f32 v92, v92\n"
    "buffer_load_dwordx4 v0, s[24:27], s47 offen offset:3072 sc1 lds\n"
    "v_exp_f32 v93, v93\n"
    "v_exp_f32 v94, v94\n"
    "v_exp_f32 v95, v95\n"
    "v_pk_add_f32 v[30:31], v[80:81], v[82:83]\n"
    "v_pk_add_f32 v[26:27], v[84:85], v[86:87]\n"
    "v_pk_add_f32 v[30:31], v[30:31], v[88:89]\n"
    "v_pk_add_f32 v[26:27], v[26:27], v[90:91]\n"
    "v_pk_add_f32 v[30:31], v[30:31], v[92:93]\n"
    "v_pk_add_f32 v[26:27], v[26:27], v[94:95]\n"
    "v_pk_add_f32 v[30:31], v[30:31], v[26:27]\n"
    "v_add_f32 v30, v30, v31\n"
    "v_rcp_f32 v30, v30\n"
    "s_nop 0\n"
    "v_pk_mul_f32 v[80:81], v[80:81], v[30:31] op_sel_hi:[1,0]\n"
    "s_add_u32 m0, s78, 0x9000\n"
    "s_add_u32 s47, s69, 0x0\n"
    "buffer_load_dwordx4 v0, s[24:27], s47 offen sc1 lds\n"
    "v_pk_mul_f32 v[82:83], v[82:83], v[30:31] op_sel_hi:[1,0]\n"
    "v_pk_mul_f32 v[84:85], v[84:85], v[30:31] op_sel_hi:[1,0]\n"
    "v_pk_mul_f32 v[86:87], v[86:87], v[30:31] op_sel_hi:[1,0]\n"
    "v_cvt_pk_bf16_f32 v120, v80, v81\n"
    "v_cvt_pk_bf16_f32 v121, v82, v83\n"
    "v_cvt_pk_bf16_f32 v122, v84, v85\n"
    "v_cvt_pk_bf16_f32 v123, v86, v87\n"
    "v_pk_mul_f32 v[88:89], v[88:89], v[30:31] op_sel_hi:[1,0]\n"
    "v_pk_mul_f32 v[90:91], v[90:91], v[30:31] op_sel_hi:[1,0]\n"
    "v_pk_mul_f32 v[92:93], v[92:93], v[30:31] op_sel_hi:[1,0]\n"
    "v_pk_mul_f32 v[94:95], v[94:95], v[30:31] op_sel_hi:[1,0]\n"
    "v_cvt_pk_bf16_f32 v124, v88, v89\n"
    "v_cvt_pk_bf16_f32 v125, v90, v91\n"
    "v_cvt_pk_bf16_f32 v126, v92, v93\n"
    "buffer_load_dwordx4 v0, s[24:27], s47 offen offset:1024 sc1 lds\n"
    "v_cvt_pk_bf16_f32 v127, v94, v95\n"
    "Lkv35_%=:\n"
    "s_waitcnt vmcnt(14)\n"
    "v_mov_b32 v44, v192\n"
    "v_mov_b32 v45, v193\n"
    "v_mov_b32 v46, v194\n"
    "v_mov_b32 v47, v195\n"
    "s_add_u32 s46, s85, 0x1000\n"
    "buffer_load_dwordx4 a[112:115], v0, s[24:27], s46 offen offset:2048 sc1\n"
    "s_add_u32 s46, s85, 0x1000\n"
    "buffer_load_dwordx4 a[116:119], v0, s[24:27], s46 offen offset:3072 sc1\n"
    "s_waitcnt vmcnt(15)\n"
    "v_mfma_f32_16x16x32_bf16 v[24:27], v[160:163], v[128:131], 0\n"
    "v_permlane32_swap_b32 v44, v45\n"
    "s_waitcnt vmcnt(14)\n"
    "v_mfma_f32_16x16x32_bf16 v[24:27], v[164:167], v[132:135], v[24:27]\n"
    "v_permlane32_swap_b32 v46, v47\n"
    "s_waitcnt vmcnt(13)\n"
    "v_mfma_f32_16x16x32_bf16 v[24:27], v[160:163], v[136:139], v[24:27]\n"
    "v_add_f32 v44, v44, v45\n"
    "s_waitcnt vmcnt(12)\n"
    "v_mfma_f32_16x16x32_bf16 v[24:27], v[164:167], v[140:143], v[24:27]\n"
    "v_add_f32 v46, v46, v47\n"
    "s_waitcnt vmcnt(11)\n"
    "v_mfma_f32_16x16x32_bf16 v[24:27], v[160:163], v[144:147], v[24:27]\n"
    "v_permlane16_swap_b32 v44, v46\n"
    "s_waitcnt vmcnt(10)\n"
    "v_mfma_f32_16x16x32_bf16 v[24:27], v[164:167], v[148:151], v[24:27]\n"
    "v_add_f32 v44, v44, v46\n"
    "s_waitcnt vmcnt(9)\n"
    "v_mfma_f32_16x16x32_bf16 v[24:27], v[160:163], v[152:155], v[24:27]\n"
    "v_add_f32_dpp v45, v44, v44 row_ror:4 row_mask:0xf bank_mask:0xf\n"
    "s_waitcnt vmcnt(8)\n"
    "v_mfma_f32_16x16x32_bf16 v[24:27], v[164:167], v[156:159], v[24:27]\n"
    "v_add_f32_dpp v44, v45, v45 row_ror:8 row_mask:0xf bank_mask:0xf\n"
    "v_cvt_pk_bf16_f32 v45, v44, v44\n"
    "ds_write_b16 v10, v45\n"
    "s_add_u32 s46, s84, 0x2000\n"
    "buffer_load_dwordx4 a[32:35], v0, s[24:27], s46 offen sc1\n"
    "s_add_u32 s46, s84, 0x2000\n"
    "buffer_load_dwordx4 a[36:39], v0, s[24:27], s46 offen offset:1024 sc1\n"
    "s_nop 0\n"
    "v_cvt_pk_bf16_f32 v28, v24, v25\n"
    "v_cvt_pk_bf16_f32 v29, v26, v27\n"
    "v_cmp_u_f32 vcc, v24, v44\n"
    "ds_write_b64 v9, v[28:29]\n"
    "s_waitcnt lgkmcnt(0)\n"
    "s_cbranch_vccnz Lre55_%=\n"
    "Lok56_%=:\n"
    "s_barrier\n"
    "v_lshlrev_b32 v60, 3, v0\n"
    "v_mov_b32 v61, 1\n"
    "s_mov_b64 exec, s[18:19]\n"
    "buffer_atomic_add v61, v60, s[24:27], s88 offen offset:4 sc1\n"
    "s_mov_b64 exec, -1\n"
    "ds_read_b128 v[128:131], v12\n"
    "ds_read_b128 v[132:135], v11\n"
    "ds_read_b128 v[136:139], v12 offset:16\n"
    "ds_read_b128 v[140:143], v11 offset:16\n"
    "ds_read_b128 v[144:147], v12 offset:32\n"
    "ds_read_b128 v[148:151], v11 offset:32\n"
    "ds_read_b128 v[152:155], v12 offset:48\n"
    "ds_read_b128 v[156:159], v11 offset:48\n"
    "s_waitcnt lgkmcnt(7)\n"
    "v_mfma_f32_32x32x16_bf16 v[80:95], v[128:131], v[112:115], 0\n"
    "s_waitcnt lgkmcnt(6)\n"
    "v_mfma_f32_32x32x16_bf16 v[64:79], v[132:135], v[112:115], 0\n"
    "buffer_load_dwordx4 v0, s[24:27], s47 offen offset:2048 sc1 lds\n"
    "s_waitcnt lgkmcnt(5)\n"
    "v_mfma_f32_32x32x16_bf16 v[80:95], v[136:139], v[116:119], v[80:95]\n"
    "s_waitcnt lgkmcnt(4)\n"
    "v_mfma_f32_32x32x16_bf16 v[64:79], v[140:143], v[116:119], v[64:79]\n"
    "buffer_load_dwordx4 v0, s[24:27], s47 offen offset:3072 sc1 lds\n"
    "s_waitcnt lgkmcnt(3)\n"
    "v_mfma_f32_32x32x16_bf16 v[80:95], v[144:147], v[120:123], v[80:95]\n"
    "s_waitcnt lgkmcnt(2)\n"
    "v_mfma_f32_32x32x16_bf16 v[64:79], v[148:151], v[120:123], v[64:79]\n"
    "s_add_u32 m0, s86, 0x200\n"
    "s_nop 0\n"
    "buffer_load_dword v3, s[24:27], s47 offen sc1 lds\n"
    "s_waitcnt lgkmcnt(1)\n"
    "v_mfma_f32_32x32x16_bf16 v[80:95], v[152:155], v[124:127], v[80:95]\n"
    "s_waitcnt lgkmcnt(0)\n"
    "v_mfma_f32_32x32x16_bf16 v[64:79], v[156:159], v[124:127], v[64:79]\n"
    "v_mov_b32 v60, 0\n"
    "s_mov_b64 exec, s[16:17]\n"
    "buffer_atomic_add v60, off, s[24:27], s92 offset:4 sc0 sc1\n"
    "s_mov_b64 exec, -1\n"
    "s_nop 5\n"
    "v_add_f32 v32, 0x358637bd, v80\n"
    "v_rcp_f32 v32, v32\n"
    "s_nop 0\n"
    "v_pk_mul_f32 v[64:65], v[64:65], v[32:33] op_sel_hi:[1,0]\n"
    "v_pk_mul_f32 v[66:67], v[66:67], v[32:33] op_sel_hi:[1,0]\n"
    "v_pk_mul_f32 v[68:69], v[68:69], v[32:33] op_sel_hi:[1,0]\n"
    "v_pk_mul_f32 v[70:71], v[70:71], v[32:33] op_sel_hi:[1,0]\n"
    "v_cvt_pk_bf16_f32 v24, v64, v65\n"
    "v_cvt_pk_bf16_f32 v25, v66, v67\n"
    "v_cvt_pk_bf16_f32 v26, v68, v69\n"
    "v_cvt_pk_bf16_f32 v27, v70, v71\n"
    "buffer_store_dwordx4 v[24:27], v0, s[24:27], s57 offen sc1\n"
    "v_pk_mul_f32 v[72:73], v[72:73], v[32:33] op_sel_hi:[1,0]\n"
    "v_pk_mul_f32 v[74:75], v[74:75], v[32:33] op_sel_hi:[1,0]\n"
    "v_pk_mul_f32 v[76:77], v[76:77], v[32:33] op_sel_hi:[1,0]\n"
    "v_pk_mul_f32 v[78:79], v[78:79], v[32:33] op_sel_hi:[1,0]\n"
    "v_cvt_pk_bf16_f32 v28, v72, v73\n"
    "v_cvt_pk_bf16_f32 v29, v74, v75\n"
    "v_cvt_pk_bf16_f32 v30, v76, v77\n"
    "v_cvt_pk_bf16_f32 v31, v78, v79\n"
    "s_add_u32 s42, s57, 0x1000\n"
    "buffer_store_dwordx4 v[28:31], v0, s[24:27], s42 offen sc1\n"
    "v_mov_b32 v61, 0\n"
    "s_mov_b64 exec, s[16:17]\n"
    "buffer_atomic_add v61, off, s[24:27], s92 offset:4 sc0 sc1\n"
    "s_mov_b64 exec, -1\n"
    "s_add_u32 m0, s78, 0x18000\n"
    "s_add_u32 s47, s68, 0x20800\n"
    "buffer_load_dwordx4 v0, s[24:27], s47 offen sc1 lds\n"
    "buffer_load_dwordx4 v0, s[24:27], s47 offen offset:1024 sc1 lds\n"
    "buffer_load_dwordx4 v0, s[24:27], s47 offen offset:2048 sc1 lds\n"
    "buffer_load_dwordx4 a[40:43], v0, s[24:27], s46 offen offset:2048 sc1\n"
    "buffer_load_dwordx4 a[44:47], v0, s[24:27], s46 offen offset:3072 sc1\n"
    "v_accvgpr_read_b32 v65, a248\n"
    "v_accvgpr_read_b32 v67, a249\n"
    "v_accvgpr_read_b32 v69, a250\n"
    "v_accvgpr_read_b32 v71, a251\n"
    "v_accvgpr_read_b32 v73, a252\n"
    "v_accvgpr_read_b32 v75, a253\n"
    "v_accvgpr_read_b32 v77, a254\n"
    "v_accvgpr_read_b32 v79, a255\n"
    "v_lshlrev_b32 v64, 16, v65\n"
    "v_and_b32 v65, 0xffff0000, v65\n"
    "v_lshlrev_b32 v66, 16, v67\n"
    "v_and_b32 v67, 0xffff0000, v67\n"
    "v_lshlrev_b32 v68, 16, v69\n"
    "v_and_b32 v69, 0xffff0000, v69\n"
    "v_lshlrev_b32 v70, 16, v71\n"
    "v_and_b32 v71, 0xffff0000, v71\n"
    "v_lshlrev_b32 v72, 16, v73\n"
    "v_and_b32 v73, 0xffff0000, v73\n"
    "v_lshlrev_b32 v74, 16, v75\n"
    "v_and_b32 v75, 0xffff0000, v75\n"
    "v_lshlrev_b32 v76, 16, v77\n"
    "v_and_b32 v77, 0xffff0000, v77\n"
    "v_lshlrev_b32 v78, 16, v79\n"
    "v_and_b32 v79, 0xffff0000, v79\n"
    "s_cmp_lg_u32 s37, 0\n"
    "s_cbranch_scc1 Lhs60_%=\n"
    "s_mov_b64 exec, 1\n"
    "v_mov_b32 v62, 0\n"
    "buffer_atomic_add v62, off, s[24:27], s92 offset:4 sc0 sc1\n"
    "s_waitcnt vmcnt(9)\n"
    "v_cmp_eq_u32 vcc, 16, v60\n"
    "s_cbranch_vccnz Lhd59_%=\n"
    "v_mov_b32 v60, 0\n"
    "buffer_atomic_add v60, off, s[24:27], s92 offset:4 sc0 sc1\n"
    "s_waitcnt vmcnt(7)\n"
    "v_cmp_eq_u32 vcc, 16, v61\n"
    "s_cbranch_vccnz Lhd59_%=\n"
    "v_mov_b32 v61, 0\n"
    "buffer_atomic_add v61, off, s[24:27], s92 offset:4 sc0 sc1\n"
    "Lhp58_%=:\n"
    "s_waitcnt vmcnt(2)\n"
    "v_cmp_eq_u32 vcc, 16, v62\n"
    "s_cbranch_vccnz Lhd59_%=\n"
    "v_mov_b32 v62, 0\n"
    "buffer_atomic_add v62, off, s[24:27], s92 offset:4 sc0 sc1\n"
    "s_waitcnt vmcnt(2)\n"
    "v_cmp_eq_u32 vcc, 16, v60\n"
    "s_cbranch_vccnz Lhd59_%=\n"
    "v_mov_b32 v60, 0\n"
    "buffer_atomic_add v60, off, s[24:27], s92 offset:4 sc0 sc1\n"
    "s_waitcnt vmcnt(2)\n"
    "v_cmp_eq_u32 vcc, 16, v61\n"
    "s_cbranch_vccnz Lhd59_%=\n"
    "v_mov_b32 v61, 0\n"
    "buffer_atomic_add v61, off, s[24:27], s92 offset:4 sc0 sc1\n"
    "s_sub_u32 s50, s50, 1\n"
    "s_cbranch_scc0 Lhp58_%=\n"
    "s_branch Lfail_%=\n"
    "Lhd59_%=:\n"
    "s_mov_b64 exec, -1\n"
    "Lhs60_%=:\n"
    "s_barrier\n"
    "buffer_load_dwordx4 v[128:131], v0, s[24:27], s59 offen sc1\n"
    "s_add_u32 s42, s59, 0x1000\n"
    "buffer_load_dwordx4 v[132:135], v0, s[24:27], s42 offen sc1\n"
    "s_add_u32 s42, s59, 0x2000\n"
    "buffer_load_dwordx4 v[136:139], v0, s[24:27], s42 offen sc1\n"
    "s_add_u32 s42, s59, 0x3000\n"
    "buffer_load_dwordx4 v[140:143], v0, s[24:27], s42 offen sc1\n"
    "s_add_u32 s42, s59, 0x4000\n"
    "buffer_load_dwordx4 v[144:147], v0, s[24:27], s42 offen sc1\n"
    "s_add_u32 s42, s59, 0x5000\n"
    "buffer_load_dwordx4 v[148:151], v0, s[24:27], s42 offen sc1\n"
    "s_add_u32 s42, s59, 0x6000\n"
    "buffer_load_dwordx4 v[152:155], v0, s[24:27], s42 offen sc1\n"
    "s_add_u32 s42, s59, 0x7000\n"
    "buffer_load_dwordx4 v[156:159], v0, s[24:27], s42 offen sc1\n"
    "buffer_load_dwordx4 v[160:163], v0, s[24:27], s58 offen sc1\n"
    "s_add_u32 s42, s58, 0x1000\n"
    "buffer_load_dwordx4 v[164:167], v0, s[24:27], s42 offen sc1\n"
    "s_add_u32 s42, s58, 0x2000\n"
    "buffer_load_dwordx4 v[168:171], v0, s[24:27], s42 offen sc1\n"
    "s_add_u32 s42, s58, 0x3000\n"
    "buffer_load_dwordx4 v[172:175], v0, s[24:27], s42 offen sc1\n"
    "s_waitcnt vmcnt(10)\n"
    "v_max3_u32 v15, v135, v129, v130\n"
    "v_max3_u32 v15, v15, v128, v131\n"
    "v_max3_u32 v15, v15, v132, v133\n"
    "v_max_u32 v15, v15, v134\n"
    "v_cmp_eq_u32 vcc, -1, v15\n"
    "s_nop 0\n"
    "s_cbranch_vccnz Lre78_%=\n"
    "Lok79_%=:\n"
    "v_mfma_f32_32x32x16_bf16 v[112:127], a[208:211], v[128:131], 0\n"
    "v_mfma_f32_32x32x16_bf16 v[96:111], a[176:179], v[128:131], 0\n"
    "s_add_u32 s42, s58, 0x4000\n"
    "buffer_load_dwordx4 v[176:179], v0, s[24:27], s42 offen sc1\n"
    "s_waitcnt vmcnt(10)\n"
    "v_max3_u32 v15, v136, v137, v138\n"
    "v_mfma_f32_32x32x16_bf16 v[112:127], a[212:215], v[132:135], v[112:127]\n"
    "s_waitcnt vmcnt(9)\n"
    "v_max3_u32 v15, v15, v139, v140\n"
    "v_max3_u32 v15, v15, v141, v142\n"
    "v_max_u32 v15, v15, v143\n"
    "v_cmp_eq_u32 vcc, -1, v15\n"
    "v_mfma_f32_32x32x16_bf16 v[96:111], a[180:183], v[132:135], v[96:111]\n"
    "s_add_u32 s42, s58, 0x5000\n"
    "buffer_load_dwordx4 v[180:183], v0, s[24:27], s42 offen sc1\n"
    "s_cbranch_vccnz Lre80_%=\n"
    "Lok81_%=:\n"
    "v_mfma_f32_32x32x16_bf16 v[112:127], a[216:219], v[136:139], v[112:127]\n"
    "v_mfma_f32_32x32x16_bf16 v[96:111], a[184:187], v[136:139], v[96:111]\n"
    "s_add_u32 s42, s58, 0x6000\n"
    "buffer_load_dwordx4 v[184:187], v0, s[24:27], s42 offen sc1\n"
    "s_waitcnt vmcnt(10)\n"
    "v_max3_u32 v15, v144, v145, v146\n"
    "v_mfma_f32_32x32x16_bf16 v[112:127], a[220:223], v[140:143], v[112:127]\n"
    "s_waitcnt vmcnt(9)\n"
    "v_max3_u32 v15, v15, v147, v148\n"
    "v_max3_u32 v15, v15, v149, v150\n"
    "v_max_u32 v15, v15, v151\n"
    "v_cmp_eq_u32 vcc, -1, v15\n"
    "v_mfma_f32_32x32x16_bf16 v[96:111], a[188:191], v[140:143], v[96:111]\n"
    "s_add_u32 s42, s58, 0x7000\n"
    "buffer_load_dwordx4 v[188:191], v0, s[24:27], s42 offen sc1\n"
    "s_cbranch_vccnz Lre82_%=\n"
    "Lok83_%=:\n"
    "v_mfma_f32_32x32x16_bf16 v[112:127], a[224:227], v[144:147], v[112:127]\n"
    "buffer_store_dwordx4 v[36:39], v0, s[24:27], s52 offen sc1\n"
    "v_mfma_f32_32x32x16_bf16 v[96:111], a[192:195], v[144:147], v[96:111]\n"
    "buffer_store_dwordx4 v[36:39], v0, s[24:27], s52 offen offset:1024 sc1\n"
    "s_waitcnt vmcnt(11)\n"
    "v_max3_u32 v15, v152, v153, v154\n"
    "v_mfma_f32_32x32x16_bf16 v[112:127], a[228:231], v[148:151], v[112:127]\n"
    "s_waitcnt vmcnt(10)\n"
    "v_max3_u32 v15, v15, v155, v156\n"
    "v_max3_u32 v15, v15, v157, v158\n"
    "v_max_u32 v15, v15, v159\n"
    "v_cmp_eq_u32 vcc, -1, v15\n"
    "v_mfma_f32_32x32x16_bf16 v[96:111], a[196:199], v[148:151], v[96:111]\n"
    "buffer_store_dword v36, v2, s[24:27], s53 offen sc1\n"
    "s_cbranch_vccnz Lre84_%=\n"
    "Lok85_%=:\n"
    "v_mfma_f32_32x32x16_bf16 v[112:127], a[232:235], v[152:155], v[112:127]\n"
    "v_mfma_f32_32x32x16_bf16 v[96:111], a[200:203], v[152:155], v[96:111]\n"
    "buffer_load_dwordx4 v0, s[24:27], s47 offen offset:3072 sc1 lds\n"
    "s_waitcnt vmcnt(11)\n"
    "v_max3_u32 v15, v160, v161, v162\n"
    "v_mfma_f32_32x32x16_bf16 v[112:127], a[236:239], v[156:159], v[112:127]\n"
    "s_waitcnt vmcnt(10)\n"
    "v_max3_u32 v15, v15, v163, v164\n"
    "v_max3_u32 v15, v15, v165, v166\n"
    "v_max_u32 v15, v15, v167\n"
    "v_cmp_eq_u32 vcc, -1, v15\n"
    "v_mfma_f32_32x32x16_bf16 v[96:111], a[204:207], v[156:159], v[96:111]\n"
    "s_add_u32 m0, s78, 0x19000\n"
    "s_add_u32 s47, s69, 0x20800\n"
    "buffer_load_dwordx4 v0, s[24:27], s47 offen sc1 lds\n"
    "s_cbranch_vccnz Lre86_%=\n"
    "Lok87_%=:\n"
    "v_mfma_f32_32x32x16_bf16 v[80:95], a[208:211], v[160:163], 0\n"
    "v_add_u32 v39, s81, v0\n"
    "v_add_u32 v38, s80, v0\n"
    "v_add_u32 v37, s79, v0\n"
    "v_mfma_f32_32x32x16_bf16 v[64:79], a[176:179], v[160:163], v[64:79]\n"
    "buffer_load_dwordx4 v0, s[24:27], s47 offen offset:1024 sc1 lds\n"
    "s_waitcnt vmcnt(11)\n"
    "v_max3_u32 v15, v168, v169, v170\n"
    "ds_write_b128 v39, v[112:115]\n"
    "v_mfma_f32_32x32x16_bf16 v[80:95], a[212:215], v[164:167], v[80:95]\n"
    "s_waitcnt vmcnt(10)\n"
    "v_max3_u32 v15, v15, v171, v172\n"
    "v_max3_u32 v15, v15, v173, v174\n"
    "v_max_u32 v15, v15, v175\n"
    "v_cmp_eq_u32 vcc, -1, v15\n"
    "ds_write_b128 v39, v[116:119] offset:1024\n"
    "v_mfma_f32_32x32x16_bf16 v[64:79], a[180:183], v[164:167], v[64:79]\n"
    "buffer_load_dwordx4 v0, s[24:27], s47 offen offset:2048 sc1 lds\n"
    "s_cbranch_vccnz Lre88_%=\n"
    "Lok89_%=:\n"
    "v_mfma_f32_32x32x16_bf16 v[80:95], a[216:219], v[168:171], v[80:95]\n"
    "ds_write_b128 v39, v[120:123] offset:2048\n"
    "v_mfma_f32_32x32x16_bf16 v[64:79], a[184:187], v[168:171], v[64:79]\n"
    "buffer_load_dwordx4 v0, s[24:27], s47 offen offset:3072 sc1 lds\n"
    "s_waitcnt vmcnt(11)\n"
    "v_max3_u32 v15, v176, v177, v178\n"
    "v_mfma_f32_32x32x16_bf16 v[80:95], a[220:223], v[172:175], v[80:95]\n"
    "s_waitcnt vmcnt(10)\n"
    "v_max3_u32 v15, v15, v179, v180\n"
    "v_max3_u32 v15, v15, v181, v182\n"
    "v_max_u32 v15, v15, v183\n"
    "v_cmp_eq_u32 vcc, -1, v15\n"
    "ds_write_b128 v39, v[124:127] offset:3072\n"
    "v_mfma_f32_32x32x16_bf16 v[64:79], a[188:191], v[172:175], v[64:79]\n"
    "s_cbranch_vccnz Lre90_%=\n"
    "Lok91_%=:\n"
    "v_mfma_f32_32x32x16_bf16 v[80:95], a[224:227], v[176:179], v[80:95]\n"
    "ds_write_b128 v38, v[96:99]\n"
    "v_mfma_f32_32x32x16_bf16 v[64:79], a[192:195], v[176:179], v[64:79]\n"
    "s_waitcnt vmcnt(9)\n"
    "v_max3_u32 v15, v184, v185, v186\n"
    "ds_write_b128 v38, v[100:103] offset:1024\n"
    "v_mfma_f32_32x32x16_bf16 v[80:95], a[228:231], v[180:183], v[80:95]\n"
    "s_waitcnt vmcnt(8)\n"
    "v_max3_u32 v15, v15, v187, v188\n"
    "v_max3_u32 v15, v15, v189, v190\n"
    "v_max_u32 v15, v15, v191\n"
    "v_cmp_eq_u32 vcc, -1, v15\n"
    "v_mfma_f32_32x32x16_bf16 v[64:79], a[196:199], v[180:183], v[64:79]\n"
    "s_cbranch_vccnz Lre92_%=\n"
    "Lok93_%=:\n"
    "ds_write_b128 v38, v[104:107] offset:2048\n"
    "v_mfma_f32_32x32x16_bf16 v[80:95], a[232:235], v[184:187], v[80:95]\n"
    "v_mfma_f32_32x32x16_bf16 v[64:79], a[200:203], v[184:187], v[64:79]\n"
    "ds_write_b128 v38, v[108:111] offset:3072\n"
    "v_mfma_f32_32x32x16_bf16 v[80:95], a[236:239], v[188:191], v[80:95]\n"
    "v_mfma_f32_32x32x16_bf16 v[64:79], a[204:207], v[188:191], v[64:79]\n"
    "s_nop 10\n"
    "ds_write_b128 v37, v[80:83]\n"
    "ds_write_b128 v37, v[84:87] offset:1024\n"
    "ds_write_b128 v37, v[88:91] offset:2048\n"
    "ds_write_b128 v37, v[92:95] offset:3072\n"
    "s_add_u32 s46, s85, 0x2000\n"
    "buffer_load_dwordx4 a[120:123], v0, s[24:27], s46 offen sc1\n"
    "buffer_load_dwordx4 a[124:127], v0, s[24:27], s46 offen offset:1024 sc1\n"
    "buffer_load_dwordx4 a[128:131], v0, s[24:27], s46 offen offset:2048 sc1\n"
    "buffer_load_dwordx4 a[132:135], v0, s[24:27], s46 offen offset:3072 sc1\n"
    "v_add_u32 v32, s82, v0\n"
    "v_add_u32 v33, s83, v0\n"
    "s_waitcnt vmcnt(9)\n"
    "s_waitcnt lgkmcnt(0)\n"
    "s_barrier\n"
    "v_lshlrev_b32 v60, 3, v0\n"
    "v_mov_b32 v61, 1\n"
    "s_mov_b64 exec, s[18:19]\n"
    "buffer_atomic_add v61, v60, s[24:27], s90 offen offset:8 sc1\n"
    "s_mov_b64 exec, -1\n"
    "ds_read_b128 v[80:83], v32\n"
    "ds_read_b128 v[96:99], v32 offset:4096\n"
    "ds_read_b128 v[112:115], v33\n"
    "ds_read_b128 v[84:87], v32 offset:1024\n"
    "ds_read_b128 v[100:103], v32 offset:5120\n"
    "ds_read_b128 v[116:119], v33 offset:1024\n"
    "ds_read_b128 v[88:91], v32 offset:2048\n"
    "ds_read_b128 v[104:107], v32 offset:6144\n"
    "ds_read_b128 v[120:123], v33 offset:2048\n"
    "ds_read_b128 v[92:95], v32 offset:3072\n"
    "ds_read_b128 v[108:111], v32 offset:7168\n"
    "ds_read_b128 v[124:127], v33 offset:3072\n"
    "s_waitcnt lgkmcnt(9)\n"
    "v_pk_add_f32 v[64:65], v[64:65], v[80:81]\n"
    "v_pk_add_f32 v[66:67], v[66:67], v[82:83]\n"
    "v_pk_add_f32 v[64:65], v[64:65], v[96:97]\n"
    "v_pk_add_f32 v[66:67], v[66:67], v[98:99]\n"
    "v_pk_add_f32 v[64:65], v[64:65], v[112:113]\n"
    "v_pk_add_f32 v[66:67], v[66:67], v[114:115]\n"
    "v_cvt_pk_bf16_f32 v24, v64, v66\n"
    "v_cvt_pk_bf16_f32 v28, v65, v67\n"
    "v_mov_b32 v60, 0\n"
    "s_mov_b64 exec, s[16:17]\n"
    "buffer_atomic_add v60, off, s[24:27], s91 offset:8 sc0 sc1\n"
    "s_mov_b64 exec, -1\n"
    "s_waitcnt lgkmcnt(6)\n"
    "v_pk_add_f32 v[68:69], v[68:69], v[84:85]\n"
    "v_pk_add_f32 v[70:71], v[70:71], v[86:87]\n"
    "v_pk_add_f32 v[68:69], v[68:69], v[100:101]\n"
    "v_pk_add_f32 v[70:71], v[70:71], v[102:103]\n"
    "v_pk_add_f32 v[68:69], v[68:69], v[116:117]\n"
    "v_pk_add_f32 v[70:71], v[70:71], v[118:119]\n"
    "v_cvt_pk_bf16_f32 v25, v68, v70\n"
    "v_cvt_pk_bf16_f32 v29, v69, v71\n"
    "s_waitcnt lgkmcnt(3)\n"
    "v_pk_add_f32 v[72:73], v[72:73], v[88:89]\n"
    "v_pk_add_f32 v[74:75], v[74:75], v[90:91]\n"
    "v_pk_add_f32 v[72:73], v[72:73], v[104:105]\n"
    "v_pk_add_f32 v[74:75], v[74:75], v[106:107]\n"
    "v_pk_add_f32 v[72:73], v[72:73], v[120:121]\n"
    "v_pk_add_f32 v[74:75], v[74:75], v[122:123]\n"
    "v_cvt_pk_bf16_f32 v26, v72, v74\n"
    "v_cvt_pk_bf16_f32 v30, v73, v75\n"
    "s_waitcnt lgkmcnt(0)\n"
    "v_pk_add_f32 v[76:77], v[76:77], v[92:93]\n"
    "v_pk_add_f32 v[78:79], v[78:79], v[94:95]\n"
    "v_pk_add_f32 v[76:77], v[76:77], v[108:109]\n"
    "v_pk_add_f32 v[78:79], v[78:79], v[110:111]\n"
    "v_pk_add_f32 v[76:77], v[76:77], v[124:125]\n"
    "v_pk_add_f32 v[78:79], v[78:79], v[126:127]\n"
    "v_cvt_pk_bf16_f32 v27, v76, v78\n"
    "v_cvt_pk_bf16_f32 v31, v77, v79\n"
    "buffer_store_dwordx4 v[24:27], v0, s[24:27], s52 offen sc1\n"
    "buffer_store_dwordx4 v[28:31], v0, s[24:27], s52 offen offset:1024 sc1\n"
    "v_mov_b32 v61, 0\n"
    "s_mov_b64 exec, s[16:17]\n"
    "buffer_atomic_add v61, off, s[24:27], s91 offset:8 sc0 sc1\n"
    "s_mov_b64 exec, -1\n"
    "s_add_u32 m0, s86, 0x600\n"
    "s_nop 0\n"
    "buffer_load_dword v3, s[24:27], s47 offen sc1 lds\n"
    "v_mov_b32 v32, 0\n"
    "v_mov_b32 v33, 0\n"
    "v_mov_b32 v34, 0x3f803f80\n"
    "s_lshl_b32 s44, s37, 8\n"
    "s_add_u32 s44, s44, 0x27000\n"
    "v_add_u32 v35, s44, v2\n"
    "v_dot2c_f32_bf16 v32, v24, v34\n"
    "v_dot2c_f32_bf16 v33, v24, v24\n"
    "v_dot2c_f32_bf16 v32, v25, v34\n"
    "v_dot2c_f32_bf16 v33, v25, v25\n"
    "v_dot2c_f32_bf16 v32, v26, v34\n"
    "v_dot2c_f32_bf16 v33, v26, v26\n"
    "v_dot2c_f32_bf16 v32, v27, v34\n"
    "v_dot2c_f32_bf16 v33, v27, v27\n"
    "v_dot2c_f32_bf16 v32, v28, v34\n"
    "v_dot2c_f32_bf16 v33, v28, v28\n"
    "v_dot2c_f32_bf16 v32, v29, v34\n"
    "v_dot2c_f32_bf16 v33, v29, v29\n"
    "v_dot2c_f32_bf16 v32, v30, v34\n"
    "v_dot2c_f32_bf16 v33, v30, v30\n"
    "v_dot2c_f32_bf16 v32, v31, v34\n"
    "v_dot2c_f32_bf16 v33, v31, v31\n"
    "v_accvgpr_write_b32 a248, v24\n"
    "v_accvgpr_write_b32 a249, v25\n"
    "v_accvgpr_write_b32 a250, v26\n"
    "v_accvgpr_write_b32 a251, v27\n"
    "v_permlane32_swap_b32 v32, v33\n"
    "v_add_f32 v32, v32, v33\n"
    "ds_write_b32 v35, v32\n"
    "v_xor_b32 v35, 0x100, v35\n"
    "s_waitcnt lgkmcnt(0)\n"
    "s_barrier\n"
    "ds_read_b32 v33, v35\n"
    "v_accvgpr_write_b32 a252, v28\n"
    "v_accvgpr_write_b32 a253, v29\n"
    "v_accvgpr_write_b32 a254, v30\n"
    "v_accvgpr_write_b32 a255, v31\n"
    "s_waitcnt lgkmcnt(0)\n"
    "v_add_f32 v32, v32, v33\n"
    "buffer_store_dword v32, v2, s[24:27], s53 offen sc1\n"
    "s_add_u32 s52, s52, 0x240000\n"
    "s_add_u32 s53, s53, 0x240000\n"
    "s_add_u32 s46, s84, 0x3000\n"
    "buffer_load_dwordx4 a[48:51], v0, s[24:27], s46 offen sc1\n"
    "buffer_load_dwordx4 a[52:55], v0, s[24:27], s46 offen offset:1024 sc1\n"
    "buffer_load_dwordx4 a[56:59], v0, s[24:27], s46 offen offset:2048 sc1\n"
    "buffer_load_dwordx4 a[60:63], v0, s[24:27], s46 offen offset:3072 sc1\n"
    "v_and_b32 v15, 0x180, v0\n"
    "s_mov_b32 m0, 162528\n"
    "s_nop 0\n"
    "buffer_load_dword v15, s[28:31], s87 offen lds\n"
    "s_cmp_lg_u32 s37, 0\n"
    "s_cbranch_scc1 Lhs96_%=\n"
    "s_mov_b64 exec, 1\n"
    "v_mov_b32 v62, 0\n"
    "buffer_atomic_add v62, off, s[24:27], s91 offset:8 sc0 sc1\n"
    "s_waitcnt vmcnt(11)\n"
    "v_cmp_eq_u32 vcc, 16, v60\n"
    "s_cbranch_vccnz Lhd95_%=\n"
    "v_mov_b32 v60, 0\n"
    "buffer_atomic_add v60, off, s[24:27], s91 offset:8 sc0 sc1\n"
    "s_waitcnt vmcnt(9)\n"
    "v_cmp_eq_u32 vcc, 16, v61\n"
    "s_cbranch_vccnz Lhd95_%=\n"
    "v_mov_b32 v61, 0\n"
    "buffer_atomic_add v61, off, s[24:27], s91 offset:8 sc0 sc1\n"
    "Lhp94_%=:\n"
    "s_waitcnt vmcnt(2)\n"
    "v_cmp_eq_u32 vcc, 16, v62\n"
    "s_cbranch_vccnz Lhd95_%=\n"
    "v_mov_b32 v62, 0\n"
    "buffer_atomic_add v62, off, s[24:27], s91 offset:8 sc0 sc1\n"
    "s_waitcnt vmcnt(2)\n"
    "v_cmp_eq_u32 vcc, 16, v60\n"
    "s_cbranch_vccnz Lhd95_%=\n"
    "v_mov_b32 v60, 0\n"
    "buffer_atomic_add v60, off, s[24:27], s91 offset:8 sc0 sc1\n"
    "s_waitcnt vmcnt(2)\n"
    "v_cmp_eq_u32 vcc, 16, v61\n"
    "s_cbranch_vccnz Lhd95_%=\n"
    "v_mov_b32 v61, 0\n"
    "buffer_atomic_add v61, off, s[24:27], s91 offset:8 sc0 sc1\n"
    "s_sub_u32 s50, s50, 1\n"
    "s_cbranch_scc0 Lhp94_%=\n"
    "s_branch Lfail_%=\n"
    "Lhd95_%=:\n"
    "s_mov_b64 exec, -1\n"
    "Lhs96_%=:\n"
    "s_barrier\n"
    "buffer_load_dwordx4 v[128:131], v0, s[24:27], s51 offen sc1\n"
    "buffer_load_dwordx4 v[132:135], v0, s[24:27], s51 offen offset:1024 sc1\n"
    "buffer_load_dwordx4 v[136:139], v0, s[24:27], s51 offen offset:2048 sc1\n"
    "buffer_load_dwordx4 v[140:143], v0, s[24:27], s51 offen offset:3072 sc1\n"
    "s_add_u32 s42, s51, 0x1000\n"
    "buffer_load_dwordx4 v[144:147], v0, s[24:27], s42 offen sc1\n"
    "buffer_load_dwordx4 v[148:151], v0, s[24:27], s42 offen offset:1024 sc1\n"
    "buffer_load_dwordx4 v[152:155], v0, s[24:27], s42 offen offset:2048 sc1\n"
    "buffer_load_dwordx4 v[156:159], v0, s[24:27], s42 offen offset:3072 sc1\n"
    "s_add_u32 s42, s51, 0x2000\n"
    "buffer_load_dwordx4 v[160:163], v0, s[24:27], s42 offen sc1\n"
    "buffer_load_dwordx4 v[164:167], v0, s[24:27], s42 offen offset:1024 sc1\n"
    "buffer_load_dwordx4 v[168:171], v0, s[24:27], s42 offen offset:2048 sc1\n"
    "buffer_load_dwordx4 v[172:175], v0, s[24:27], s42 offen offset:3072 sc1\n"
    "s_waitcnt vmcnt(18)\n"
    "s_barrier\n"
    "ds_read_b128 v[40:43], v0 offset:32768\n"
    "ds_read_b128 v[44:47], v1 offset:32768\n"
    "ds_read_b128 v[48:51], v0 offset:33792\n"
    "ds_read_b128 v[52:55], v1 offset:33792\n"
    "ds_read_b128 v[56:59], v0 offset:34816\n"
    "s_waitcnt vmcnt(10)\n"
    "v_max3_u32 v15, v135, v129, v130\n"
    "v_max3_u32 v15, v15, v128, v131\n"
    "v_max3_u32 v15, v15, v132, v133\n"
    "v_max_u32 v15, v15, v134\n"
    "v_cmp_eq_u32 vcc, -1, v15\n"
    "s_waitcnt lgkmcnt(3)\n"
    "s_cbranch_vccnz Lre133_%=\n"
    "Lok134_%=:\n"
    "v_mfma_f32_32x32x16_bf16 v[64:79], v[40:43], v[128:131], 0\n"
    "s_add_u32 s42, s51, 0x3000\n"
    "buffer_load_dwordx4 v[176:179], v0, s[24:27], s42 offen sc1\n"
    "ds_read_b128 v[60:63], v1 offset:34816\n"
    "v_mfma_f32_32x32x16_bf16 v[80:95], v[44:47], v[128:131], 0\n"
    "ds_read_b128 v[40:43], v0\n"
    "s_waitcnt vmcnt(10)\n"
    "v_max3_u32 v15, v136, v137, v138\n"
    "s_waitcnt lgkmcnt(3)\n"
    "v_mfma_f32_32x32x16_bf16 v[64:79], v[48:51], v[132:135], v[64:79]\n"
    "ds_read_b128 v[44:47], v0 offset:35840\n"
    "s_waitcnt vmcnt(9)\n"
    "v_max3_u32 v15, v15, v139, v140\n"
    "v_max3_u32 v15, v15, v141, v142\n"
    "v_max_u32 v15, v15, v143\n"
    "v_cmp_eq_u32 vcc, -1, v15\n"
    "v_mfma_f32_32x32x16_bf16 v[80:95], v[52:55], v[132:135], v[80:95]\n"
    "buffer_load_dwordx4 v[180:183], v0, s[24:27], s42 offen offset:1024 sc1\n"
    "ds_read_b128 v[48:51], v1 offset:35840\n"
    "s_cbranch_vccnz Lre135_%=\n"
    "Lok136_%=:\n"
    "s_waitcnt lgkmcnt(3)\n"
    "v_mfma_f32_32x32x16_bf16 v[64:79], v[56:59], v[136:139], v[64:79]\n"
    "buffer_load_dwordx4 v[184:187], v0, s[24:27], s42 offen offset:2048 sc1\n"
    "ds_read_b128 v[52:55], v0 offset:36864\n"
    "v_mov_b32 v36, -1\n"
    "v_mov_b32 v37, -1\n"
    "v_mov_b32 v38, -1\n"
    "v_mfma_f32_32x32x16_bf16 v[80:95], v[60:63], v[136:139], v[80:95]\n"
    "ds_read_b128 v[56:59], v1 offset:36864\n"
    "v_mov_b32 v39, -1\n"
    "s_waitcnt lgkmcnt(3)\n"
    "v_mfma_f32_32x32x16_bf16 v[96:111], v[40:43], v[128:131], 0\n"
    "ds_read_b128 v[60:63], v0 offset:37888\n"
    "s_waitcnt vmcnt(10)\n"
    "v_max3_u32 v15, v144, v145, v146\n"
    "v_mfma_f32_32x32x16_bf16 v[64:79], v[44:47], v[140:143], v[64:79]\n"
    "ds_read_b128 v[40:43], v1 offset:37888\n"
    "s_waitcnt vmcnt(9)\n"
    "v_max3_u32 v15, v15, v147, v148\n"
    "v_max3_u32 v15, v15, v149, v150\n"
    "v_max_u32 v15, v15, v151\n"
    "v_cmp_eq_u32 vcc, -1, v15\n"
    "s_waitcnt lgkmcnt(3)\n"
    "v_mfma_f32_32x32x16_bf16 v[80:95], v[48:51], v[140:143], v[80:95]\n"
    "buffer_load_dwordx4 v[188:191], v0, s[24:27], s42 offen offset:3072 sc1\n"
    "ds_read_b128 v[44:47], v0 offset:1024\n"
    "s_cbranch_vccnz Lre137_%=\n"
    "Lok138_%=:\n"
    "v_mfma_f32_32x32x16_bf16 v[64:79], v[52:55], v[144:147], v[64:79]\n"
    "s_add_u32 s42, s51, 0x4000\n"
    "buffer_load_dwordx4 v[192:195], v0, s[24:27], s42 offen sc1\n"
    "ds_read_b128 v[48:51], v0 offset:38912\n"
    "s_waitcnt lgkmcnt(3)\n"
    "v_mfma_f32_32x32x16_bf16 v[80:95], v[56:59], v[144:147], v[80:95]\n"
    "ds_read_b128 v[52:55], v1 offset:38912\n"
    "v_mfma_f32_32x32x16_bf16 v[64:79], v[60:63], v[148:151], v[64:79]\n"
    "buffer_load_dwordx4 v[196:199], v0, s[24:27], s42 offen offset:1024 sc1\n"
    "ds_read_b128 v[56:59], v0 offset:39936\n"
    "s_waitcnt vmcnt(11)\n"
    "v_max3_u32 v15, v152, v153, v154\n"
    "s_waitcnt lgkmcnt(3)\n"
    "v_mfma_f32_32x32x16_bf16 v[80:95], v[40:43], v[148:151], v[80:95]\n"
    "ds_read_b128 v[60:63], v1 offset:39936\n"
    "s_waitcnt vmcnt(10)\n"
    "v_max3_u32 v15, v15, v155, v156\n"
    "v_max3_u32 v15, v15, v157, v158\n"
    "v_max_u32 v15, v15, v159\n"
    "v_cmp_eq_u32 vcc, -1, v15\n"
    "v_mfma_f32_32x32x16_bf16 v[96:111], v[44:47], v[132:135], v[96:111]\n"
    "ds_read_b128 v[40:43], v0 offset:2048\n"
    "s_cbranch_vccnz Lre139_%=\n"
    "Lok140_%=:\n"
    "s_waitcnt lgkmcnt(3)\n"
    "v_mfma_f32_32x32x16_bf16 v[64:79], v[48:51], v[152:155], v[64:79]\n"
    "buffer_load_dwordx4 v[200:203], v0, s[24:27], s42 offen offset:2048 sc1\n"
    "ds_read_b128 v[44:47], v0 offset:40960\n"
    "v_mfma_f32_32x32x16_bf16 v[80:95], v[52:55], v[152:155], v[80:95]\n"
    "s_add_u32 s43, s51, 0x8000\n"
    "buffer_load_dwordx4 v[112:115], v0, s[24:27], s43 offen sc1\n"
    "ds_read_b128 v[48:51], v1 offset:40960\n"
    "s_waitcnt lgkmcnt(3)\n"
    "v_mfma_f32_32x32x16_bf16 v[64:79], v[56:59], v[156:159], v[64:79]\n"
    "buffer_load_dwordx4 v[204:207], v0, s[24:27], s42 offen offset:3072 sc1\n"
    "ds_read_b128 v[52:55], v0 offset:41984\n"
    "s_waitcnt vmcnt(12)\n"
    "v_max3_u32 v15, v160, v161, v162\n"
    "v_mfma_f32_32x32x16_bf16 v[80:95], v[60:63], v[156:159], v[80:95]\n"
    "ds_read_b128 v[56:59], v1 offset:41984\n"
    "s_waitcnt vmcnt(11)\n"
    "v_max3_u32 v15, v15, v163, v164\n"
    "v_max3_u32 v15, v15, v165, v166\n"
    "v_max_u32 v15, v15, v167\n"
    "v_cmp_eq_u32 vcc, -1, v15\n"
    "s_waitcnt lgkmcnt(3)\n"
    "v_mfma_f32_32x32x16_bf16 v[96:111], v[40:43], v[136:139], v[96:111]\n"
    "ds_read_b128 v[60:63], v0 offset:43008\n"
    "s_cbranch_vccnz Lre141_%=\n"
    "Lok142_%=:\n"
    "v_mfma_f32_32x32x16_bf16 v[64:79], v[44:47], v[160:163], v[64:79]\n"
    "s_add_u32 s42, s51, 0x5000\n"
    "buffer_load_dwordx4 v[208:211], v0, s[24:27], s42 offen sc1\n"
    "ds_read_b128 v[40:43], v1 offset:43008\n"
    "s_waitcnt lgkmcnt(3)\n"
    "v_mfma_f32_32x32x16_bf16 v[80:95], v[48:51], v[160:163], v[80:95]\n"
    "ds_read_b128 v[44:47], v0 offset:3072\n"
    "s_waitcnt vmcnt(11)\n"
    "v_max3_u32 v15, v168, v169, v170\n"
    "v_mfma_f32_32x32x16_bf16 v[64:79], v[52:55], v[164:167], v[64:79]\n"
    "ds_read_b128 v[48:51], v0 offset:44032\n"
    "s_waitcnt vmcnt(10)\n"
    "v_max3_u32 v15, v15, v171, v172\n"
    "v_max3_u32 v15, v15, v173, v174\n"
    "v_max_u32 v15, v15, v175\n"
    "v_cmp_eq_u32 vcc, -1, v15\n"
    "s_waitcnt lgkmcnt(3)\n"
    "v_mfma_f32_32x32x16_bf16 v[80:95], v[56:59], v[164:167], v[80:95]\n"
    "buffer_load_dwordx4 v[212:215], v0, s[24:27], s42 offen offset:1024 sc1\n"
    "ds_read_b128 v[52:55], v1 offset:44032\n"
    "s_cbranch_vccnz Lre143_%=\n"
    "Lok144_%=:\n"
    "v_mfma_f32_32x32x16_bf16 v[64:79], v[60:63], v[168:171], v[64:79]\n"
    "buffer_load_dwordx4 v[216:219], v0, s[24:27], s42 offen offset:2048 sc1\n"
    "ds_read_b128 v[56:59], v0 offset:45056\n"
    "s_waitcnt lgkmcnt(3)\n"
    "v_mfma_f32_32x32x16_bf16 v[80:95], v[40:43], v[168:171], v[80:95]\n"
    "buffer_load_dwordx4 v[116:119], v0, s[24:27], s43 offen offset:1024 sc1\n"
    "ds_read_b128 v[60:63], v1 offset:45056\n"
    "v_mfma_f32_32x32x16_bf16 v[96:111], v[44:47], v[140:143], v[96:111]\n"
    "ds_read_b128 v[40:43], v0 offset:46080\n"
    "s_waitcnt vmcnt(12)\n"
    "v_max3_u32 v15, v176, v177, v178\n"
    "s_waitcnt lgkmcnt(3)\n"
    "v_mfma_f32_32x32x16_bf16 v[64:79], v[48:51], v[172:175], v[64:79]\n"
    "ds_read_b128 v[44:47], v1 offset:46080\n"
    "s_waitcnt vmcnt(11)\n"
    "v_max3_u32 v15, v15, v179, v180\n"
    "v_max3_u32 v15, v15, v181, v182\n"
    "v_max_u32 v15, v15, v183\n"
    "v_cmp_eq_u32 vcc, -1, v15\n"
    "v_mfma_f32_32x32x16_bf16 v[80:95], v[52:55], v[172:175], v[80:95]\n"
    "buffer_load_dwordx4 v[220:223], v0, s[24:27], s42 offen offset:3072 sc1\n"
    "ds_read_b128 v[48:51], v0 offset:4096\n"
    "s_cbranch_vccnz Lre145_%=\n"
    "Lok146_%=:\n"
    "s_waitcnt lgkmcnt(3)\n"
    "v_mfma_f32_32x32x16_bf16 v[64:79], v[56:59], v[176:179], v[64:79]\n"
    "s_add_u32 s42, s51, 0x6000\n"
    "buffer_load_dwordx4 v[224:227], v0, s[24:27], s42 offen sc1\n"
    "ds_read_b128 v[52:55], v0 offset:47104\n"
    "v_mfma_f32_32x32x16_bf16 v[80:95], v[60:63], v[176:179], v[80:95]\n"
    "ds_read_b128 v[56:59], v1 offset:47104\n"
    "s_waitcnt lgkmcnt(3)\n"
    "v_mfma_f32_32x32x16_bf16 v[64:79], v[40:43], v[180:183], v[64:79]\n"
    "buffer_load_dwordx4 v[228:231], v0, s[24:27], s42 offen offset:1024 sc1\n"
    "ds_read_b128 v[60:63], v0 offset:48128\n"
    "s_waitcnt vmcnt(13)\n"
    "v_max3_u32 v15, v184, v185, v186\n"
    "v_mfma_f32_32x32x16_bf16 v[80:95], v[44:47], v[180:183], v[80:95]\n"
    "ds_read_b128 v[40:43], v1 offset:48128\n"
    "s_waitcnt vmcnt(12)\n"
    "v_max3_u32 v15, v15, v187, v188\n"
    "v_max3_u32 v15, v15, v189, v190\n"
    "v_max_u32 v15, v15, v191\n"
    "v_cmp_eq_u32 vcc, -1, v15\n"
    "s_waitcnt lgkmcnt(3)\n"
    "v_mfma_f32_32x32x16_bf16 v[96:111], v[48:51], v[144:147], v[96:111]\n"
    "ds_read_b128 v[44:47], v0 offset:5120\n"
    "s_cbranch_vccnz Lre147_%=\n"
    "Lok148_%=:\n"
    "v_mfma_f32_32x32x16_bf16 v[64:79], v[52:55], v[184:187], v[64:79]\n"
    "buffer_load_dwordx4 v[232:235], v0, s[24:27], s42 offen offset:2048 sc1\n"
    "ds_read_b128 v[48:51], v0 offset:49152\n"
    "s_waitcnt lgkmcnt(3)\n"
    "v_mfma_f32_32x32x16_bf16 v[80:95], v[56:59], v[184:187], v[80:95]\n"
    "ds_read_b128 v[52:55], v1 offset:49152\n"
    "v_mfma_f32_32x32x16_bf16 v[64:79], v[60:63], v[188:191], v[64:79]\n"
    "buffer_load_dwordx4 v[236:239], v0, s[24:27], s42 offen offset:3072 sc1\n"
    "ds_read_b128 v[56:59], v0 offset:50176\n"
    "s_waitcnt vmcnt(13)\n"
    "v_max3_u32 v15, v192, v193, v194\n"
    "s_waitcnt lgkmcnt(3)\n"
    "v_mfma_f32_32x32x16_bf16 v[80:95], v[40:43], v[188:191], v[80:95]\n"
    "ds_read_b128 v[60:63], v1 offset:50176\n"
    "s_waitcnt vmcnt(12)\n"
    "v_max3_u32 v15, v15, v195, v196\n"
    "v_max3_u32 v15, v15, v197, v198\n"
    "v_max_u32 v15, v15, v199\n"
    "v_cmp_eq_u32 vcc, -1, v15\n"
    "v_mfma_f32_32x32x16_bf16 v[96:111], v[44:47], v[148:151], v[96:111]\n"
    "ds_read_b128 v[40:43], v0 offset:51200\n"
    "s_cbranch_vccnz Lre149_%=\n"
    "Lok150_%=:\n"
    "s_waitcnt lgkmcnt(3)\n"
    "v_mfma_f32_32x32x16_bf16 v[64:79], v[48:51], v[192:195], v[64:79]\n"
    "s_add_u32 s42, s51, 0x7000\n"
    "buffer_load_dwordx4 v[240:243], v0, s[24:27], s42 offen sc1\n"
    "ds_read_b128 v[44:47], v1 offset:51200\n"
    "v_mfma_f32_32x32x16_bf16 v[80:95], v[52:55], v[192:195], v[80:95]\n"
    "ds_read_b128 v[48:51], v0 offset:6144\n"
    "s_waitcnt vmcnt(12)\n"
    "v_max3_u32 v15, v200, v201, v202\n"
    "s_waitcnt lgkmcnt(3)\n"
    "v_mfma_f32_32x32x16_bf16 v[64:79], v[56:59], v[196:199], v[64:79]\n"
    "ds_read_b128 v[52:55], v0 offset:52224\n"
    "s_waitcnt vmcnt(10)\n"
    "v_max3_u32 v15, v15, v203, v204\n"
    "v_max3_u32 v15, v15, v205, v206\n"
    "v_max_u32 v15, v15, v207\n"
    "v_cmp_eq_u32 vcc, -1, v15\n"
    "v_mfma_f32_32x32x16_bf16 v[80:95], v[60:63], v[196:199], v[80:95]\n"
    "buffer_load_dwordx4 v[244:247], v0, s[24:27], s42 offen offset:1024 sc1\n"
    "ds_read_b128 v[56:59], v1 offset:52224\n"
    "s_cbranch_vccnz Lre151_%=\n"
    "Lok152_%=:\n"
    "s_waitcnt lgkmcnt(3)\n"
    "v_mfma_f32_32x32x16_bf16 v[64:79], v[40:43], v[200:203], v[64:79]\n"
    "buffer_load_dwordx4 v[248:251], v0, s[24:27], s42 offen offset:2048 sc1\n"
    "ds_read_b128 v[60:63], v0 offset:53248\n"
    "v_mfma_f32_32x32x16_bf16 v[80:95], v[44:47], v[200:203], v[80:95]\n"
    "ds_read_b128 v[40:43], v1 offset:53248\n"
    "s_waitcnt lgkmcnt(3)\n"
    "v_mfma_f32_32x32x16_bf16 v[96:111], v[48:51], v[152:155], v[96:111]\n"
    "ds_read_b128 v[44:47], v0 offset:54272\n"
    "s_waitcnt vmcnt(11)\n"
    "v_max3_u32 v15, v208, v209, v210\n"
    "v_mfma_f32_32x32x16_bf16 v[64:79], v[52:55], v[204:207], v[64:79]\n"
    "ds_read_b128 v[48:51], v1 offset:54272\n"
    "s_waitcnt vmcnt(10)\n"
    "v_max3_u32 v15, v15, v211, v212\n"
    "v_max3_u32 v15, v15, v213, v214\n"
    "v_max_u32 v15, v15, v215\n"
    "v_cmp_eq_u32 vcc, -1, v15\n"
    "s_waitcnt lgkmcnt(3)\n"
    "v_mfma_f32_32x32x16_bf16 v[80:95], v[56:59], v[204:207], v[80:95]\n"
    "buffer_load_dwordx4 v[252:255], v0, s[24:27], s42 offen offset:3072 sc1\n"
    "ds_read_b128 v[52:55], v0 offset:7168\n"
    "s_cbranch_vccnz Lre153_%=\n"
    "Lok154_%=:\n"
    "v_mfma_f32_32x32x16_bf16 v[64:79], v[60:63], v[208:211], v[64:79]\n"
    "ds_read_b128 v[56:59], v0 offset:55296\n"
    "s_waitcnt lgkmcnt(3)\n"
    "v_mfma_f32_32x32x16_bf16 v[80:95], v[40:43], v[208:211], v[80:95]\n"
    "s_add_u32 m0, s78, 0x10000\n"
    "s_add_u32 s47, s68, 0x61800\n"
    "buffer_load_dwordx4 v0, s[24:27], s47 offen sc1 lds\n"
    "ds_read_b128 v[60:63], v1 offset:55296\n"
    "s_waitcnt vmcnt(10)\n"
    "v_max3_u32 v32, v119, v113, v114\n"
    "v_mfma_f32_32x32x16_bf16 v[64:79], v[44:47], v[212:215], v[64:79]\n"
    "ds_read_b128 v[40:43], v0 offset:56320\n"
    "v_max3_u32 v15, v216, v217, v218\n"
    "v_max3_u32 v32, v32, v112, v115\n"
    "v_max3_u32 v32, v32, v116, v117\n"
    "s_waitcnt lgkmcnt(3)\n"
    "v_mfma_f32_32x32x16_bf16 v[80:95], v[48:51], v[212:215], v[80:95]\n"
    "ds_read_b128 v[44:47], v1 offset:56320\n"
    "s_waitcnt vmcnt(9)\n"
    "v_max3_u32 v15, v15, v219, v220\n"
    "v_max3_u32 v15, v15, v221, v222\n"
    "v_max_u32 v15, v15, v223\n"
    "v_cmp_eq_u32 vcc, -1, v15\n"
    "v_mfma_f32_32x32x16_bf16 v[96:111], v[52:55], v[156:159], v[96:111]\n"
    "buffer_load_dwordx4 v0, s[24:27], s47 offen offset:1024 sc1 lds\n"
    "ds_read_b128 v[48:51], v0 offset:8192\n"
    "s_cbranch_vccnz Lre155_%=\n"
    "Lok156_%=:\n"
    "v_max_u32 v32, v32, v118\n"
    "s_waitcnt lgkmcnt(3)\n"
    "v_cmp_eq_u32 vcc, -1, v32\n"
    "v_mfma_f32_32x32x16_bf16 v[64:79], v[56:59], v[216:219], v[64:79]\n"
    "ds_read_b128 v[52:55], v0 offset:57344\n"
    "s_cbranch_vccnz Lre165_%=\n"
    "Lok166_%=:\n"
    "v_add_f32 v24, v112, v116\n"
    "v_add_f32 v25, v113, v117\n"
    "v_add_f32 v26, v114, v118\n"
    "v_add_f32 v27, v115, v119\n"
    "v_mfma_f32_32x32x16_bf16 v[80:95], v[60:63], v[216:219], v[80:95]\n"
    "buffer_load_dwordx4 v0, s[24:27], s47 offen offset:2048 sc1 lds\n"
    "ds_read_b128 v[56:59], v1 offset:57344\n"
    "v_permlane32_swap_b32 v24, v25\n"
    "v_permlane32_swap_b32 v26, v27\n"
    "s_waitcnt lgkmcnt(3)\n"
    "v_mfma_f32_32x32x16_bf16 v[64:79], v[40:43], v[220:223], v[64:79]\n"
    "ds_read_b128 v[60:63], v0 offset:58368\n"
    "s_waitcnt vmcnt(10)\n"
    "v_max3_u32 v15, v224, v225, v226\n"
    "v_add_f32 v24, v24, v25\n"
    "v_add_f32 v26, v26, v27\n"
    "s_nop 1\n"
    "v_permlane16_swap_b32 v24, v26\n"
    "v_mfma_f32_32x32x16_bf16 v[80:95], v[44:47], v[220:223], v[80:95]\n"
    "ds_read_b128 v[40:43], v1 offset:58368\n"
    "s_waitcnt vmcnt(9)\n"
    "v_max3_u32 v15, v15, v227, v228\n"
    "v_max3_u32 v15, v15, v229, v230\n"
    "v_max_u32 v15, v15, v231\n"
    "v_cmp_eq_u32 vcc, -1, v15\n"
    "s_waitcnt lgkmcnt(3)\n"
    "v_mfma_f32_32x32x16_bf16 v[96:111], v[48:51], v[160:163], v[96:111]\n"
    "buffer_load_dwordx4 v0, s[24:27], s47 offen offset:3072 sc1 lds\n"
    "ds_read_b128 v[44:47], v0 offset:59392\n"
    "s_cbranch_vccnz Lre157_%=\n"
    "Lok158_%=:\n"
    "v_add_f32 v24, v24, v26\n"
    "ds_bpermute_b32 v28, v13, v24\n"
    "v_mfma_f32_32x32x16_bf16 v[64:79], v[52:55], v[224:227], v[64:79]\n"
    "ds_read_b128 v[48:51], v1 offset:59392\n"
    "ds_bpermute_b32 v29, v13, v24 offset:32\n"
    "s_waitcnt lgkmcnt(5)\n"
    "v_mfma_f32_32x32x16_bf16 v[80:95], v[56:59], v[224:227], v[80:95]\n"
    "s_add_u32 m0, s78, 0x11000\n"
    "s_add_u32 s47, s69, 0x61800\n"
    "buffer_load_dwordx4 v0, s[24:27], s47 offen sc1 lds\n"
    "ds_read_b128 v[52:55], v0 offset:9216\n"
    "s_waitcnt vmcnt(10)\n"
    "v_max3_u32 v15, v232, v233, v234\n"
    "v_mfma_f32_32x32x16_bf16 v[64:79], v[60:63], v[228:231], v[64:79]\n"
    "ds_read_b128 v[56:59], v0 offset:60416\n"
    "s_waitcnt vmcnt(9)\n"
    "v_max3_u32 v15, v15, v235, v236\n"
    "v_max3_u32 v15, v15, v237, v238\n"
    "v_max_u32 v15, v15, v239\n"
    "v_cmp_eq_u32 vcc, -1, v15\n"
    "s_waitcnt lgkmcnt(5)\n"
    "v_mfma_f32_32x32x16_bf16 v[80:95], v[40:43], v[228:231], v[80:95]\n"
    "ds_read_b128 v[60:63], v1 offset:60416\n"
    "s_cbranch_vccnz Lre159_%=\n"
    "Lok160_%=:\n"
    "s_waitcnt lgkmcnt(3)\n"
    "v_mul_f32 v28, 0xbb000000, v28\n"
    "v_mul_f32 v29, 0x3b000000, v29\n"
    "v_cvt_pk_bf16_f32 v30, v28, v28\n"
    "v_mfma_f32_32x32x16_bf16 v[64:79], v[44:47], v[232:235], v[64:79]\n"
    "buffer_load_dwordx4 v0, s[24:27], s47 offen offset:1024 sc1 lds\n"
    "ds_read_b128 v[40:43], v0 offset:61440\n"
    "v_fma_f32 v29, -v28, v28, v29\n"
    "v_lshlrev_b32 v31, 16, v30\n"
    "s_waitcnt lgkmcnt(3)\n"
    "v_mfma_f32_32x32x16_bf16 v[80:95], v[48:51], v[232:235], v[80:95]\n"
    "ds_read_b128 v[44:47], v1 offset:61440\n"
    "v_max_f32 v29, 0, v29\n"
    "v_sub_f32 v31, v28, v31\n"
    "v_add_f32 v29, 0x3727c5ac, v29\n"
    "v_cvt_pk_bf16_f32 v31, v31, v31\n"
    "v_mfma_f32_32x32x16_bf16 v[96:111], v[52:55], v[164:167], v[96:111]\n"
    "buffer_load_dwordx4 v0, s[24:27], s47 offen offset:2048 sc1 lds\n"
    "ds_read_b128 v[48:51], v0 offset:62464\n"
    "s_waitcnt vmcnt(10)\n"
    "v_max3_u32 v15, v240, v241, v242\n"
    "s_waitcnt lgkmcnt(3)\n"
    "v_mfma_f32_32x32x16_bf16 v[64:79], v[56:59], v[236:239], v[64:79]\n"
    "ds_read_b128 v[52:55], v1 offset:62464\n"
    "s_waitcnt vmcnt(9)\n"
    "v_max3_u32 v15, v15, v243, v244\n"
    "v_max3_u32 v15, v15, v245, v246\n"
    "v_max_u32 v15, v15, v247\n"
    "v_cmp_eq_u32 vcc, -1, v15\n"
    "v_mfma_f32_32x32x16_bf16 v[80:95], v[60:63], v[236:239], v[80:95]\n"
    "ds_read_b128 v[56:59], v0 offset:10240\n"
    "s_cbranch_vccnz Lre161_%=\n"
    "Lok162_%=:\n"
    "v_rsq_f32 v16, v29\n"
    "v_permlane32_swap_b32 v30, v31\n"
    "s_waitcnt lgkmcnt(3)\n"
    "v_mfma_f32_32x32x16_bf16 v[64:79], v[40:43], v[240:243], v[64:79]\n"
    "buffer_load_dwordx4 v0, s[24:27], s47 offen offset:3072 sc1 lds\n"
    "ds_read_b128 v[60:63], v0 offset:63488\n"
    "v_sqrt_f32 v18, v29\n"
    "v_mul_f32 v17, 0x3eeff558, v16\n"
    "v_mfma_f32_32x32x16_bf16 v[80:95], v[44:47], v[240:243], v[80:95]\n"
    "ds_read_b128 v[40:43], v1 offset:63488\n"
    "v_mul_f32 v18, 0x3eeff558, v18\n"
    "v_mov_b32 v20, v30\n"
    "v_mov_b32 v21, v30\n"
    "v_mov_b32 v22, v30\n"
    "s_waitcnt lgkmcnt(3)\n"
    "v_mfma_f32_32x32x16_bf16 v[64:79], v[48:51], v[244:247], v[64:79]\n"
    "s_add_u32 m0, s86, 0x400\n"
    "s_nop 0\n"
    "buffer_load_dword v3, s[24:27], s47 offen sc1 lds\n"
    "ds_read_b128 v[44:47], v0 offset:64512\n"
    "s_waitcnt vmcnt(10)\n"
    "v_max3_u32 v15, v248, v249, v250\n"
    "v_mfma_f32_32x32x16_bf16 v[80:95], v[52:55], v[244:247], v[80:95]\n"
    "ds_read_b128 v[48:51], v1 offset:64512\n"
    "s_waitcnt vmcnt(9)\n"
    "v_max3_u32 v15, v15, v251, v252\n"
    "v_max3_u32 v15, v15, v253, v254\n"
    "v_max_u32 v15, v15, v255\n"
    "v_cmp_eq_u32 vcc, -1, v15\n"
    "s_waitcnt lgkmcnt(3)\n"
    "v_mfma_f32_32x32x16_bf16 v[96:111], v[56:59], v[168:171], v[96:111]\n"
    "ds_read_b128 v[52:55], v0 offset:11264\n"
    "s_cbranch_vccnz Lre163_%=\n"
    "Lok164_%=:\n"
    "v_mov_b32 v23, v30\n"
    "v_mfma_f32_32x32x16_bf16 v[64:79], v[60:63], v[248:251], v[64:79]\n"
    "s_add_u32 s44, s60, 0x4000\n"
    "buffer_store_dwordx4 v[36:39], v0, s[24:27], s44 offen sc1\n"
    "ds_read_b128 v[56:59], v4 offset:512\n"
    "s_waitcnt lgkmcnt(3)\n"
    "v_mfma_f32_32x32x16_bf16 v[80:95], v[40:43], v[248:251], v[80:95]\n"
    "ds_read_b128 v[60:63], v4 offset:1536\n"
    "v_mfma_f32_32x32x16_bf16 v[64:79], v[44:47], v[252:255], v[64:79]\n"
    "s_add_u32 s44, s61, 0x5000\n"
    "buffer_store_dwordx4 v[36:39], v0, s[24:27], s44 offen sc1\n"
    "ds_read_b128 v[40:43], v0 offset:12288\n"
    "s_waitcnt lgkmcnt(3)\n"
    "v_mfma_f32_32x32x16_bf16 v[80:95], v[48:51], v[252:255], v[80:95]\n"
    "ds_read_b128 v[44:47], v0 offset:13312\n"
    "v_mfma_f32_32x32x16_bf16 v[96:111], v[52:55], v[172:175], v[96:111]\n"
    "s_add_u32 s46, s85, 0x3000\n"
    "buffer_load_dwordx4 a[136:139], v0, s[24:27], s46 offen sc1\n"
    "ds_read_b128 v[48:51], v0 offset:14336\n"
    "s_waitcnt lgkmcnt(3)\n"
    "v_mfma_f32_32x32x16_bf16 v[64:79], v[56:59], v[20:23], v[64:79]\n"
    "ds_read_b128 v[52:55], v0 offset:15360\n"
    "v_mfma_f32_32x32x16_bf16 v[80:95], v[60:63], v[20:23], v[80:95]\n"
    "s_barrier\n"
    "buffer_load_dwordx4 a[140:143], v0, s[24:27], s46 offen offset:1024 sc1\n"
    "ds_read_b128 v[56:59], v0 offset:16384\n"
    "s_add_u32 s51, s51, 0x240000\n"
    "s_waitcnt lgkmcnt(3)\n"
    "v_mfma_f32_32x32x16_bf16 v[96:111], v[40:43], v[176:179], v[96:111]\n"
    "ds_read_b128 v[60:63], v0 offset:17408\n"
    "v_mfma_f32_32x32x16_bf16 v[96:111], v[44:47], v[180:183], v[96:111]\n"
    "s_add_u32 m0, s78, 0x8000\n"
    "s_add_u32 s47, s68, 0x82000\n"
    "buffer_load_dwordx4 v0, s[24:27], s47 offen sc1 lds\n"
    "ds_read_b128 v[40:43], v0 offset:18432\n"
    "s_waitcnt lgkmcnt(3)\n"
    "v_mfma_f32_32x32x16_bf16 v[96:111], v[48:51], v[184:187], v[96:111]\n"
    "ds_read_b128 v[44:47], v0 offset:19456\n"
    "v_mul_f32 v64, v17, v64\n"
    "v_mul_f32 v65, v17, v65\n"
    "v_mul_f32 v66, v17, v66\n"
    "v_mul_f32 v67, v17, v67\n"
    "v_fmaak_f32 v24, v64, v64, 0x409d30fc\n"
    "v_fmaak_f32 v25, v65, v65, 0x409d30fc\n"
    "v_mfma_f32_32x32x16_bf16 v[96:111], v[52:55], v[188:191], v[96:111]\n"
    "buffer_load_dwordx4 v0, s[24:27], s47 offen offset:1024 sc1 lds\n"
    "ds_read_b128 v[48:51], v0 offset:20480\n"
    "v_fmaak_f32 v26, v66, v66, 0x409d30fc\n"
    "v_fmaak_f32 v27, v67, v67, 0x409d30fc\n"
    "v_mul_f32 v24, v24, v64\n"
    "s_waitcnt lgkmcnt(3)\n"
    "v_mfma_f32_32x32x16_bf16 v[96:111], v[56:59], v[192:195], v[96:111]\n"
    "ds_read_b128 v[52:55], v0 offset:21504\n"
    "v_mul_f32 v25, v25, v65\n"
    "v_mul_f32 v26, v26, v66\n"
    "v_mul_f32 v27, v27, v67\n"
    "v_exp_f32 v24, -v24\n"
    "v_mul_f32 v64, v64, v72\n"
    "v_mfma_f32_32x32x16_bf16 v[96:111], v[60:63], v[196:199], v[96:111]\n"
    "buffer_load_dwordx4 v0, s[24:27], s47 offen offset:2048 sc1 lds\n"
    "ds_read_b128 v[56:59], v0 offset:22528\n"
    "v_exp_f32 v25, -v25\n"
    "v_mul_f32 v65, v65, v73\n"
    "s_waitcnt lgkmcnt(3)\n"
    "v_mfma_f32_32x32x16_bf16 v[96:111], v[40:43], v[200:203], v[96:111]\n"
    "ds_read_b128 v[60:63], v0 offset:23552\n"
    "v_exp_f32 v26, -v26\n"
    "v_mul_f32 v66, v66, v74\n"
    "v_exp_f32 v27, -v27\n"
    "v_mul_f32 v67, v67, v75\n"
    "v_mfma_f32_32x32x16_bf16 v[96:111], v[44:47], v[204:207], v[96:111]\n"
    "buffer_load_dwordx4 v0, s[24:27], s47 offen offset:3072 sc1 lds\n"
    "ds_read_b128 v[40:43], v0 offset:24576\n"
    "v_fma_f32 v24, v24, v18, v18\n"
    "v_fma_f32 v25, v25, v18, v18\n"
    "v_fma_f32 v26, v26, v18, v18\n"
    "s_waitcnt lgkmcnt(3)\n"
    "v_mfma_f32_32x32x16_bf16 v[96:111], v[48:51], v[208:211], v[96:111]\n"
    "ds_read_b128 v[44:47], v0 offset:25600\n"
    "v_fma_f32 v27, v27, v18, v18\n"
    "v_rcp_f32 v24, v24\n"
    "v_rcp_f32 v25, v25\n"
    "v_mfma_f32_32x32x16_bf16 v[96:111], v[52:55], v[212:215], v[96:111]\n"
    "s_add_u32 m0, s78, 0x9000\n"
    "s_add_u32 s47, s69, 0x82000\n"
    "buffer_load_dwordx4 v0, s[24:27], s47 offen sc1 lds\n"
    "ds_read_b128 v[48:51], v0 offset:26624\n"
    "s_waitcnt lgkmcnt(3)\n"
    "v_mfma_f32_32x32x16_bf16 v[96:111], v[56:59], v[216:219], v[96:111]\n"
    "ds_read_b128 v[52:55], v0 offset:27648\n"
    "v_rcp_f32 v26, v26\n"
    "v_rcp_f32 v27, v27\n"
    "v_mul_f32 v64, v64, v24\n"
    "v_mul_f32 v65, v65, v25\n"
    "v_mfma_f32_32x32x16_bf16 v[96:111], v[60:63], v[220:223], v[96:111]\n"
    "buffer_load_dwordx4 v0, s[24:27], s47 offen offset:1024 sc1 lds\n"
    "ds_read_b128 v[56:59], v0 offset:28672\n"
    "v_mul_f32 v66, v66, v26\n"
    "v_mul_f32 v67, v67, v27\n"
    "v_cvt_pk_bf16_f32 v28, v64, v65\n"
    "s_waitcnt lgkmcnt(3)\n"
    "v_mfma_f32_32x32x16_bf16 v[96:111], v[40:43], v[224:227], v[96:111]\n"
    "ds_read_b128 v[60:63], v0 offset:29696\n"
    "v_cvt_pk_bf16_f32 v29, v66, v67\n"
    "v_mul_f32 v68, v17, v68\n"
    "v_mul_f32 v69, v17, v69\n"
    "v_mul_f32 v70, v17, v70\n"
    "v_mul_f32 v71, v17, v71\n"
    "v_fmaak_f32 v24, v68, v68, 0x409d30fc\n"
    "v_mfma_f32_32x32x16_bf16 v[96:111], v[44:47], v[228:231], v[96:111]\n"
    "buffer_load_dwordx4 v0, s[24:27], s47 offen offset:2048 sc1 lds\n"
    "ds_read_b128 v[40:43], v0 offset:30720\n"
    "v_fmaak_f32 v25, v69, v69, 0x409d30fc\n"
    "v_fmaak_f32 v26, v70, v70, 0x409d30fc\n"
    "v_fmaak_f32 v27, v71, v71, 0x409d30fc\n"
    "s_waitcnt lgkmcnt(3)\n"
    "v_mfma_f32_32x32x16_bf16 v[96:111], v[48:51], v[232:235], v[96:111]\n"
    "ds_read_b128 v[44:47], v0 offset:31744\n"
    "v_mul_f32 v24, v24, v68\n"
    "v_mul_f32 v25, v25, v69\n"
    "v_mul_f32 v26, v26, v70\n"
    "v_mul_f32 v27, v27, v71\n"
    "v_exp_f32 v24, -v24\n"
    "v_mfma_f32_32x32x16_bf16 v[96:111], v[52:55], v[236:239], v[96:111]\n"
    "s_waitcnt vmcnt(11)\n"
    "s_barrier\n"
    "buffer_load_dwordx4 v0, s[24:27], s47 offen offset:3072 sc1 lds\n"
    "ds_read_b128 v[48:51], v4 offset:0\n"
    "v_mul_f32 v68, v68, v76\n"
    "s_waitcnt lgkmcnt(3)\n"
    "v_mfma_f32_32x32x16_bf16 v[96:111], v[56:59], v[240:243], v[96:111]\n"
    "ds_read_b128 v[52:55], v1\n"
    "v_exp_f32 v25, -v25\n"
    "v_mul_f32 v69, v69, v77\n"
    "v_exp_f32 v26, -v26\n"
    "v_mul_f32 v70, v70, v78\n"
    "v_mfma_f32_32x32x16_bf16 v[96:111], v[60:63], v[244:247], v[96:111]\n"
    "s_add_u32 m0, s86, 0x200\n"
    "s_nop 0\n"
    "buffer_load_dword v3, s[24:27], s47 offen sc1 lds\n"
    "ds_read_b128 v[56:59], v1 offset:1024\n"
    "v_exp_f32 v27, -v27\n"
    "s_waitcnt lgkmcnt(3)\n"
    "v_mfma_f32_32x32x16_bf16 v[96:111], v[40:43], v[248:251], v[96:111]\n"
    "ds_read_b128 v[60:63], v1 offset:2048\n"
    "v_mul_f32 v71, v71, v79\n"
    "v_fma_f32 v24, v24, v18, v18\n"
    "v_fma_f32 v25, v25, v18, v18\n"
    "v_fma_f32 v26, v26, v18, v18\n"
    "v_fma_f32 v27, v27, v18, v18\n"
    "v_mfma_f32_32x32x16_bf16 v[96:111], v[44:47], v[252:255], v[96:111]\n"
    "s_add_u32 m0, s78, 0x18000\n"
    "s_add_u32 s47, s70, 0xa2800\n"
    "buffer_load_dwordx4 v0, s[24:27], s47 offen sc1 lds\n"
    "ds_read_b128 v[40:43], v1 offset:3072\n"
    "s_waitcnt lgkmcnt(3)\n"
    "v_mfma_f32_32x32x16_bf16 v[96:111], v[48:51], v[20:23], v[96:111]\n"
    "ds_read_b128 v[44:47], v1 offset:4096\n"
    "v_rcp_f32 v24, v24\n"
    "v_rcp_f32 v25, v25\n"
    "v_rcp_f32 v26, v26\n"
    "v_mfma_f32_32x32x16_bf16 v[112:127], v[52:55], v[128:131], 0\n"
    "buffer_load_dwordx4 v0, s[24:27], s47 offen offset:1024 sc1 lds\n"
    "ds_read_b128 v[48:51], v1 offset:5120\n"
    "v_rcp_f32 v27, v27\n"
    "v_mul_f32 v68, v68, v24\n"
    "s_waitcnt lgkmcnt(3)\n"
    "v_mfma_f32_32x32x16_bf16 v[112:127], v[56:59], v[132:135], v[112:127]\n"
    "ds_read_b128 v[52:55], v1 offset:6144\n"
    "v_mul_f32 v69, v69, v25\n"
    "v_mul_f32 v70, v70, v26\n"
    "v_mul_f32 v71, v71, v27\n"
    "v_cvt_pk_bf16_f32 v30, v68, v69\n"
    "v_cvt_pk_bf16_f32 v31, v70, v71\n"
    "v_mfma_f32_32x32x16_bf16 v[112:127], v[60:63], v[136:139], v[112:127]\n"
    "buffer_load_dwordx4 v0, s[24:27], s47 offen offset:2048 sc1 lds\n"
    "ds_read_b128 v[56:59], v1 offset:7168\n"
    "buffer_store_dwordx4 v[28:31], v0, s[24:27], s60 offen sc1\n"
    "v_mul_f32 v80, v17, v80\n"
    "s_waitcnt lgkmcnt(3)\n"
    "v_mfma_f32_32x32x16_bf16 v[112:127], v[40:43], v[140:143], v[112:127]\n"
    "ds_read_b128 v[60:63], v1 offset:8192\n"
    "v_mul_f32 v81, v17, v81\n"
    "v_mul_f32 v82, v17, v82\n"
    "v_mul_f32 v83, v17, v83\n"
    "v_fmaak_f32 v24, v80, v80, 0x409d30fc\n"
    "v_fmaak_f32 v25, v81, v81, 0x409d30fc\n"
    "v_fmaak_f32 v26, v82, v82, 0x409d30fc\n"
    "v_mfma_f32_32x32x16_bf16 v[112:127], v[44:47], v[144:147], v[112:127]\n"
    "buffer_load_dwordx4 v0, s[24:27], s47 offen offset:3072 sc1 lds\n"
    "ds_read_b128 v[40:43], v1 offset:9216\n"
    "v_fmaak_f32 v27, v83, v83, 0x409d30fc\n"
    "v_mul_f32 v24, v24, v80\n"
    "v_mul_f32 v25, v25, v81\n"
    "s_waitcnt lgkmcnt(3)\n"
    "v_mfma_f32_32x32x16_bf16 v[112:127], v[48:51], v[148:151], v[112:127]\n"
    "ds_read_b128 v[44:47], v1 offset:10240\n"
    "v_mul_f32 v26, v26, v82\n"
    "v_mul_f32 v27, v27, v83\n"
    "v_exp_f32 v24, -v24\n"
    "v_mul_f32 v80, v80, v88\n"
    "v_mfma_f32_32x32x16_bf16 v[112:127], v[52:55], v[152:155], v[112:127]\n"
    "s_add_u32 m0, s78, 0x19000\n"
    "s_add_u32 s47, s71, 0xa2800\n"
    "buffer_load_dwordx4 v0, s[24:27], s47 offen sc1 lds\n"
    "ds_read_b128 v[48:51], v1 offset:11264\n"
    "s_waitcnt lgkmcnt(3)\n"
    "v_mfma_f32_32x32x16_bf16 v[112:127], v[56:59], v[156:159], v[112:127]\n"
    "ds_read_b128 v[52:55], v1 offset:12288\n"
    "v_exp_f32 v25, -v25\n"
    "v_mul_f32 v81, v81, v89\n"
    "v_exp_f32 v26, -v26\n"
    "v_mul_f32 v82, v82, v90\n"
    "v_mfma_f32_32x32x16_bf16 v[112:127], v[60:63], v[160:163], v[112:127]\n"
    "buffer_load_dwordx4 v0, s[24:27], s47 offen offset:1024 sc1 lds\n"
    "ds_read_b128 v[56:59], v1 offset:13312\n"
    "v_exp_f32 v27, -v27\n"
    "v_mul_f32 v83, v83, v91\n"
    "s_waitcnt lgkmcnt(3)\n"
    "v_mfma_f32_32x32x16_bf16 v[112:127], v[40:43], v[164:167], v[112:127]\n"
    "ds_read_b128 v[60:63], v1 offset:14336\n"
    "v_fma_f32 v24, v24, v18, v18\n"
    "v_fma_f32 v25, v25, v18, v18\n"
    "v_fma_f32 v26, v26, v18, v18\n"
    "v_fma_f32 v27, v27, v18, v18\n"
    "v_rcp_f32 v24, v24\n"
    "v_mfma_f32_32x32x16_bf16 v[112:127], v[44:47], v[168:171], v[112:127]\n"
    "buffer_load_dwordx4 v0, s[24:27], s47 offen offset:2048 sc1 lds\n"
    "ds_read_b128 v[40:43], v1 offset:15360\n"
    "v_rcp_f32 v25, v25\n"
    "s_waitcnt lgkmcnt(3)\n"
    "v_mfma_f32_32x32x16_bf16 v[112:127], v[48:51], v[172:175], v[112:127]\n"
    "ds_read_b128 v[44:47], v1 offset:16384\n"
    "v_rcp_f32 v26, v26\n"
    "v_rcp_f32 v27, v27\n"
    "v_mul_f32 v80, v80, v24\n"
    "v_mul_f32 v81, v81, v25\n"
    "v_mfma_f32_32x32x16_bf16 v[112:127], v[52:55], v[176:179], v[112:127]\n"
    "buffer_load_dwordx4 v0, s[24:27], s47 offen offset:3072 sc1 lds\n"
    "ds_read_b128 v[48:51], v1 offset:17408\n"
    "v_mul_f32 v82, v82, v26\n"
    "v_mul_f32 v83, v83, v27\n"
    "v_cvt_pk_bf16_f32 v32, v80, v81\n"
    "s_waitcnt lgkmcnt(3)\n"
    "v_mfma_f32_32x32x16_bf16 v[112:127], v[56:59], v[180:183], v[112:127]\n"
    "ds_read_b128 v[52:55], v1 offset:18432\n"
    "v_cvt_pk_bf16_f32 v33, v82, v83\n"
    "v_mul_f32 v84, v17, v84\n"
    "v_mul_f32 v85, v17, v85\n"
    "v_mul_f32 v86, v17, v86\n"
    "v_mul_f32 v87, v17, v87\n"
    "v_fmaak_f32 v24, v84, v84, 0x409d30fc\n"
    "v_mfma_f32_32x32x16_bf16 v[112:127], v[60:63], v[184:187], v[112:127]\n"
    "s_add_u32 m0, s86, 0x600\n"
    "s_nop 0\n"
    "buffer_load_dword v3, s[24:27], s47 offen sc1 lds\n"
    "ds_read_b128 v[56:59], v1 offset:19456\n"
    "v_fmaak_f32 v25, v85, v85, 0x409d30fc\n"
    "v_fmaak_f32 v26, v86, v86, 0x409d30fc\n"
    "s_waitcnt lgkmcnt(3)\n"
    "v_mfma_f32_32x32x16_bf16 v[112:127], v[40:43], v[188:191], v[112:127]\n"
    "ds_read_b128 v[60:63], v1 offset:20480\n"
    "v_fmaak_f32 v27, v87, v87, 0x409d30fc\n"
    "v_mul_f32 v24, v24, v84\n"
    "v_mul_f32 v25, v25, v85\n"
    "v_mul_f32 v26, v26, v86\n"
    "v_mul_f32 v27, v27, v87\n"
    "v_mfma_f32_32x32x16_bf16 v[112:127], v[44:47], v[192:195], v[112:127]\n"
    "buffer_load_dwordx4 a[144:147], v0, s[24:27], s46 offen offset:2048 sc1\n"
    "ds_read_b128 v[40:43], v1 offset:21504\n"
    "v_exp_f32 v24, -v24\n"
    "v_mul_f32 v84, v84, v92\n"
    "s_waitcnt lgkmcnt(3)\n"
    "v_mfma_f32_32x32x16_bf16 v[112:127], v[48:51], v[196:199], v[112:127]\n"
    "ds_read_b128 v[44:47], v1 offset:22528\n"
    "v_exp_f32 v25, -v25\n"
    "v_mul_f32 v85, v85, v93\n"
    "v_exp_f32 v26, -v26\n"
    "v_mul_f32 v86, v86, v94\n"
    "v_mfma_f32_32x32x16_bf16 v[112:127], v[52:55], v[200:203], v[112:127]\n"
    "buffer_load_dwordx4 a[148:151], v0, s[24:27], s46 offen offset:3072 sc1\n"
    "ds_read_b128 v[48:51], v1 offset:23552\n"
    "v_exp_f32 v27, -v27\n"
    "v_mul_f32 v87, v87, v95\n"
    "s_waitcnt lgkmcnt(3)\n"
    "v_mfma_f32_32x32x16_bf16 v[112:127], v[56:59], v[204:207], v[112:127]\n"
    "ds_read_b128 v[52:55], v1 offset:24576\n"
    "v_fma_f32 v24, v24, v18, v18\n"
    "v_fma_f32 v25, v25, v18, v18\n"
    "v_fma_f32 v26, v26, v18, v18\n"
    "v_fma_f32 v27, v27, v18, v18\n"
    "v_rcp_f32 v24, v24\n"
    "v_mfma_f32_32x32x16_bf16 v[112:127], v[60:63], v[208:211], v[112:127]\n"
    "s_add_u32 s46, s84, 0x4000\n"
    "buffer_load_dwordx4 a[64:67], v0, s[24:27], s46 offen sc1\n"
    "ds_read_b128 v[56:59], v1 offset:25600\n"
    "v_rcp_f32 v25, v25\n"
    "s_waitcnt lgkmcnt(3)\n"
    "v_mfma_f32_32x32x16_bf16 v[112:127], v[40:43], v[212:215], v[112:127]\n"
    "ds_read_b128 v[60:63], v1 offset:26624\n"
    "v_rcp_f32 v26, v26\n"
    "v_rcp_f32 v27, v27\n"
    "v_mul_f32 v84, v84, v24\n"
    "v_mul_f32 v85, v85, v25\n"
    "v_mfma_f32_32x32x16_bf16 v[112:127], v[44:47], v[216:219], v[112:127]\n"
    "buffer_load_dwordx4 a[68:71], v0, s[24:27], s46 offen offset:1024 sc1\n"
    "ds_read_b128 v[40:43], v1 offset:27648\n"
    "v_mul_f32 v86, v86, v26\n"
    "v_mul_f32 v87, v87, v27\n"
    "v_cvt_pk_bf16_f32 v34, v84, v85\n"
    "s_waitcnt lgkmcnt(3)\n"
    "v_mfma_f32_32x32x16_bf16 v[112:127], v[48:51], v[220:223], v[112:127]\n"
    "ds_read_b128 v[44:47], v1 offset:28672\n"
    "v_cvt_pk_bf16_f32 v35, v86, v87\n"
    "s_add_u32 s44, s60, 0x1000\n"
    "buffer_store_dwordx4 v[32:35], v0, s[24:27], s44 offen sc1\n"
    "v_mul_f32 v96, v17, v96\n"
    "v_mul_f32 v97, v17, v97\n"
    "v_mfma_f32_32x32x16_bf16 v[112:127], v[52:55], v[224:227], v[112:127]\n"
    "buffer_load_dwordx4 a[72:75], v0, s[24:27], s46 offen offset:2048 sc1\n"
    "ds_read_b128 v[48:51], v1 offset:29696\n"
    "v_mul_f32 v98, v17, v98\n"
    "v_mul_f32 v99, v17, v99\n"
    "v_fmaak_f32 v24, v96, v96, 0x409d30fc\n"
    "s_waitcnt lgkmcnt(3)\n"
    "v_mfma_f32_32x32x16_bf16 v[112:127], v[56:59], v[228:231], v[112:127]\n"
    "ds_read_b128 v[52:55], v1 offset:30720\n"
    "v_fmaak_f32 v25, v97, v97, 0x409d30fc\n"
    "v_fmaak_f32 v26, v98, v98, 0x409d30fc\n"
    "v_fmaak_f32 v27, v99, v99, 0x409d30fc\n"
    "v_mul_f32 v24, v24, v96\n"
    "v_mul_f32 v25, v25, v97\n"
    "v_mul_f32 v26, v26, v98\n"
    "v_mfma_f32_32x32x16_bf16 v[112:127], v[60:63], v[232:235], v[112:127]\n"
    "buffer_load_dwordx4 a[76:79], v0, s[24:27], s46 offen offset:3072 sc1\n"
    "ds_read_b128 v[56:59], v1 offset:31744\n"
    "v_mul_f32 v27, v27, v99\n"
    "v_exp_f32 v24, -v24\n"
    "s_waitcnt lgkmcnt(3)\n"
    "v_mfma_f32_32x32x16_bf16 v[112:127], v[40:43], v[236:239], v[112:127]\n"
    "s_waitcnt vmcnt(17)\n"
    "s_barrier\n"
    "ds_read_b128 v[60:63], v4 offset:1024\n"
    "v_mul_f32 v96, v96, v104\n"
    "v_exp_f32 v25, -v25\n"
    "v_mul_f32 v97, v97, v105\n"
    "v_mfma_f32_32x32x16_bf16 v[112:127], v[44:47], v[240:243], v[112:127]\n"
    "s_add_u32 s46, s85, 0x4000\n"
    "buffer_load_dwordx4 a[152:155], v0, s[24:27], s46 offen sc1\n"
    "ds_read_b128 v[40:43], v0 offset:32768\n"
    "v_exp_f32 v26, -v26\n"
    "s_waitcnt lgkmcnt(3)\n"
    "v_mfma_f32_32x32x16_bf16 v[112:127], v[48:51], v[244:247], v[112:127]\n"
    "ds_read_b128 v[44:47], v0 offset:33792\n"
    "v_mul_f32 v98, v98, v106\n"
    "v_exp_f32 v27, -v27\n"
    "v_mul_f32 v99, v99, v107\n"
    "v_fma_f32 v24, v24, v18, v18\n"
    "v_fma_f32 v25, v25, v18, v18\n"
    "v_mfma_f32_32x32x16_bf16 v[112:127], v[52:55], v[248:251], v[112:127]\n"
    "buffer_load_dwordx4 a[156:159], v0, s[24:27], s46 offen offset:1024 sc1\n"
    "ds_read_b128 v[48:51], v0 offset:34816\n"
    "v_fma_f32 v26, v26, v18, v18\n"
    "v_fma_f32 v27, v27, v18, v18\n"
    "s_waitcnt lgkmcnt(3)\n"
    "v_mfma_f32_32x32x16_bf16 v[112:127], v[56:59], v[252:255], v[112:127]\n"
    "ds_read_b128 v[52:55], v0 offset:35840\n"
    "v_rcp_f32 v24, v24\n"
    "v_rcp_f32 v25, v25\n"
    "v_rcp_f32 v26, v26\n"
    "v_mfma_f32_32x32x16_bf16 v[112:127], v[60:63], v[20:23], v[112:127]\n"
    "buffer_load_dwordx4 a[160:163], v0, s[24:27], s46 offen offset:2048 sc1\n"
    "ds_read_b128 v[56:59], v0 offset:36864\n"
    "v_rcp_f32 v27, v27\n"
    "v_mul_f32 v96, v96, v24\n"
    "s_waitcnt lgkmcnt(3)\n"
    "v_mfma_f32_32x32x16_bf16 v[64:79], v[40:43], v[128:131], 0\n"
    "ds_read_b128 v[60:63], v0 offset:37888\n"
    "v_mul_f32 v97, v97, v25\n"
    "v_mul_f32 v98, v98, v26\n"
    "v_mul_f32 v99, v99, v27\n"
    "v_cvt_pk_bf16_f32 v28, v96, v97\n"
    "v_cvt_pk_bf16_f32 v29, v98, v99\n"
    "v_mul_f32 v100, v17, v100\n"
    "v_mfma_f32_32x32x16_bf16 v[64:79], v[44:47], v[132:135], v[64:79]\n"
    "buffer_load_dwordx4 a[164:167], v0, s[24:27], s46 offen offset:3072 sc1\n"
    "ds_read_b128 v[40:43], v0 offset:38912\n"
    "v_mul_f32 v101, v17, v101\n"
    "v_mul_f32 v102, v17, v102\n"
    "v_mul_f32 v103, v17, v103\n"
    "s_waitcnt lgkmcnt(3)\n"
    "v_mfma_f32_32x32x16_bf16 v[64:79], v[48:51], v[136:139], v[64:79]\n"
    "ds_read_b128 v[44:47], v0 offset:39936\n"
    "v_fmaak_f32 v24, v100, v100, 0x409d30fc\n"
    "v_fmaak_f32 v25, v101, v101, 0x409d30fc\n"
    "v_fmaak_f32 v26, v102, v102, 0x409d30fc\n"
    "v_fmaak_f32 v27, v103, v103, 0x409d30fc\n"
    "v_mul_f32 v24, v24, v100\n"
    "v_mfma_f32_32x32x16_bf16 v[64:79], v[52:55], v[140:143], v[64:79]\n"
    "s_add_u32 s46, s84, 0x5000\n"
    "buffer_load_dwordx4 a[80:83], v0, s[24:27], s46 offen sc1\n"
    "ds_read_b128 v[48:51], v0 offset:40960\n"
    "v_mul_f32 v25, v25, v101\n"
    "s_waitcnt lgkmcnt(3)\n"
    "v_mfma_f32_32x32x16_bf16 v[64:79], v[56:59], v[144:147], v[64:79]\n"
    "ds_read_b128 v[52:55], v0 offset:41984\n"
    "v_mul_f32 v26, v26, v102\n"
    "v_mul_f32 v27, v27, v103\n"
    "v_exp_f32 v24, -v24\n"
    "v_mul_f32 v100, v100, v108\n"
    "v_mfma_f32_32x32x16_bf16 v[64:79], v[60:63], v[148:151], v[64:79]\n"
    "buffer_load_dwordx4 a[84:87], v0, s[24:27], s46 offen offset:1024 sc1\n"
    "ds_read_b128 v[56:59], v0 offset:43008\n"
    "v_exp_f32 v25, -v25\n"
    "s_waitcnt lgkmcnt(3)\n"
    "v_mfma_f32_32x32x16_bf16 v[64:79], v[40:43], v[152:155], v[64:79]\n"
    "ds_read_b128 v[60:63], v0 offset:44032\n"
    "v_mul_f32 v101, v101, v109\n"
    "v_exp_f32 v26, -v26\n"
    "v_mul_f32 v102, v102, v110\n"
    "v_mfma_f32_32x32x16_bf16 v[64:79], v[44:47], v[156:159], v[64:79]\n"
    "s_add_u32 s46, s85, 0x5000\n"
    "buffer_load_dwordx4 a[168:171], v0, s[24:27], s46 offen sc1\n"
    "ds_read_b128 v[40:43], v0 offset:45056\n"
    "s_waitcnt lgkmcnt(3)\n"
    "v_mfma_f32_32x32x16_bf16 v[64:79], v[48:51], v[160:163], v[64:79]\n"
    "s_waitcnt vmcnt(9)\n"
    "s_barrier\n"
    "v_lshlrev_b32 v36, 3, v0\n"
    "v_mov_b32 v37, 1\n"
    "s_mov_b64 exec, s[18:19]\n"
    "buffer_atomic_add v37, v36, s[24:27], s88 offen offset:12 sc1\n"
    "s_mov_b64 exec, -1\n"
    "ds_read_b128 v[44:47], v0 offset:46080\n"
    "v_mfma_f32_32x32x16_bf16 v[64:79], v[52:55], v[164:167], v[64:79]\n"
    "buffer_load_dwordx4 a[172:175], v0, s[24:27], s46 offen offset:1024 sc1\n"
    "ds_read_b128 v[48:51], v0 offset:47104\n"
    "v_exp_f32 v27, -v27\n"
    "s_waitcnt lgkmcnt(3)\n"
    "v_mfma_f32_32x32x16_bf16 v[64:79], v[56:59], v[168:171], v[64:79]\n"
    "ds_read_b128 v[52:55], v0 offset:48128\n"
    "v_mul_f32 v103, v103, v111\n"
    "v_fma_f32 v24, v24, v18, v18\n"
    "v_fma_f32 v25, v25, v18, v18\n"
    "v_fma_f32 v26, v26, v18, v18\n"
    "v_fma_f32 v27, v27, v18, v18\n"
    "v_mfma_f32_32x32x16_bf16 v[64:79], v[60:63], v[172:175], v[64:79]\n"
    "s_add_u32 m0, s78, 0x0\n"
    "s_add_u32 s47, s66, 0x82000\n"
    "buffer_load_dwordx4 v0, s[24:27], s47 offen sc1 lds\n"
    "ds_read_b128 v[56:59], v0 offset:49152\n"
    "s_waitcnt lgkmcnt(3)\n"
    "v_mfma_f32_32x32x16_bf16 v[64:79], v[40:43], v[176:179], v[64:79]\n"
    "ds_read_b128 v[60:63], v0 offset:50176\n"
    "v_rcp_f32 v24, v24\n"
    "v_rcp_f32 v25, v25\n"
    "v_mfma_f32_32x32x16_bf16 v[64:79], v[44:47], v[180:183], v[64:79]\n"
    "buffer_load_dwordx4 v0, s[24:27], s47 offen offset:1024 sc1 lds\n"
    "ds_read_b128 v[40:43], v0 offset:51200\n"
    "v_rcp_f32 v26, v26\n"
    "s_waitcnt lgkmcnt(3)\n"
    "v_mfma_f32_32x32x16_bf16 v[64:79], v[48:51], v[184:187], v[64:79]\n"
    "ds_read_b128 v[44:47], v0 offset:52224\n"
    "v_rcp_f32 v27, v27\n"
    "v_mul_f32 v100, v100, v24\n"
    "v_mul_f32 v101, v101, v25\n"
    "v_mul_f32 v102, v102, v26\n"
    "v_mfma_f32_32x32x16_bf16 v[64:79], v[52:55], v[188:191], v[64:79]\n"
    "buffer_load_dwordx4 v0, s[24:27], s47 offen offset:2048 sc1 lds\n"
    "ds_read_b128 v[48:51], v0 offset:53248\n"
    "v_mul_f32 v103, v103, v27\n"
    "v_cvt_pk_bf16_f32 v30, v100, v101\n"
    "s_waitcnt lgkmcnt(3)\n"
    "v_mfma_f32_32x32x16_bf16 v[64:79], v[56:59], v[192:195], v[64:79]\n"
    "ds_read_b128 v[52:55], v0 offset:54272\n"
    "v_cvt_pk_bf16_f32 v31, v102, v103\n"
    "s_add_u32 s44, s60, 0x2000\n"
    "buffer_store_dwordx4 v[28:31], v0, s[24:27], s44 offen sc1\n"
    "v_mfma_f32_32x32x16_bf16 v[64:79], v[60:63], v[196:199], v[64:79]\n"
    "buffer_load_dwordx4 v0, s[24:27], s47 offen offset:3072 sc1 lds\n"
    "ds_read_b128 v[56:59], v0 offset:55296\n"
    "v_mul_f32 v112, v17, v112\n"
    "v_mul_f32 v113, v17, v113\n"
    "s_waitcnt lgkmcnt(3)\n"
    "v_mfma_f32_32x32x16_bf16 v[64:79], v[40:43], v[200:203], v[64:79]\n"
    "ds_read_b128 v[60:63], v0 offset:56320\n"
    "v_mul_f32 v114, v17, v114\n"
    "v_mul_f32 v115, v17, v115\n"
    "v_fmaak_f32 v24, v112, v112, 0x409d30fc\n"
    "v_fmaak_f32 v25, v113, v113, 0x409d30fc\n"
    "v_fmaak_f32 v26, v114, v114, 0x409d30fc\n"
    "v_mfma_f32_32x32x16_bf16 v[64:79], v[44:47], v[204:207], v[64:79]\n"
    "s_add_u32 m0, s78, 0x1000\n"
    "s_add_u32 s47, s67, 0x82000\n"
    "buffer_load_dwordx4 v0, s[24:27], s47 offen sc1 lds\n"
    "ds_read_b128 v[40:43], v0 offset:57344\n"
    "s_waitcnt lgkmcnt(3)\n"
    "v_mfma_f32_32x32x16_bf16 v[64:79], v[48:51], v[208:211], v[64:79]\n"
    "v_mov_b32 v36, 0\n"
    "s_mov_b64 exec, s[16:17]\n"
    "buffer_atomic_add v36, off, s[24:27], s92 offset:12 sc0 sc1\n"
    "s_mov_b64 exec, -1\n"
    "ds_read_b128 v[44:47], v0 offset:58368\n"
    "v_mfma_f32_32x32x16_bf16 v[64:79], v[52:55], v[212:215], v[64:79]\n"
    "buffer_load_dwordx4 v0, s[24:27], s47 offen offset:1024 sc1 lds\n"
    "ds_read_b128 v[48:51], v0 offset:59392\n"
    "v_fmaak_f32 v27, v115, v115, 0x409d30fc\n"
    "v_mul_f32 v24, v24, v112\n"
    "s_waitcnt lgkmcnt(3)\n"
    "v_mfma_f32_32x32x16_bf16 v[64:79], v[56:59], v[216:219], v[64:79]\n"
    "ds_read_b128 v[52:55], v0 offset:60416\n"
    "v_mul_f32 v25, v25, v113\n"
    "v_mul_f32 v26, v26, v114\n"
    "v_mul_f32 v27, v27, v115\n"
    "v_exp_f32 v24, -v24\n"
    "v_mfma_f32_32x32x16_bf16 v[64:79], v[60:63], v[220:223], v[64:79]\n"
    "buffer_load_dwordx4 v0, s[24:27], s47 offen offset:2048 sc1 lds\n"
    "ds_read_b128 v[56:59], v0 offset:61440\n"
    "v_mul_f32 v112, v112, v120\n"
    "s_waitcnt lgkmcnt(3)\n"
    "v_mfma_f32_32x32x16_bf16 v[64:79], v[40:43], v[224:227], v[64:79]\n"
    "ds_read_b128 v[60:63], v0 offset:62464\n"
    "v_exp_f32 v25, -v25\n"
    "v_mul_f32 v113, v113, v121\n"
    "v_exp_f32 v26, -v26\n"
    "v_mfma_f32_32x32x16_bf16 v[64:79], v[44:47], v[228:231], v[64:79]\n"
    "buffer_load_dwordx4 v0, s[24:27], s47 offen offset:3072 sc1 lds\n"
    "ds_read_b128 v[40:43], v0 offset:63488\n"
    "v_mul_f32 v114, v114, v122\n"
    "s_waitcnt lgkmcnt(3)\n"
    "v_mfma_f32_32x32x16_bf16 v[64:79], v[48:51], v[232:235], v[64:79]\n"
    "ds_read_b128 v[44:47], v0 offset:64512\n"
    "v_exp_f32 v27, -v27\n"
    "v_mul_f32 v115, v115, v123\n"
    "v_fma_f32 v24, v24, v18, v18\n"
    "v_fma_f32 v25, v25, v18, v18\n"
    "v_mfma_f32_32x32x16_bf16 v[64:79], v[52:55], v[236:239], v[64:79]\n"
    "s_barrier\n"
    "s_add_u32 m0, s86, 0x0\n"
    "s_nop 0\n"
    "buffer_load_dword v3, s[24:27], s47 offen sc1 lds\n"
    "ds_read_b128 v[48:51], v4 offset:512\n"
    "s_waitcnt lgkmcnt(3)\n"
    "v_mfma_f32_32x32x16_bf16 v[64:79], v[56:59], v[240:243], v[64:79]\n"
    "ds_read_b128 v[52:55], v1 offset:32768\n"
    "v_fma_f32 v26, v26, v18, v18\n"
    "v_fma_f32 v27, v27, v18, v18\n"
    "v_rcp_f32 v24, v24\n"
    "v_mfma_f32_32x32x16_bf16 v[64:79], v[60:63], v[244:247], v[64:79]\n"
    "ds_read_b128 v[56:59], v1 offset:33792\n"
    "v_rcp_f32 v25, v25\n"
    "v_rcp_f32 v26, v26\n"
    "s_waitcnt lgkmcnt(3)\n"
    "v_mfma_f32_32x32x16_bf16 v[64:79], v[40:43], v[248:251], v[64:79]\n"
    "ds_read_b128 v[60:63], v1 offset:34816\n"
    "v_rcp_f32 v27, v27\n"
    "v_mul_f32 v112, v112, v24\n"
    "v_mul_f32 v113, v113, v25\n"
    "v_mul_f32 v114, v114, v26\n"
    "v_mfma_f32_32x32x16_bf16 v[64:79], v[44:47], v[252:255], v[64:79]\n"
    "ds_read_b128 v[40:43], v1 offset:35840\n"
    "v_mul_f32 v115, v115, v27\n"
    "v_cvt_pk_bf16_f32 v32, v112, v113\n"
    "v_cvt_pk_bf16_f32 v33, v114, v115\n"
    "v_mul_f32 v116, v17, v116\n"
    "s_waitcnt lgkmcnt(3)\n"
    "v_mfma_f32_32x32x16_bf16 v[64:79], v[48:51], v[20:23], v[64:79]\n"
    "ds_read_b128 v[44:47], v1 offset:36864\n"
    "v_mul_f32 v117, v17, v117\n"
    "v_mul_f32 v118, v17, v118\n"
    "v_mul_f32 v119, v17, v119\n"
    "v_fmaak_f32 v24, v116, v116, 0x409d30fc\n"
    "v_fmaak_f32 v25, v117, v117, 0x409d30fc\n"
    "v_mfma_f32_32x32x16_bf16 v[80:95], v[52:55], v[128:131], 0\n"
    "ds_read_b128 v[48:51], v1 offset:37888\n"
    "v_fmaak_f32 v26, v118, v118, 0x409d30fc\n"
    "v_fmaak_f32 v27, v119, v119, 0x409d30fc\n"
    "v_mul_f32 v24, v24, v116\n"
    "v_mul_f32 v25, v25, v117\n"
    "s_waitcnt lgkmcnt(3)\n"
    "v_mfma_f32_32x32x16_bf16 v[80:95], v[56:59], v[132:135], v[80:95]\n"
    "ds_read_b128 v[52:55], v1 offset:38912\n"
    "v_mul_f32 v26, v26, v118\n"
    "v_mul_f32 v27, v27, v119\n"
    "v_exp_f32 v24, -v24\n"
    "v_mul_f32 v116, v116, v124\n"
    "v_mfma_f32_32x32x16_bf16 v[80:95], v[60:63], v[136:139], v[80:95]\n"
    "ds_read_b128 v[56:59], v1 offset:39936\n"
    "v_exp_f32 v25, -v25\n"
    "v_mul_f32 v117, v117, v125\n"
    "s_waitcnt lgkmcnt(3)\n"
    "v_mfma_f32_32x32x16_bf16 v[80:95], v[40:43], v[140:143], v[80:95]\n"
    "ds_read_b128 v[60:63], v1 offset:40960\n"
    "s_cmp_lg_u32 s37, 0\n"
    "s_cbranch_scc1 Lhs169_%=\n"
    "s_mov_b64 exec, 1\n"
    "v_mov_b32 v37, 0\n"
    "buffer_atomic_add v37, off, s[24:27], s92 offset:12 sc0 sc1\n"
    "s_waitcnt vmcnt(5)\n"
    "v_cmp_eq_u32 vcc, 16, v36\n"
    "s_cbranch_vccnz Lhd168_%=\n"
    "v_mov_b32 v36, 0\n"
    "buffer_atomic_add v36, off, s[24:27], s92 offset:12 sc0 sc1\n"
    "Lhp167_%=:\n"
    "s_waitcnt vmcnt(1)\n"
    "v_cmp_eq_u32 vcc, 16, v37\n"
    "s_cbranch_vccnz Lhd168_%=\n"
    "v_mov_b32 v37, 0\n"
    "buffer_atomic_add v37, off, s[24:27], s92 offset:12 sc0 sc1\n"
    "s_waitcnt vmcnt(1)\n"
    "v_cmp_eq_u32 vcc, 16, v36\n"
    "s_cbranch_vccnz Lhd168_%=\n"
    "v_mov_b32 v36, 0\n"
    "buffer_atomic_add v36, off, s[24:27], s92 offset:12 sc0 sc1\n"
    "s_sub_u32 s50, s50, 1\n"
    "s_cbranch_scc0 Lhp167_%=\n"
    "s_branch Lfail_%=\n"
    "Lhd168_%=:\n"
    "s_mov_b64 exec, -1\n"
    "Lhs169_%=:\n"
    "s_barrier\n"
    "v_mfma_f32_32x32x16_bf16 v[80:95], v[44:47], v[144:147], v[80:95]\n"
    "buffer_load_dwordx4 v[128:131], v0, s[24:27], s63 offen sc1\n"
    "ds_read_b128 v[40:43], v1 offset:41984\n"
    "v_exp_f32 v26, -v26\n"
    "s_waitcnt lgkmcnt(3)\n"
    "v_mfma_f32_32x32x16_bf16 v[80:95], v[48:51], v[148:151], v[80:95]\n"
    "ds_read_b128 v[44:47], v1 offset:43008\n"
    "v_mul_f32 v118, v118, v126\n"
    "v_exp_f32 v27, -v27\n"
    "v_mul_f32 v119, v119, v127\n"
    "v_fma_f32 v24, v24, v18, v18\n"
    "v_mfma_f32_32x32x16_bf16 v[80:95], v[52:55], v[152:155], v[80:95]\n"
    "buffer_load_dwordx4 v[132:135], v0, s[24:27], s63 offen offset:1024 sc1\n"
    "ds_read_b128 v[48:51], v1 offset:44032\n"
    "v_fma_f32 v25, v25, v18, v18\n"
    "v_fma_f32 v26, v26, v18, v18\n"
    "s_waitcnt lgkmcnt(3)\n"
    "v_mfma_f32_32x32x16_bf16 v[80:95], v[56:59], v[156:159], v[80:95]\n"
    "ds_read_b128 v[52:55], v1 offset:45056\n"
    "v_fma_f32 v27, v27, v18, v18\n"
    "v_rcp_f32 v24, v24\n"
    "v_rcp_f32 v25, v25\n"
    "v_mfma_f32_32x32x16_bf16 v[80:95], v[60:63], v[160:163], v[80:95]\n"
    "buffer_load_dwordx4 v[136:139], v0, s[24:27], s63 offen offset:2048 sc1\n"
    "ds_read_b128 v[56:59], v1 offset:46080\n"
    "v_rcp_f32 v26, v26\n"
    "s_waitcnt lgkmcnt(3)\n"
    "v_mfma_f32_32x32x16_bf16 v[80:95], v[40:43], v[164:167], v[80:95]\n"
    "ds_read_b128 v[60:63], v1 offset:47104\n"
    "v_rcp_f32 v27, v27\n"
    "v_mul_f32 v116, v116, v24\n"
    "v_mul_f32 v117, v117, v25\n"
    "v_mul_f32 v118, v118, v26\n"
    "v_mfma_f32_32x32x16_bf16 v[80:95], v[44:47], v[168:171], v[80:95]\n"
    "buffer_load_dwordx4 v[140:143], v0, s[24:27], s63 offen offset:3072 sc1\n"
    "ds_read_b128 v[40:43], v1 offset:48128\n"
    "v_mul_f32 v119, v119, v27\n"
    "v_cvt_pk_bf16_f32 v34, v116, v117\n"
    "s_waitcnt lgkmcnt(3)\n"
    "v_mfma_f32_32x32x16_bf16 v[80:95], v[48:51], v[172:175], v[80:95]\n"
    "ds_read_b128 v[44:47], v1 offset:49152\n"
    "v_cvt_pk_bf16_f32 v35, v118, v119\n"
    "s_add_u32 s44, s60, 0x3000\n"
    "buffer_store_dwordx4 v[32:35], v0, s[24:27], s44 offen sc1\n"
    "v_mul_f32 v64, v17, v64\n"
    "v_mfma_f32_32x32x16_bf16 v[80:95], v[52:55], v[176:179], v[80:95]\n"
    "s_add_u32 s42, s63, 0x1000\n"
    "buffer_load_dwordx4 v[144:147], v0, s[24:27], s42 offen sc1\n"
    "ds_read_b128 v[48:51], v1 offset:50176\n"
    "v_mul_f32 v65, v17, v65\n"
    "s_waitcnt lgkmcnt(3)\n"
    "v_mfma_f32_32x32x16_bf16 v[80:95], v[56:59], v[180:183], v[80:95]\n"
    "ds_read_b128 v[52:55], v1 offset:51200\n"
    "v_mul_f32 v66, v17, v66\n"
    "v_mul_f32 v67, v17, v67\n"
    "v_fmaak_f32 v24, v64, v64, 0x409d30fc\n"
    "v_fmaak_f32 v25, v65, v65, 0x409d30fc\n"
    "v_fmaak_f32 v26, v66, v66, 0x409d30fc\n"
    "v_mfma_f32_32x32x16_bf16 v[80:95], v[60:63], v[184:187], v[80:95]\n"
    "buffer_load_dwordx4 v[148:151], v0, s[24:27], s42 offen offset:1024 sc1\n"
    "ds_read_b128 v[56:59], v1 offset:52224\n"
    "v_fmaak_f32 v27, v67, v67, 0x409d30fc\n"
    "v_mul_f32 v24, v24, v64\n"
    "s_waitcnt lgkmcnt(3)\n"
    "v_mfma_f32_32x32x16_bf16 v[80:95], v[40:43], v[188:191], v[80:95]\n"
    "ds_read_b128 v[60:63], v1 offset:53248\n"
    "v_mul_f32 v25, v25, v65\n"
    "v_mul_f32 v26, v26, v66\n"
    "v_mul_f32 v27, v27, v67\n"
    "v_exp_f32 v24, -v24\n"
    "v_mfma_f32_32x32x16_bf16 v[80:95], v[44:47], v[192:195], v[80:95]\n"
    "buffer_load_dwordx4 v[152:155], v0, s[24:27], s42 offen offset:2048 sc1\n"
    "ds_read_b128 v[40:43], v1 offset:54272\n"
    "v_mul_f32 v64, v64, v72\n"
    "s_waitcnt lgkmcnt(3)\n"
    "v_mfma_f32_32x32x16_bf16 v[80:95], v[48:51], v[196:199], v[80:95]\n"
    "ds_read_b128 v[44:47], v1 offset:55296\n"
    "v_exp_f32 v25, -v25\n"
    "v_mul_f32 v65, v65, v73\n"
    "v_exp_f32 v26, -v26\n"
    "v_mfma_f32_32x32x16_bf16 v[80:95], v[52:55], v[200:203], v[80:95]\n"
    "buffer_load_dwordx4 v[156:159], v0, s[24:27], s42 offen offset:3072 sc1\n"
    "ds_read_b128 v[48:51], v1 offset:56320\n"
    "v_mul_f32 v66, v66, v74\n"
    "s_waitcnt lgkmcnt(3)\n"
    "v_mfma_f32_32x32x16_bf16 v[80:95], v[56:59], v[204:207], v[80:95]\n"
    "ds_read_b128 v[52:55], v1 offset:57344\n"
    "v_exp_f32 v27, -v27\n"
    "v_mul_f32 v67, v67, v75\n"
    "v_fma_f32 v24, v24, v18, v18\n"
    "v_fma_f32 v25, v25, v18, v18\n"
    "v_mfma_f32_32x32x16_bf16 v[80:95], v[60:63], v[208:211], v[80:95]\n"
    "buffer_load_dwordx4 v[160:163], v0, s[24:27], s62 offen sc1\n"
    "ds_read_b128 v[56:59], v1 offset:58368\n"
    "v_fma_f32 v26, v26, v18, v18\n"
    "v_fma_f32 v27, v27, v18, v18\n"
    "s_waitcnt lgkmcnt(3)\n"
    "v_mfma_f32_32x32x16_bf16 v[80:95], v[40:43], v[212:215], v[80:95]\n"
    "ds_read_b128 v[60:63], v1 offset:59392\n"
    "v_rcp_f32 v24, v24\n"
    "v_rcp_f32 v25, v25\n"
    "v_mfma_f32_32x32x16_bf16 v[80:95], v[44:47], v[216:219], v[80:95]\n"
    "buffer_load_dwordx4 v[164:167], v0, s[24:27], s62 offen offset:1024 sc1\n"
    "ds_read_b128 v[40:43], v1 offset:60416\n"
    "v_rcp_f32 v26, v26\n"
    "s_waitcnt lgkmcnt(3)\n"
    "v_mfma_f32_32x32x16_bf16 v[80:95], v[48:51], v[220:223], v[80:95]\n"
    "ds_read_b128 v[44:47], v1 offset:61440\n"
    "v_rcp_f32 v27, v27\n"
    "v_mul_f32 v64, v64, v24\n"
    "v_mul_f32 v65, v65, v25\n"
    "v_mul_f32 v66, v66, v26\n"
    "v_mfma_f32_32x32x16_bf16 v[80:95], v[52:55], v[224:227], v[80:95]\n"
    "buffer_load_dwordx4 v[168:171], v0, s[24:27], s62 offen offset:2048 sc1\n"
    "ds_read_b128 v[48:51], v1 offset:62464\n"
    "v_mul_f32 v67, v67, v27\n"
    "v_cvt_pk_bf16_f32 v28, v64, v65\n"
    "s_waitcnt lgkmcnt(3)\n"
    "v_mfma_f32_32x32x16_bf16 v[80:95], v[56:59], v[228:231], v[80:95]\n"
    "ds_read_b128 v[52:55], v1 offset:63488\n"
    "v_cvt_pk_bf16_f32 v29, v66, v67\n"
    "v_mul_f32 v68, v17, v68\n"
    "v_mul_f32 v69, v17, v69\n"
    "v_mul_f32 v70, v17, v70\n"
    "v_mul_f32 v71, v17, v71\n"
    "v_mfma_f32_32x32x16_bf16 v[80:95], v[60:63], v[232:235], v[80:95]\n"
    "buffer_load_dwordx4 v[172:175], v0, s[24:27], s62 offen offset:3072 sc1\n"
    "ds_read_b128 v[56:59], v1 offset:64512\n"
    "v_fmaak_f32 v24, v68, v68, 0x409d30fc\n"
    "v_fmaak_f32 v25, v69, v69, 0x409d30fc\n"
    "s_waitcnt lgkmcnt(3)\n"
    "v_mfma_f32_32x32x16_bf16 v[80:95], v[40:43], v[236:239], v[80:95]\n"
    "ds_read_b128 v[60:63], v4 offset:1536\n"
    "v_fmaak_f32 v26, v70, v70, 0x409d30fc\n"
    "v_fmaak_f32 v27, v71, v71, 0x409d30fc\n"
    "v_mul_f32 v24, v24, v68\n"
    "v_mul_f32 v25, v25, v69\n"
    "v_mul_f32 v26, v26, v70\n"
    "v_mfma_f32_32x32x16_bf16 v[80:95], v[44:47], v[240:243], v[80:95]\n"
    "s_add_u32 s42, s62, 0x1000\n"
    "buffer_load_dwordx4 v[176:179], v0, s[24:27], s42 offen sc1\n"
    "v_mul_f32 v27, v27, v71\n"
    "s_waitcnt lgkmcnt(2)\n"
    "v_mfma_f32_32x32x16_bf16 v[80:95], v[48:51], v[244:247], v[80:95]\n"
    "v_exp_f32 v24, -v24\n"
    "v_mul_f32 v68, v68, v76\n"
    "v_exp_f32 v25, -v25\n"
    "v_mul_f32 v69, v69, v77\n"
    "v_mfma_f32_32x32x16_bf16 v[80:95], v[52:55], v[248:251], v[80:95]\n"
    "buffer_load_dwordx4 v[180:183], v0, s[24:27], s42 offen offset:1024 sc1\n"
    "v_exp_f32 v26, -v26\n"
    "v_mul_f32 v70, v70, v78\n"
    "s_waitcnt lgkmcnt(0)\n"
    "v_mfma_f32_32x32x16_bf16 v[80:95], v[56:59], v[252:255], v[80:95]\n"
    "v_exp_f32 v27, -v27\n"
    "v_mul_f32 v71, v71, v79\n"
    "v_fma_f32 v24, v24, v18, v18\n"
    "v_fma_f32 v25, v25, v18, v18\n"
    "v_fma_f32 v26, v26, v18, v18\n"
    "v_mfma_f32_32x32x16_bf16 v[80:95], v[60:63], v[20:23], v[80:95]\n"
    "buffer_load_dwordx4 v[184:187], v0, s[24:27], s42 offen offset:2048 sc1\n"
    "v_fma_f32 v27, v27, v18, v18\n"
    "v_rcp_f32 v24, v24\n"
    "s_barrier\n"
    "s_waitcnt vmcnt(14)\n"
    "v_mfma_f32_32x32x16_bf16 v[112:127], a[88:91], v[128:131], 0\n"
    "v_rcp_f32 v25, v25\n"
    "v_rcp_f32 v26, v26\n"
    "v_rcp_f32 v27, v27\n"
    "v_mul_f32 v68, v68, v24\n"
    "v_mfma_f32_32x32x16_bf16 v[96:111], a[0:3], v[128:131], 0\n"
    "buffer_load_dwordx4 v[188:191], v0, s[24:27], s42 offen offset:3072 sc1\n"
    "v_mul_f32 v69, v69, v25\n"
    "v_mul_f32 v70, v70, v26\n"
    "v_mul_f32 v71, v71, v27\n"
    "v_cvt_pk_bf16_f32 v30, v68, v69\n"
    "v_cvt_pk_bf16_f32 v31, v70, v71\n"
    "v_mfma_f32_32x32x16_bf16 v[112:127], a[92:95], v[132:135], v[112:127]\n"
    "s_add_u32 s44, s60, 0x4000\n"
    "buffer_store_dwordx4 v[28:31], v0, s[24:27], s44 offen sc1\n"
    "v_mul_f32 v80, v17, v80\n"
    "v_mul_f32 v81, v17, v81\n"
    "v_mul_f32 v82, v17, v82\n"
    "v_mul_f32 v83, v17, v83\n"
    "v_mfma_f32_32x32x16_bf16 v[96:111], a[4:7], v[132:135], v[96:111]\n"
    "s_add_u32 s42, s63, 0x2000\n"
    "buffer_load_dwordx4 v[192:195], v0, s[24:27], s42 offen sc1\n"
    "v_fmaak_f32 v24, v80, v80, 0x409d30fc\n"
    "v_fmaak_f32 v25, v81, v81, 0x409d30fc\n"
    "v_fmaak_f32 v26, v82, v82, 0x409d30fc\n"
    "s_waitcnt vmcnt(15)\n"
    "v_mfma_f32_32x32x16_bf16 v[112:127], a[96:99], v[136:139], v[112:127]\n"
    "v_fmaak_f32 v27, v83, v83, 0x409d30fc\n"
    "v_mul_f32 v24, v24, v80\n"
    "v_mul_f32 v25, v25, v81\n"
    "v_mul_f32 v26, v26, v82\n"
    "v_mul_f32 v27, v27, v83\n"
    "v_exp_f32 v24, -v24\n"
    "v_mfma_f32_32x32x16_bf16 v[96:111], a[8:11], v[136:139], v[96:111]\n"
    "buffer_load_dwordx4 v[196:199], v0, s[24:27], s42 offen offset:1024 sc1\n"
    "v_mul_f32 v80, v80, v88\n"
    "v_exp_f32 v25, -v25\n"
    "v_mul_f32 v81, v81, v89\n"
    "v_mfma_f32_32x32x16_bf16 v[112:127], a[100:103], v[140:143], v[112:127]\n"
    "v_exp_f32 v26, -v26\n"
    "v_mul_f32 v82, v82, v90\n"
    "v_exp_f32 v27, -v27\n"
    "v_mul_f32 v83, v83, v91\n"
    "v_fma_f32 v24, v24, v18, v18\n"
    "v_mfma_f32_32x32x16_bf16 v[96:111], a[12:15], v[140:143], v[96:111]\n"
    "buffer_load_dwordx4 v[200:203], v0, s[24:27], s42 offen offset:2048 sc1\n"
    "v_fma_f32 v25, v25, v18, v18\n"
    "v_fma_f32 v26, v26, v18, v18\n"
    "v_fma_f32 v27, v27, v18, v18\n"
    "s_waitcnt vmcnt(14)\n"
    "v_mfma_f32_32x32x16_bf16 v[112:127], a[104:107], v[144:147], v[112:127]\n"
    "v_rcp_f32 v24, v24\n"
    "v_rcp_f32 v25, v25\n"
    "v_rcp_f32 v26, v26\n"
    "v_mfma_f32_32x32x16_bf16 v[96:111], a[16:19], v[144:147], v[96:111]\n"
    "buffer_load_dwordx4 v[204:207], v0, s[24:27], s42 offen offset:3072 sc1\n"
    "v_rcp_f32 v27, v27\n"
    "v_mul_f32 v80, v80, v24\n"
    "v_mul_f32 v81, v81, v25\n"
    "v_mul_f32 v82, v82, v26\n"
    "v_mfma_f32_32x32x16_bf16 v[112:127], a[108:111], v[148:151], v[112:127]\n"
    "v_mul_f32 v83, v83, v27\n"
    "v_cvt_pk_bf16_f32 v32, v80, v81\n"
    "v_cvt_pk_bf16_f32 v33, v82, v83\n"
    "v_mul_f32 v84, v17, v84\n"
    "v_mul_f32 v85, v17, v85\n"
    "v_mul_f32 v86, v17, v86\n"
    "v_mul_f32 v87, v17, v87\n"
    "v_mfma_f32_32x32x16_bf16 v[96:111], a[20:23], v[148:151], v[96:111]\n"
    "s_add_u32 s42, s62, 0x2000\n"
    "buffer_load_dwordx4 v[208:211], v0, s[24:27], s42 offen sc1\n"
    "v_fmaak_f32 v24, v84, v84, 0x409d30fc\n"
    "v_fmaak_f32 v25, v85, v85, 0x409d30fc\n"
    "v_fmaak_f32 v26, v86, v86, 0x409d30fc\n"
    "s_waitcnt vmcnt(14)\n"
    "v_mfma_f32_32x32x16_bf16 v[112:127], a[112:115], v[152:155], v[112:127]\n"
    "v_fmaak_f32 v27, v87, v87, 0x409d30fc\n"
    "v_mul_f32 v24, v24, v84\n"
    "v_mul_f32 v25, v25, v85\n"
    "v_mul_f32 v26, v26, v86\n"
    "v_mul_f32 v27, v27, v87\n"
    "v_exp_f32 v24, -v24\n"
    "v_mfma_f32_32x32x16_bf16 v[96:111], a[24:27], v[152:155], v[96:111]\n"
    "buffer_load_dwordx4 v[212:215], v0, s[24:27], s42 offen offset:1024 sc1\n"
    "v_mul_f32 v84, v84, v92\n"
    "v_exp_f32 v25, -v25\n"
    "v_mul_f32 v85, v85, v93\n"
    "v_mfma_f32_32x32x16_bf16 v[112:127], a[116:119], v[156:159], v[112:127]\n"
    "v_exp_f32 v26, -v26\n"
    "v_mul_f32 v86, v86, v94\n"
    "v_exp_f32 v27, -v27\n"
    "v_mul_f32 v87, v87, v95\n"
    "v_fma_f32 v24, v24, v18, v18\n"
    "v_mfma_f32_32x32x16_bf16 v[96:111], a[28:31], v[156:159], v[96:111]\n"
    "buffer_load_dwordx4 v[216:219], v0, s[24:27], s42 offen offset:2048 sc1\n"
    "v_fma_f32 v25, v25, v18, v18\n"
    "v_fma_f32 v26, v26, v18, v18\n"
    "v_fma_f32 v27, v27, v18, v18\n"
    "s_waitcnt vmcnt(14)\n"
    "v_mfma_f32_32x32x16_bf16 v[64:79], a[88:91], v[160:163], 0\n"
    "v_rcp_f32 v24, v24\n"
    "v_rcp_f32 v25, v25\n"
    "v_rcp_f32 v26, v26\n"
    "v_mfma_f32_32x32x16_bf16 v[40:55], a[0:3], v[160:163], 0\n"
    "buffer_load_dwordx4 v[220:223], v0, s[24:27], s42 offen offset:3072 sc1\n"
    "v_rcp_f32 v27, v27\n"
    "v_mul_f32 v84, v84, v24\n"
    "v_mul_f32 v85, v85, v25\n"
    "v_mul_f32 v86, v86, v26\n"
    "v_mfma_f32_32x32x16_bf16 v[64:79], a[92:95], v[164:167], v[64:79]\n"
    "v_mul_f32 v87, v87, v27\n"
    "v_cvt_pk_bf16_f32 v34, v84, v85\n"
    "v_cvt_pk_bf16_f32 v35, v86, v87\n"
    "s_add_u32 s44, s61, 0x5000\n"
    "buffer_store_dwordx4 v[32:35], v0, s[24:27], s44 offen sc1\n"
    "v_accvgpr_read_b32 v82, a248\n"
    "v_mfma_f32_32x32x16_bf16 v[40:55], a[4:7], v[164:167], v[40:55]\n"
    "s_add_u32 s42, s63, 0x3000\n"
    "buffer_load_dwordx4 v[224:227], v0, s[24:27], s42 offen sc1\n"
    "v_accvgpr_read_b32 v86, a249\n"
    "v_accvgpr_read_b32 v90, a250\n"
    "v_accvgpr_read_b32 v94, a251\n"
    "s_waitcnt vmcnt(15)\n"
    "v_mfma_f32_32x32x16_bf16 v[64:79], a[96:99], v[168:171], v[64:79]\n"
    "v_accvgpr_read_b32 v83, a252\n"
    "v_accvgpr_read_b32 v87, a253\n"
    "v_accvgpr_read_b32 v91, a254\n"
    "v_accvgpr_read_b32 v95, a255\n"
    "v_lshlrev_b32 v80, 16, v82\n"
    "v_and_b32 v82, 0xffff0000, v82\n"
    "v_lshlrev_b32 v84, 16, v86\n"
    "v_mfma_f32_32x32x16_bf16 v[40:55], a[8:11], v[168:171], v[40:55]\n"
    "buffer_load_dwordx4 v[228:231], v0, s[24:27], s42 offen offset:1024 sc1\n"
    "v_and_b32 v86, 0xffff0000, v86\n"
    "v_lshlrev_b32 v88, 16, v90\n"
    "v_and_b32 v90, 0xffff0000, v90\n"
    "v_lshlrev_b32 v92, 16, v94\n"
    "v_and_b32 v94, 0xffff0000, v94\n"
    "v_mfma_f32_32x32x16_bf16 v[64:79], a[100:103], v[172:175], v[64:79]\n"
    "v_lshlrev_b32 v81, 16, v83\n"
    "v_and_b32 v83, 0xffff0000, v83\n"
    "v_lshlrev_b32 v85, 16, v87\n"
    "v_and_b32 v87, 0xffff0000, v87\n"
    "v_lshlrev_b32 v89, 16, v91\n"
    "v_and_b32 v91, 0xffff0000, v91\n"
    "v_lshlrev_b32 v93, 16, v95\n"
    "v_mfma_f32_32x32x16_bf16 v[40:55], a[12:15], v[172:175], v[40:55]\n"
    "buffer_load_dwordx4 v[232:235], v0, s[24:27], s42 offen offset:2048 sc1\n"
    "v_and_b32 v95, 0xffff0000, v95\n"
    "v_mov_b32 v36, -1\n"
    "v_mov_b32 v37, -1\n"
    "v_mov_b32 v38, -1\n"
    "s_waitcnt vmcnt(15)\n"
    "v_mfma_f32_32x32x16_bf16 v[64:79], a[104:107], v[176:179], v[64:79]\n"
    "v_mov_b32 v39, -1\n"
    "v_mfma_f32_32x32x16_bf16 v[40:55], a[16:19], v[176:179], v[40:55]\n"
    "buffer_load_dwordx4 v[236:239], v0, s[24:27], s42 offen offset:3072 sc1\n"
    "buffer_store_dwordx4 v[36:39], v0, s[24:27], s52 offen sc1\n"
    "v_mfma_f32_32x32x16_bf16 v[64:79], a[108:111], v[180:183], v[64:79]\n"
    "v_mfma_f32_32x32x16_bf16 v[40:55], a[20:23], v[180:183], v[40:55]\n"
    "s_add_u32 s42, s62, 0x3000\n"
    "buffer_load_dwordx4 v[240:243], v0, s[24:27], s42 offen sc1\n"
    "s_waitcnt vmcnt(16)\n"
    "v_mfma_f32_32x32x16_bf16 v[64:79], a[112:115], v[184:187], v[64:79]\n"
    "v_mfma_f32_32x32x16_bf16 v[40:55], a[24:27], v[184:187], v[40:55]\n"
    "buffer_load_dwordx4 v[244:247], v0, s[24:27], s42 offen offset:1024 sc1\n"
    "buffer_store_dwordx4 v[36:39], v0, s[24:27], s52 offen offset:1024 sc1\n"
    "s_waitcnt vmcnt(16)\n"
    "v_max3_u32 v15, v192, v193, v194\n"
    "v_mfma_f32_32x32x16_bf16 v[64:79], a[116:119], v[188:191], v[64:79]\n"
    "s_waitcnt vmcnt(15)\n"
    "v_max3_u32 v15, v15, v195, v196\n"
    "v_max3_u32 v15, v15, v197, v198\n"
    "v_max_u32 v15, v15, v199\n"
    "v_cmp_eq_u32 vcc, -1, v15\n"
    "v_mfma_f32_32x32x16_bf16 v[40:55], a[28:31], v[188:191], v[40:55]\n"
    "buffer_load_dwordx4 v[248:251], v0, s[24:27], s42 offen offset:2048 sc1\n"
    "s_cbranch_vccnz Lre199_%=\n"
    "Lok200_%=:\n"
    "v_mfma_f32_32x32x16_bf16 v[112:127], a[120:123], v[192:195], v[112:127]\n"
    "v_mfma_f32_32x32x16_bf16 v[96:111], a[32:35], v[192:195], v[96:111]\n"
    "buffer_load_dwordx4 v[252:255], v0, s[24:27], s42 offen offset:3072 sc1\n"
    "buffer_store_dword v36, v2, s[24:27], s53 offen sc1\n"
    "s_waitcnt vmcnt(17)\n"
    "v_max3_u32 v15, v200, v201, v202\n"
    "v_mfma_f32_32x32x16_bf16 v[112:127], a[124:127], v[196:199], v[112:127]\n"
    "s_waitcnt vmcnt(16)\n"
    "v_max3_u32 v15, v15, v203, v204\n"
    "v_max3_u32 v15, v15, v205, v206\n"
    "v_max_u32 v15, v15, v207\n"
    "v_cmp_eq_u32 vcc, -1, v15\n"
    "v_mfma_f32_32x32x16_bf16 v[96:111], a[36:39], v[196:199], v[96:111]\n"
    "s_add_u32 s42, s63, 0x4000\n"
    "buffer_load_dwordx4 v[128:131], v0, s[24:27], s42 offen sc1\n"
    "s_cbranch_vccnz Lre201_%=\n"
    "Lok202_%=:\n"
    "v_mfma_f32_32x32x16_bf16 v[112:127], a[128:131], v[200:203], v[112:127]\n"
    "v_mfma_f32_32x32x16_bf16 v[96:111], a[40:43], v[200:203], v[96:111]\n"
    "buffer_load_dwordx4 v[132:135], v0, s[24:27], s42 offen offset:1024 sc1\n"
    "s_waitcnt vmcnt(17)\n"
    "v_max3_u32 v15, v208, v209, v210\n"
    "v_mfma_f32_32x32x16_bf16 v[112:127], a[132:135], v[204:207], v[112:127]\n"
    "s_waitcnt vmcnt(16)\n"
    "v_max3_u32 v15, v15, v211, v212\n"
    "v_max3_u32 v15, v15, v213, v214\n"
    "v_max_u32 v15, v15, v215\n"
    "v_cmp_eq_u32 vcc, -1, v15\n"
    "v_mfma_f32_32x32x16_bf16 v[96:111], a[44:47], v[204:207], v[96:111]\n"
    "buffer_load_dwordx4 v[136:139], v0, s[24:27], s42 offen offset:2048 sc1\n"
    "s_cbranch_vccnz Lre203_%=\n"
    "Lok204_%=:\n"
    "v_mfma_f32_32x32x16_bf16 v[64:79], a[120:123], v[208:211], v[64:79]\n"
    "v_mfma_f32_32x32x16_bf16 v[40:55], a[32:35], v[208:211], v[40:55]\n"
    "buffer_load_dwordx4 v[140:143], v0, s[24:27], s42 offen offset:3072 sc1\n"
    "s_waitcnt vmcnt(17)\n"
    "v_max3_u32 v15, v216, v217, v218\n"
    "v_mfma_f32_32x32x16_bf16 v[64:79], a[124:127], v[212:215], v[64:79]\n"
    "s_waitcnt vmcnt(16)\n"
    "v_max3_u32 v15, v15, v219, v220\n"
    "v_max3_u32 v15, v15, v221, v222\n"
    "v_max_u32 v15, v15, v223\n"
    "v_cmp_eq_u32 vcc, -1, v15\n"
    "v_mfma_f32_32x32x16_bf16 v[40:55], a[36:39], v[212:215], v[40:55]\n"
    "s_cbranch_vccnz Lre205_%=\n"
    "Lok206_%=:\n"
    "v_mfma_f32_32x32x16_bf16 v[64:79], a[128:131], v[216:219], v[64:79]\n"
    "v_mfma_f32_32x32x16_bf16 v[40:55], a[40:43], v[216:219], v[40:55]\n"
    "s_waitcnt vmcnt(14)\n"
    "v_max3_u32 v15, v224, v225, v226\n"
    "v_mfma_f32_32x32x16_bf16 v[64:79], a[132:135], v[220:223], v[64:79]\n"
    "s_waitcnt vmcnt(13)\n"
    "v_max3_u32 v15, v15, v227, v228\n"
    "v_max3_u32 v15, v15, v229, v230\n"
    "v_max_u32 v15, v15, v231\n"
    "v_cmp_eq_u32 vcc, -1, v15\n"
    "v_mfma_f32_32x32x16_bf16 v[40:55], a[44:47], v[220:223], v[40:55]\n"
    "s_cbranch_vccnz Lre207_%=\n"
    "Lok208_%=:\n"
    "v_mfma_f32_32x32x16_bf16 v[112:127], a[136:139], v[224:227], v[112:127]\n"
    "v_mfma_f32_32x32x16_bf16 v[96:111], a[48:51], v[224:227], v[96:111]\n"
    "s_waitcnt vmcnt(12)\n"
    "v_max3_u32 v15, v232, v233, v234\n"
    "v_mfma_f32_32x32x16_bf16 v[112:127], a[140:143], v[228:231], v[112:127]\n"
    "s_add_u32 s42, s63, 0x5000\n"
    "buffer_load_dwordx4 v[144:147], v0, s[24:27], s42 offen sc1\n"
    "s_waitcnt vmcnt(12)\n"
    "v_max3_u32 v15, v15, v235, v236\n"
    "v_max3_u32 v15, v15, v237, v238\n"
    "v_max_u32 v15, v15, v239\n"
    "v_cmp_eq_u32 vcc, -1, v15\n"
    "v_mfma_f32_32x32x16_bf16 v[96:111], a[52:55], v[228:231], v[96:111]\n"
    "s_cbranch_vccnz Lre209_%=\n"
    "Lok210_%=:\n"
    "v_mfma_f32_32x32x16_bf16 v[112:127], a[144:147], v[232:235], v[112:127]\n"
    "buffer_load_dwordx4 v[148:151], v0, s[24:27], s65 offen sc1\n"
    "v_mfma_f32_32x32x16_bf16 v[96:111], a[56:59], v[232:235], v[96:111]\n"
    "s_waitcnt vmcnt(11)\n"
    "v_max3_u32 v15, v240, v241, v242\n"
    "v_mfma_f32_32x32x16_bf16 v[112:127], a[148:151], v[236:239], v[112:127]\n"
    "s_add_u32 s42, s62, 0x4000\n"
    "buffer_load_dwordx4 v[152:155], v0, s[24:27], s42 offen sc1\n"
    "s_waitcnt vmcnt(11)\n"
    "v_max3_u32 v15, v15, v243, v244\n"
    "v_max3_u32 v15, v15, v245, v246\n"
    "v_max_u32 v15, v15, v247\n"
    "v_cmp_eq_u32 vcc, -1, v15\n"
    "v_mfma_f32_32x32x16_bf16 v[96:111], a[60:63], v[236:239], v[96:111]\n"
    "s_cbranch_vccnz Lre211_%=\n"
    "Lok212_%=:\n"
    "v_mfma_f32_32x32x16_bf16 v[64:79], a[136:139], v[240:243], v[64:79]\n"
    "buffer_load_dwordx4 v[156:159], v0, s[24:27], s42 offen offset:1024 sc1\n"
    "v_mfma_f32_32x32x16_bf16 v[40:55], a[48:51], v[240:243], v[40:55]\n"
    "s_waitcnt vmcnt(10)\n"
    "v_max3_u32 v15, v248, v249, v250\n"
    "v_mfma_f32_32x32x16_bf16 v[64:79], a[140:143], v[244:247], v[64:79]\n"
    "buffer_load_dwordx4 v[160:163], v0, s[24:27], s42 offen offset:2048 sc1\n"
    "s_waitcnt vmcnt(10)\n"
    "v_max3_u32 v15, v15, v251, v252\n"
    "v_max3_u32 v15, v15, v253, v254\n"
    "v_max_u32 v15, v15, v255\n"
    "v_cmp_eq_u32 vcc, -1, v15\n"
    "v_mfma_f32_32x32x16_bf16 v[40:55], a[52:55], v[244:247], v[40:55]\n"
    "s_cbranch_vccnz Lre213_%=\n"
    "Lok214_%=:\n"
    "v_mfma_f32_32x32x16_bf16 v[64:79], a[144:147], v[248:251], v[64:79]\n"
    "buffer_load_dwordx4 v[164:167], v0, s[24:27], s42 offen offset:3072 sc1\n"
    "v_mfma_f32_32x32x16_bf16 v[40:55], a[56:59], v[248:251], v[40:55]\n"
    "s_waitcnt vmcnt(9)\n"
    "v_max3_u32 v15, v128, v129, v130\n"
    "v_mfma_f32_32x32x16_bf16 v[64:79], a[148:151], v[252:255], v[64:79]\n"
    "s_add_u32 s42, s62, 0x5000\n"
    "buffer_load_dwordx4 v[168:171], v0, s[24:27], s42 offen sc1\n"
    "s_waitcnt vmcnt(9)\n"
    "v_max3_u32 v15, v15, v131, v132\n"
    "v_max3_u32 v15, v15, v133, v134\n"
    "v_max_u32 v15, v15, v135\n"
    "v_cmp_eq_u32 vcc, -1, v15\n"
    "v_mfma_f32_32x32x16_bf16 v[40:55], a[60:63], v[252:255], v[40:55]\n"
    "s_cbranch_vccnz Lre215_%=\n"
    "Lok216_%=:\n"
    "v_mfma_f32_32x32x16_bf16 v[112:127], a[152:155], v[128:131], v[112:127]\n"
    "buffer_load_dwordx4 v[172:175], v0, s[24:27], s64 offen sc1\n"
    "v_mfma_f32_32x32x16_bf16 v[96:111], a[64:67], v[128:131], v[96:111]\n"
    "s_waitcnt vmcnt(9)\n"
    "v_max3_u32 v15, v136, v137, v138\n"
    "v_mfma_f32_32x32x16_bf16 v[112:127], a[156:159], v[132:135], v[112:127]\n"
    "s_waitcnt vmcnt(8)\n"
    "v_max3_u32 v15, v15, v139, v140\n"
    "v_max3_u32 v15, v15, v141, v142\n"
    "v_max_u32 v15, v15, v143\n"
    "v_cmp_eq_u32 vcc, -1, v15\n"
    "v_mfma_f32_32x32x16_bf16 v[96:111], a[68:71], v[132:135], v[96:111]\n"
    "s_add_u32 m0, s78, 0x10000\n"
    "s_add_u32 s47, s66, 0x104000\n"
    "buffer_load_dwordx4 v0, s[24:27], s47 offen sc1 lds\n"
    "s_cbranch_vccnz Lre217_%=\n"
    "Lok218_%=:\n"
    "v_mfma_f32_32x32x16_bf16 v[112:127], a[160:163], v[136:139], v[112:127]\n"
    "v_add_u32 v39, s81, v0\n"
    "v_add_u32 v38, s80, v0\n"
    "v_add_u32 v37, s79, v0\n"
    "v_mfma_f32_32x32x16_bf16 v[96:111], a[72:75], v[136:139], v[96:111]\n"
    "buffer_load_dwordx4 v0, s[24:27], s47 offen offset:1024 sc1 lds\n"
    "s_waitcnt vmcnt(9)\n"
    "v_max3_u32 v15, v144, v145, v146\n"
    "v_mfma_f32_32x32x16_bf16 v[112:127], a[164:167], v[140:143], v[112:127]\n"
    "s_waitcnt vmcnt(8)\n"
    "v_max3_u32 v15, v15, v147, v148\n"
    "v_max3_u32 v15, v15, v149, v150\n"
    "v_max_u32 v15, v15, v151\n"
    "v_cmp_eq_u32 vcc, -1, v15\n"
    "v_mfma_f32_32x32x16_bf16 v[96:111], a[76:79], v[140:143], v[96:111]\n"
    "buffer_load_dwordx4 v0, s[24:27], s47 offen offset:2048 sc1 lds\n"
    "s_cbranch_vccnz Lre219_%=\n"
    "Lok220_%=:\n"
    "v_mfma_f32_32x32x16_bf16 v[112:127], a[168:171], v[144:147], v[112:127]\n"
    "v_mfma_f32_32x32x16_bf16 v[96:111], a[80:83], v[144:147], v[96:111]\n"
    "buffer_load_dwordx4 v0, s[24:27], s47 offen offset:3072 sc1 lds\n"
    "s_waitcnt vmcnt(9)\n"
    "v_max3_u32 v15, v152, v153, v154\n"
    "v_mfma_f32_32x32x16_bf16 v[112:127], a[172:175], v[148:151], v[112:127]\n"
    "s_waitcnt vmcnt(8)\n"
    "v_max3_u32 v15, v15, v155, v156\n"
    "v_max3_u32 v15, v15, v157, v158\n"
    "v_max_u32 v15, v15, v159\n"
    "v_cmp_eq_u32 vcc, -1, v15\n"
    "v_mfma_f32_32x32x16_bf16 v[96:111], a[84:87], v[148:151], v[96:111]\n"
    "s_add_u32 m0, s78, 0x11000\n"
    "s_add_u32 s47, s67, 0x104000\n"
    "buffer_load_dwordx4 v0, s[24:27], s47 offen sc1 lds\n"
    "s_cbranch_vccnz Lre221_%=\n"
    "Lok222_%=:\n"
    "v_mfma_f32_32x32x16_bf16 v[64:79], a[152:155], v[152:155], v[64:79]\n"
    "v_mfma_f32_32x32x16_bf16 v[40:55], a[64:67], v[152:155], v[40:55]\n"
    "buffer_load_dwordx4 v0, s[24:27], s47 offen offset:1024 sc1 lds\n"
    "s_waitcnt vmcnt(9)\n"
    "v_max3_u32 v15, v160, v161, v162\n"
    "ds_write_b128 v39, v[112:115]\n"
    "v_mfma_f32_32x32x16_bf16 v[64:79], a[156:159], v[156:159], v[64:79]\n"
    "s_waitcnt vmcnt(8)\n"
    "v_max3_u32 v15, v15, v163, v164\n"
    "v_max3_u32 v15, v15, v165, v166\n"
    "v_max_u32 v15, v15, v167\n"
    "v_cmp_eq_u32 vcc, -1, v15\n"
    "ds_write_b128 v39, v[116:119] offset:1024\n"
    "v_mfma_f32_32x32x16_bf16 v[40:55], a[68:71], v[156:159], v[40:55]\n"
    "buffer_load_dwordx4 v0, s[24:27], s47 offen offset:2048 sc1 lds\n"
    "s_cbranch_vccnz Lre223_%=\n"
    "Lok224_%=:\n"
    "v_mfma_f32_32x32x16_bf16 v[64:79], a[160:163], v[160:163], v[64:79]\n"
    "ds_write_b128 v39, v[120:123] offset:2048\n"
    "v_mfma_f32_32x32x16_bf16 v[40:55], a[72:75], v[160:163], v[40:55]\n"
    "buffer_load_dwordx4 v0, s[24:27], s47 offen offset:3072 sc1 lds\n"
    "s_waitcnt vmcnt(9)\n"
    "v_max3_u32 v15, v168, v169, v170\n"
    "ds_write_b128 v39, v[124:127] offset:3072\n"
    "v_mfma_f32_32x32x16_bf16 v[64:79], a[164:167], v[164:167], v[64:79]\n"
    "s_waitcnt vmcnt(8)\n"
    "v_max3_u32 v15, v15, v171, v172\n"
    "v_max3_u32 v15, v15, v173, v174\n"
    "v_max_u32 v15, v15, v175\n"
    "v_cmp_eq_u32 vcc, -1, v15\n"
    "v_mfma_f32_32x32x16_bf16 v[40:55], a[76:79], v[164:167], v[40:55]\n"
    "s_add_u32 m0, s86, 0x400\n"
    "s_nop 0\n"
    "buffer_load_dword v3, s[24:27], s47 offen sc1 lds\n"
    "s_cbranch_vccnz Lre225_%=\n"
    "Lok226_%=:\n"
    "ds_write_b128 v38, v[96:99] offset:32768\n"
    "v_mfma_f32_32x32x16_bf16 v[64:79], a[168:171], v[168:171], v[64:79]\n"
    "ds_write_b128 v38, v[100:103] offset:33792\n"
    "v_mfma_f32_32x32x16_bf16 v[40:55], a[80:83], v[168:171], v[40:55]\n"
    "s_add_u32 m0, s78, 0x8000\n"
    "s_add_u32 s47, s66, 0x0\n"
    "buffer_load_dwordx4 v0, s[24:27], s47 offen sc1 lds\n"
    "v_mfma_f32_32x32x16_bf16 v[64:79], a[172:175], v[172:175], v[64:79]\n"
    "ds_write_b128 v38, v[104:107] offset:34816\n"
    "v_mfma_f32_32x32x16_bf16 v[40:55], a[84:87], v[172:175], v[40:55]\n"
    "ds_write_b128 v38, v[108:111] offset:35840\n"
    "s_nop 8\n"
    "ds_write_b128 v37, v[64:67] offset:32768\n"
    "ds_write_b128 v37, v[68:71] offset:33792\n"
    "ds_write_b128 v37, v[72:75] offset:34816\n"
    "ds_write_b128 v37, v[76:79] offset:35840\n"
    "v_pk_add_f32 v[40:41], v[40:41], v[80:81]\n"
    "v_pk_add_f32 v[42:43], v[42:43], v[82:83]\n"
    "v_pk_add_f32 v[44:45], v[44:45], v[84:85]\n"
    "v_pk_add_f32 v[46:47], v[46:47], v[86:87]\n"
    "v_pk_add_f32 v[48:49], v[48:49], v[88:89]\n"
    "v_pk_add_f32 v[50:51], v[50:51], v[90:91]\n"
    "v_pk_add_f32 v[52:53], v[52:53], v[92:93]\n"
    "v_pk_add_f32 v[54:55], v[54:55], v[94:95]\n"
    "buffer_load_dwordx4 v0, s[24:27], s47 offen offset:1024 sc1 lds\n"
    "buffer_load_dwordx4 v0, s[24:27], s47 offen offset:2048 sc1 lds\n"
    "buffer_load_dwordx4 v0, s[24:27], s47 offen offset:3072 sc1 lds\n"
    "s_add_u32 m0, s78, 0x9000\n"
    "s_add_u32 s47, s67, 0x0\n"
    "buffer_load_dwordx4 v0, s[24:27], s47 offen sc1 lds\n"
    "v_add_u32 v32, s82, v0\n"
    "v_add_u32 v33, s83, v0\n"
    "s_waitcnt lgkmcnt(0)\n"
    "s_barrier\n"
    "v_lshlrev_b32 v60, 3, v0\n"
    "v_mov_b32 v61, 1\n"
    "s_mov_b64 exec, s[18:19]\n"
    "buffer_atomic_add v61, v60, s[24:27], s90 offen offset:16 sc1\n"
    "s_mov_b64 exec, -1\n"
    "ds_read_b128 v[64:67], v32 offset:32768\n"
    "ds_read_b128 v[96:99], v32 offset:36864\n"
    "ds_read_b128 v[112:115], v33\n"
    "ds_read_b128 v[68:71], v32 offset:33792\n"
    "ds_read_b128 v[100:103], v32 offset:37888\n"
    "ds_read_b128 v[116:119], v33 offset:1024\n"
    "ds_read_b128 v[72:75], v32 offset:34816\n"
    "ds_read_b128 v[104:107], v32 offset:38912\n"
    "ds_read_b128 v[120:123], v33 offset:2048\n"
    "ds_read_b128 v[76:79], v32 offset:35840\n"
    "ds_read_b128 v[108:111], v32 offset:39936\n"
    "ds_read_b128 v[124:127], v33 offset:3072\n"
    "s_waitcnt lgkmcnt(9)\n"
    "v_pk_add_f32 v[40:41], v[40:41], v[64:65]\n"
    "v_pk_add_f32 v[42:43], v[42:43], v[66:67]\n"
    "v_pk_add_f32 v[40:41], v[40:41], v[96:97]\n"
    "v_pk_add_f32 v[42:43], v[42:43], v[98:99]\n"
    "v_pk_add_f32 v[40:41], v[40:41], v[112:113]\n"
    "v_pk_add_f32 v[42:43], v[42:43], v[114:115]\n"
    "v_cvt_pk_bf16_f32 v24, v40, v41\n"
    "v_cvt_pk_bf16_f32 v25, v42, v43\n"
    "v_mov_b32 v60, 0\n"
    "s_mov_b64 exec, s[16:17]\n"
    "buffer_atomic_add v60, off, s[24:27], s91 offset:16 sc0 sc1\n"
    "s_mov_b64 exec, -1\n"
    "s_waitcnt lgkmcnt(6)\n"
    "v_pk_add_f32 v[44:45], v[44:45], v[68:69]\n"
    "v_pk_add_f32 v[46:47], v[46:47], v[70:71]\n"
    "v_pk_add_f32 v[44:45], v[44:45], v[100:101]\n"
    "v_pk_add_f32 v[46:47], v[46:47], v[102:103]\n"
    "v_pk_add_f32 v[44:45], v[44:45], v[116:117]\n"
    "v_pk_add_f32 v[46:47], v[46:47], v[118:119]\n"
    "v_cvt_pk_bf16_f32 v26, v44, v45\n"
    "v_cvt_pk_bf16_f32 v27, v46, v47\n"
    "s_waitcnt lgkmcnt(3)\n"
    "v_pk_add_f32 v[48:49], v[48:49], v[72:73]\n"
    "v_pk_add_f32 v[50:51], v[50:51], v[74:75]\n"
    "v_pk_add_f32 v[48:49], v[48:49], v[104:105]\n"
    "v_pk_add_f32 v[50:51], v[50:51], v[106:107]\n"
    "v_pk_add_f32 v[48:49], v[48:49], v[120:121]\n"
    "v_pk_add_f32 v[50:51], v[50:51], v[122:123]\n"
    "v_cvt_pk_bf16_f32 v28, v48, v49\n"
    "v_cvt_pk_bf16_f32 v29, v50, v51\n"
    "s_waitcnt lgkmcnt(0)\n"
    "v_pk_add_f32 v[52:53], v[52:53], v[76:77]\n"
    "v_pk_add_f32 v[54:55], v[54:55], v[78:79]\n"
    "v_pk_add_f32 v[52:53], v[52:53], v[108:109]\n"
    "v_pk_add_f32 v[54:55], v[54:55], v[110:111]\n"
    "v_pk_add_f32 v[52:53], v[52:53], v[124:125]\n"
    "v_pk_add_f32 v[54:55], v[54:55], v[126:127]\n"
    "v_cvt_pk_bf16_f32 v30, v52, v53\n"
    "v_cvt_pk_bf16_f32 v31, v54, v55\n"
    "buffer_store_dwordx4 v[24:27], v0, s[24:27], s52 offen sc1\n"
    "buffer_store_dwordx4 v[28:31], v0, s[24:27], s52 offen offset:1024 sc1\n"
    "v_mov_b32 v61, 0\n"
    "s_mov_b64 exec, s[16:17]\n"
    "buffer_atomic_add v61, off, s[24:27], s91 offset:16 sc0 sc1\n"
    "s_mov_b64 exec, -1\n"
    "buffer_load_dwordx4 v0, s[24:27], s47 offen offset:1024 sc1 lds\n"
    "v_mov_b32 v32, 0\n"
    "v_mov_b32 v33, 0\n"
    "v_mov_b32 v34, 0x3f803f80\n"
    "s_lshl_b32 s44, s37, 8\n"
    "s_add_u32 s44, s44, 0x27000\n"
    "v_add_u32 v35, s44, v2\n"
    "v_dot2c_f32_bf16 v32, v24, v34\n"
    "v_dot2c_f32_bf16 v33, v24, v24\n"
    "v_dot2c_f32_bf16 v32, v25, v34\n"
    "v_dot2c_f32_bf16 v33, v25, v25\n"
    "v_dot2c_f32_bf16 v32, v26, v34\n"
    "v_dot2c_f32_bf16 v33, v26, v26\n"
    "v_dot2c_f32_bf16 v32, v27, v34\n"
    "v_dot2c_f32_bf16 v33, v27, v27\n"
    "v_dot2c_f32_bf16 v32, v28, v34\n"
    "v_dot2c_f32_bf16 v33, v28, v28\n"
    "v_dot2c_f32_bf16 v32, v29, v34\n"
    "v_dot2c_f32_bf16 v33, v29, v29\n"
    "v_dot2c_f32_bf16 v32, v30, v34\n"
    "buffer_load_dwordx4 v0, s[24:27], s47 offen offset:2048 sc1 lds\n"
    "v_dot2c_f32_bf16 v33, v30, v30\n"
    "v_dot2c_f32_bf16 v32, v31, v34\n"
    "v_dot2c_f32_bf16 v33, v31, v31\n"
    "v_accvgpr_write_b32 a248, v24\n"
    "v_accvgpr_write_b32 a249, v25\n"
    "v_accvgpr_write_b32 a250, v26\n"
    "v_accvgpr_write_b32 a251, v27\n"
    "v_permlane32_swap_b32 v32, v33\n"
    "v_add_f32 v32, v32, v33\n"
    "ds_write_b32 v35, v32\n"
    "v_xor_b32 v35, 0x100, v35\n"
    "s_waitcnt lgkmcnt(0)\n"
    "s_barrier\n"
    "ds_read_b32 v33, v35\n"
    "v_accvgpr_write_b32 a252, v28\n"
    "v_accvgpr_write_b32 a253, v29\n"
    "v_accvgpr_write_b32 a254, v30\n"
    "v_accvgpr_write_b32 a255, v31\n"
    "buffer_load_dwordx4 v0, s[24:27], s47 offen offset:3072 sc1 lds\n"
    "s_waitcnt lgkmcnt(0)\n"
    "v_add_f32 v32, v32, v33\n"
    "buffer_store_dword v32, v2, s[24:27], s53 offen sc1\n"
    "s_add_u32 s52, s52, 0x240000\n"
    "s_add_u32 s53, s53, 0x240000\n"
    "s_add_u32 m0, s86, 0x200\n"
    "s_nop 0\n"
    "buffer_load_dword v3, s[24:27], s47 offen sc1 lds\n"
    "s_add_u32 s54, s54, 0xa0000\n"
    "s_add_u32 s55, s55, 0xa0000\n"
    "s_add_u32 s56, s56, 0xa0000\n"
    "s_add_u32 s57, s57, 0x200000\n"
    "s_add_u32 s58, s58, 0x200000\n"
    "s_add_u32 s59, s59, 0x200000\n"
    "s_add_u32 s60, s60, 0x580000\n"
    "s_add_u32 s61, s61, 0x580000\n"
    "s_add_u32 s62, s62, 0x580000\n"
    "s_add_u32 s63, s63, 0x580000\n"
    "s_add_u32 s64, s64, 0x580000\n"
    "s_add_u32 s65, s65, 0x580000\n"
    "v_and_b32 v15, 0x180, v0\n"
    "s_mov_b32 m0, 162528\n"
    "s_nop 0\n"
    "buffer_load_dword v15, s[28:31], s87 offen lds\n"
    "s_cmp_lg_u32 s37, 0\n"
    "s_cbranch_scc1 Lhs229_%=\n"
    "s_mov_b64 exec, 1\n"
    "v_mov_b32 v62, 0\n"
    "buffer_atomic_add v62, off, s[24:27], s91 offset:16 sc0 sc1\n"
    "s_waitcnt vmcnt(10)\n"
    "v_cmp_eq_u32 vcc, 16, v60\n"
    "s_cbranch_vccnz Lhd228_%=\n"
    "v_mov_b32 v60, 0\n"
    "buffer_atomic_add v60, off, s[24:27], s91 offset:16 sc0 sc1\n"
    "s_waitcnt vmcnt(8)\n"
    "v_cmp_eq_u32 vcc, 16, v61\n"
    "s_cbranch_vccnz Lhd228_%=\n"
    "v_mov_b32 v61, 0\n"
    "buffer_atomic_add v61, off, s[24:27], s91 offset:16 sc0 sc1\n"
    "Lhp227_%=:\n"
    "s_waitcnt vmcnt(2)\n"
    "v_cmp_eq_u32 vcc, 16, v62\n"
    "s_cbranch_vccnz Lhd228_%=\n"
    "v_mov_b32 v62, 0\n"
    "buffer_atomic_add v62, off, s[24:27], s91 offset:16 sc0 sc1\n"
    "s_waitcnt vmcnt(2)\n"
    "v_cmp_eq_u32 vcc, 16, v60\n"
    "s_cbranch_vccnz Lhd228_%=\n"
    "v_mov_b32 v60, 0\n"
    "buffer_atomic_add v60, off, s[24:27], s91 offset:16 sc0 sc1\n"
    "s_waitcnt vmcnt(2)\n"
    "v_cmp_eq_u32 vcc, 16, v61\n"
    "s_cbranch_vccnz Lhd228_%=\n"
    "v_mov_b32 v61, 0\n"
    "buffer_atomic_add v61, off, s[24:27], s91 offset:16 sc0 sc1\n"
    "s_sub_u32 s50, s50, 1\n"
    "s_cbranch_scc0 Lhp227_%=\n"
    "s_branch Lfail_%=\n"
    "Lhd228_%=:\n"
    "s_mov_b64 exec, -1\n"
    "Lhs229_%=:\n"
    "s_barrier\n"
    "buffer_load_dwordx4 v[128:131], v0, s[24:27], s51 offen sc1\n"
    "buffer_load_dwordx4 v[132:135], v0, s[24:27], s51 offen offset:1024 sc1\n"
    "buffer_load_dwordx4 v[136:139], v0, s[24:27], s51 offen offset:2048 sc1\n"
    "buffer_load_dwordx4 v[140:143], v0, s[24:27], s51 offen offset:3072 sc1\n"
    "s_add_u32 s42, s51, 0x1000\n"
    "buffer_load_dwordx4 v[144:147], v0, s[24:27], s42 offen sc1\n"
    "buffer_load_dwordx4 v[148:151], v0, s[24:27], s42 offen offset:1024 sc1\n"
    "buffer_load_dwordx4 v[152:155], v0, s[24:27], s42 offen offset:2048 sc1\n"
    "buffer_load_dwordx4 v[156:159], v0, s[24:27], s42 offen offset:3072 sc1\n"
    "s_add_u32 s42, s51, 0x2000\n"
    "buffer_load_dwordx4 v[160:163], v0, s[24:27], s42 offen sc1\n"
    "buffer_load_dwordx4 v[164:167], v0, s[24:27], s42 offen offset:1024 sc1\n"
    "buffer_load_dwordx4 v[168:171], v0, s[24:27], s42 offen offset:2048 sc1\n"
    "buffer_load_dwordx4 v[172:175], v0, s[24:27], s42 offen offset:3072 sc1\n"
    "s_waitcnt vmcnt(13)\n"
    "s_barrier\n"
    "ds_read_b128 v[40:43], v0\n"
    "ds_read_b128 v[44:47], v1\n"
    "ds_read_b128 v[48:51], v0 offset:1024\n"
    "ds_read_b128 v[52:55], v1 offset:1024\n"
    "ds_read_b128 v[56:59], v0 offset:2048\n"
    "s_waitcnt vmcnt(10)\n"
    "v_max3_u32 v15, v135, v129, v130\n"
    "v_max3_u32 v15, v15, v128, v131\n"
    "v_max3_u32 v15, v15, v132, v133\n"
    "v_max_u32 v15, v15, v134\n"
    "v_cmp_eq_u32 vcc, -1, v15\n"
    "s_waitcnt lgkmcnt(3)\n"
    "s_cbranch_vccnz Lre230_%=\n"
    "Lok231_%=:\n"
    "v_mfma_f32_32x32x16_bf16 v[64:79], v[40:43], v[128:131], 0\n"
    "s_add_u32 s42, s51, 0x3000\n"
    "buffer_load_dwordx4 v[176:179], v0, s[24:27], s42 offen sc1\n"
    "ds_read_b128 v[60:63], v1 offset:2048\n"
    "v_mfma_f32_32x32x16_bf16 v[80:95], v[44:47], v[128:131], 0\n"
    "ds_read_b128 v[40:43], v0 offset:32768\n"
    "s_waitcnt vmcnt(10)\n"
    "v_max3_u32 v15, v136, v137, v138\n"
    "s_waitcnt lgkmcnt(3)\n"
    "v_mfma_f32_32x32x16_bf16 v[64:79], v[48:51], v[132:135], v[64:79]\n"
    "ds_read_b128 v[44:47], v0 offset:3072\n"
    "s_waitcnt vmcnt(9)\n"
    "v_max3_u32 v15, v15, v139, v140\n"
    "v_max3_u32 v15, v15, v141, v142\n"
    "v_max_u32 v15, v15, v143\n"
    "v_cmp_eq_u32 vcc, -1, v15\n"
    "v_mfma_f32_32x32x16_bf16 v[80:95], v[52:55], v[132:135], v[80:95]\n"
    "buffer_load_dwordx4 v[180:183], v0, s[24:27], s42 offen offset:1024 sc1\n"
    "ds_read_b128 v[48:51], v1 offset:3072\n"
    "s_cbranch_vccnz Lre232_%=\n"
    "Lok233_%=:\n"
    "s_waitcnt lgkmcnt(3)\n"
    "v_mfma_f32_32x32x16_bf16 v[64:79], v[56:59], v[136:139], v[64:79]\n"
    "buffer_load_dwordx4 v[184:187], v0, s[24:27], s42 offen offset:2048 sc1\n"
    "ds_read_b128 v[52:55], v0 offset:4096\n"
    "v_mov_b32 v36, -1\n"
    "v_mov_b32 v37, -1\n"
    "v_mov_b32 v38, -1\n"
    "v_mfma_f32_32x32x16_bf16 v[80:95], v[60:63], v[136:139], v[80:95]\n"
    "ds_read_b128 v[56:59], v1 offset:4096\n"
    "v_mov_b32 v39, -1\n"
    "s_waitcnt lgkmcnt(3)\n"
    "v_mfma_f32_32x32x16_bf16 v[96:111], v[40:43], v[128:131], 0\n"
    "ds_read_b128 v[60:63], v0 offset:33792\n"
    "s_waitcnt vmcnt(10)\n"
    "v_max3_u32 v15, v144, v145, v146\n"
    "v_mfma_f32_32x32x16_bf16 v[64:79], v[44:47], v[140:143], v[64:79]\n"
    "ds_read_b128 v[40:43], v0 offset:5120\n"
    "s_waitcnt vmcnt(9)\n"
    "v_max3_u32 v15, v15, v147, v148\n"
    "v_max3_u32 v15, v15, v149, v150\n"
    "v_max_u32 v15, v15, v151\n"
    "v_cmp_eq_u32 vcc, -1, v15\n"
    "s_waitcnt lgkmcnt(3)\n"
    "v_mfma_f32_32x32x16_bf16 v[80:95], v[48:51], v[140:143], v[80:95]\n"
    "buffer_load_dwordx4 v[188:191], v0, s[24:27], s42 offen offset:3072 sc1\n"
    "ds_read_b128 v[44:47], v1 offset:5120\n"
    "s_cbranch_vccnz Lre234_%=\n"
    "Lok235_%=:\n"
    "v_mfma_f32_32x32x16_bf16 v[64:79], v[52:55], v[144:147], v[64:79]\n"
    "s_add_u32 s42, s51, 0x4000\n"
    "buffer_load_dwordx4 v[192:195], v0, s[24:27], s42 offen sc1\n"
    "ds_read_b128 v[48:51], v0 offset:6144\n"
    "s_waitcnt lgkmcnt(3)\n"
    "v_mfma_f32_32x32x16_bf16 v[80:95], v[56:59], v[144:147], v[80:95]\n"
    "ds_read_b128 v[52:55], v1 offset:6144\n"
    "v_mfma_f32_32x32x16_bf16 v[96:111], v[60:63], v[132:135], v[96:111]\n"
    "ds_read_b128 v[56:59], v0 offset:34816\n"
    "s_waitcnt vmcnt(10)\n"
    "v_max3_u32 v15, v152, v153, v154\n"
    "s_waitcnt lgkmcnt(3)\n"
    "v_mfma_f32_32x32x16_bf16 v[64:79], v[40:43], v[148:151], v[64:79]\n"
    "ds_read_b128 v[60:63], v0 offset:7168\n"
    "s_waitcnt vmcnt(9)\n"
    "v_max3_u32 v15, v15, v155, v156\n"
    "v_max3_u32 v15, v15, v157, v158\n"
    "v_max_u32 v15, v15, v159\n"
    "v_cmp_eq_u32 vcc, -1, v15\n"
    "v_mfma_f32_32x32x16_bf16 v[80:95], v[44:47], v[148:151], v[80:95]\n"
    "buffer_load_dwordx4 v[196:199], v0, s[24:27], s42 offen offset:1024 sc1\n"
    "ds_read_b128 v[40:43], v1 offset:7168\n"
    "s_cbranch_vccnz Lre236_%=\n"
    "Lok237_%=:\n"
    "s_waitcnt lgkmcnt(3)\n"
    "v_mfma_f32_32x32x16_bf16 v[64:79], v[48:51], v[152:155], v[64:79]\n"
    "buffer_load_dwordx4 v[200:203], v0, s[24:27], s42 offen offset:2048 sc1\n"
    "ds_read_b128 v[44:47], v0 offset:8192\n"
    "v_mfma_f32_32x32x16_bf16 v[80:95], v[52:55], v[152:155], v[80:95]\n"
    "s_add_u32 s43, s51, 0x8000\n"
    "buffer_load_dwordx4 v[112:115], v0, s[24:27], s43 offen sc1\n"
    "ds_read_b128 v[48:51], v1 offset:8192\n"
    "s_waitcnt lgkmcnt(3)\n"
    "v_mfma_f32_32x32x16_bf16 v[96:111], v[56:59], v[136:139], v[96:111]\n"
    "ds_read_b128 v[52:55], v0 offset:9216\n"
    "s_waitcnt vmcnt(11)\n"
    "v_max3_u32 v15, v160, v161, v162\n"
    "v_mfma_f32_32x32x16_bf16 v[64:79], v[60:63], v[156:159], v[64:79]\n"
    "ds_read_b128 v[56:59], v1 offset:9216\n"
    "s_waitcnt vmcnt(10)\n"
    "v_max3_u32 v15, v15, v163, v164\n"
    "v_max3_u32 v15, v15, v165, v166\n"
    "v_max_u32 v15, v15, v167\n"
    "v_cmp_eq_u32 vcc, -1, v15\n"
    "s_waitcnt lgkmcnt(3)\n"
    "v_mfma_f32_32x32x16_bf16 v[80:95], v[40:43], v[156:159], v[80:95]\n"
    "buffer_load_dwordx4 v[204:207], v0, s[24:27], s42 offen offset:3072 sc1\n"
    "ds_read_b128 v[60:63], v0 offset:35840\n"
    "s_cbranch_vccnz Lre238_%=\n"
    "Lok239_%=:\n"
    "v_mfma_f32_32x32x16_bf16 v[64:79], v[44:47], v[160:163], v[64:79]\n"
    "s_add_u32 s42, s51, 0x5000\n"
    "buffer_load_dwordx4 v[208:211], v0, s[24:27], s42 offen sc1\n"
    "ds_read_b128 v[40:43], v0 offset:10240\n"
    "s_waitcnt lgkmcnt(3)\n"
    "v_mfma_f32_32x32x16_bf16 v[80:95], v[48:51], v[160:163], v[80:95]\n"
    "ds_read_b128 v[44:47], v1 offset:10240\n"
    "v_mfma_f32_32x32x16_bf16 v[64:79], v[52:55], v[164:167], v[64:79]\n"
    "buffer_load_dwordx4 v[212:215], v0, s[24:27], s42 offen offset:1024 sc1\n"
    "ds_read_b128 v[48:51], v0 offset:11264\n"
    "s_waitcnt vmcnt(12)\n"
    "v_max3_u32 v15, v168, v169, v170\n"
    "s_waitcnt lgkmcnt(3)\n"
    "v_mfma_f32_32x32x16_bf16 v[80:95], v[56:59], v[164:167], v[80:95]\n"
    "ds_read_b128 v[52:55], v1 offset:11264\n"
    "s_waitcnt vmcnt(11)\n"
    "v_max3_u32 v15, v15, v171, v172\n"
    "v_max3_u32 v15, v15, v173, v174\n"
    "v_max_u32 v15, v15, v175\n"
    "v_cmp_eq_u32 vcc, -1, v15\n"
    "v_mfma_f32_32x32x16_bf16 v[96:111], v[60:63], v[140:143], v[96:111]\n"
    "ds_read_b128 v[56:59], v0 offset:36864\n"
    "s_cbranch_vccnz Lre240_%=\n"
    "Lok241_%=:\n"
    "s_waitcnt lgkmcnt(3)\n"
    "v_mfma_f32_32x32x16_bf16 v[64:79], v[40:43], v[168:171], v[64:79]\n"
    "buffer_load_dwordx4 v[216:219], v0, s[24:27], s42 offen offset:2048 sc1\n"
    "ds_read_b128 v[60:63], v0 offset:12288\n"
    "v_mfma_f32_32x32x16_bf16 v[80:95], v[44:47], v[168:171], v[80:95]\n"
    "buffer_load_dwordx4 v[116:119], v0, s[24:27], s43 offen offset:1024 sc1\n"
    "ds_read_b128 v[40:43], v1 offset:12288\n"
    "s_waitcnt lgkmcnt(3)\n"
    "v_mfma_f32_32x32x16_bf16 v[64:79], v[48:51], v[172:175], v[64:79]\n"
    "buffer_load_dwordx4 v[220:223], v0, s[24:27], s42 offen offset:3072 sc1\n"
    "ds_read_b128 v[44:47], v0 offset:13312\n"
    "s_waitcnt vmcnt(13)\n"
    "v_max3_u32 v15, v176, v177, v178\n"
    "v_mfma_f32_32x32x16_bf16 v[80:95], v[52:55], v[172:175], v[80:95]\n"
    "ds_read_b128 v[48:51], v1 offset:13312\n"
    "s_waitcnt vmcnt(12)\n"
    "v_max3_u32 v15, v15, v179, v180\n"
    "v_max3_u32 v15, v15, v181, v182\n"
    "v_max_u32 v15, v15, v183\n"
    "v_cmp_eq_u32 vcc, -1, v15\n"
    "s_waitcnt lgkmcnt(3)\n"
    "v_mfma_f32_32x32x16_bf16 v[96:111], v[56:59], v[144:147], v[96:111]\n"
    "ds_read_b128 v[52:55], v0 offset:37888\n"
    "s_cbranch_vccnz Lre242_%=\n"
    "Lok243_%=:\n"
    "v_mfma_f32_32x32x16_bf16 v[64:79], v[60:63], v[176:179], v[64:79]\n"
    "s_add_u32 s42, s51, 0x6000\n"
    "buffer_load_dwordx4 v[224:227], v0, s[24:27], s42 offen sc1\n"
    "ds_read_b128 v[56:59], v0 offset:14336\n"
    "s_waitcnt lgkmcnt(3)\n"
    "v_mfma_f32_32x32x16_bf16 v[80:95], v[40:43], v[176:179], v[80:95]\n"
    "ds_read_b128 v[60:63], v1 offset:14336\n"
    "v_mfma_f32_32x32x16_bf16 v[64:79], v[44:47], v[180:183], v[64:79]\n"
    "buffer_load_dwordx4 v[228:231], v0, s[24:27], s42 offen offset:1024 sc1\n"
    "ds_read_b128 v[40:43], v0 offset:15360\n"
    "s_waitcnt vmcnt(13)\n"
    "v_max3_u32 v15, v184, v185, v186\n"
    "s_waitcnt lgkmcnt(3)\n"
    "v_mfma_f32_32x32x16_bf16 v[80:95], v[48:51], v[180:183], v[80:95]\n"
    "ds_read_b128 v[44:47], v1 offset:15360\n"
    "s_waitcnt vmcnt(12)\n"
    "v_max3_u32 v15, v15, v187, v188\n"
    "v_max3_u32 v15, v15, v189, v190\n"
    "v_max_u32 v15, v15, v191\n"
    "v_cmp_eq_u32 vcc, -1, v15\n"
    "v_mfma_f32_32x32x16_bf16 v[96:111], v[52:55], v[148:151], v[96:111]\n"
    "ds_read_b128 v[48:51], v0 offset:38912\n"
    "s_cbranch_vccnz Lre244_%=\n"
    "Lok245_%=:\n"
    "s_waitcnt lgkmcnt(3)\n"
    "v_mfma_f32_32x32x16_bf16 v[64:79], v[56:59], v[184:187], v[64:79]\n"
    "buffer_load_dwordx4 v[232:235], v0, s[24:27], s42 offen offset:2048 sc1\n"
    "ds_read_b128 v[52:55], v0 offset:16384\n"
    "v_mfma_f32_32x32x16_bf16 v[80:95], v[60:63], v[184:187], v[80:95]\n"
    "ds_read_b128 v[56:59], v1 offset:16384\n"
    "s_waitcnt lgkmcnt(3)\n"
    "v_mfma_f32_32x32x16_bf16 v[64:79], v[40:43], v[188:191], v[64:79]\n"
    "buffer_load_dwordx4 v[236:239], v0, s[24:27], s42 offen offset:3072 sc1\n"
    "ds_read_b128 v[60:63], v0 offset:17408\n"
    "s_waitcnt vmcnt(13)\n"
    "v_max3_u32 v15, v192, v193, v194\n"
    "v_mfma_f32_32x32x16_bf16 v[80:95], v[44:47], v[188:191], v[80:95]\n"
    "ds_read_b128 v[40:43], v1 offset:17408\n"
    "s_waitcnt vmcnt(12)\n"
    "v_max3_u32 v15, v15, v195, v196\n"
    "v_max3_u32 v15, v15, v197, v198\n"
    "v_max_u32 v15, v15, v199\n"
    "v_cmp_eq_u32 vcc, -1, v15\n"
    "s_waitcnt lgkmcnt(3)\n"
    "v_mfma_f32_32x32x16_bf16 v[96:111], v[48:51], v[152:155], v[96:111]\n"
    "ds_read_b128 v[44:47], v0 offset:18432\n"
    "s_cbranch_vccnz Lre246_%=\n"
    "Lok247_%=:\n"
    "v_mfma_f32_32x32x16_bf16 v[64:79], v[52:55], v[192:195], v[64:79]\n"
    "s_add_u32 s42, s51, 0x7000\n"
    "buffer_load_dwordx4 v[240:243], v0, s[24:27], s42 offen sc1\n"
    "ds_read_b128 v[48:51], v1 offset:18432\n"
    "s_waitcnt lgkmcnt(3)\n"
    "v_mfma_f32_32x32x16_bf16 v[80:95], v[56:59], v[192:195], v[80:95]\n"
    "ds_read_b128 v[52:55], v0 offset:39936\n"
    "s_waitcnt vmcnt(12)\n"
    "v_max3_u32 v15, v200, v201, v202\n"
    "v_mfma_f32_32x32x16_bf16 v[64:79], v[60:63], v[196:199], v[64:79]\n"
    "ds_read_b128 v[56:59], v0 offset:19456\n"
    "s_waitcnt vmcnt(10)\n"
    "v_max3_u32 v15, v15, v203, v204\n"
    "v_max3_u32 v15, v15, v205, v206\n"
    "v_max_u32 v15, v15, v207\n"
    "v_cmp_eq_u32 vcc, -1, v15\n"
    "s_waitcnt lgkmcnt(3)\n"
    "v_mfma_f32_32x32x16_bf16 v[80:95], v[40:43], v[196:199], v[80:95]\n"
    "buffer_load_dwordx4 v[244:247], v0, s[24:27], s42 offen offset:1024 sc1\n"
    "ds_read_b128 v[60:63], v1 offset:19456\n"
    "s_cbranch_vccnz Lre248_%=\n"
    "Lok249_%=:\n"
    "v_mfma_f32_32x32x16_bf16 v[64:79], v[44:47], v[200:203], v[64:79]\n"
    "buffer_load_dwordx4 v[248:251], v0, s[24:27], s42 offen offset:2048 sc1\n"
    "ds_read_b128 v[40:43], v0 offset:20480\n"
    "s_waitcnt lgkmcnt(3)\n"
    "v_mfma_f32_32x32x16_bf16 v[80:95], v[48:51], v[200:203], v[80:95]\n"
    "ds_read_b128 v[44:47], v1 offset:20480\n"
    "v_mfma_f32_32x32x16_bf16 v[96:111], v[52:55], v[156:159], v[96:111]\n"
    "ds_read_b128 v[48:51], v0 offset:40960\n"
    "s_waitcnt vmcnt(11)\n"
    "v_max3_u32 v15, v208, v209, v210\n"
    "s_waitcnt lgkmcnt(3)\n"
    "v_mfma_f32_32x32x16_bf16 v[64:79], v[56:59], v[204:207], v[64:79]\n"
    "ds_read_b128 v[52:55], v0 offset:21504\n"
    "s_waitcnt vmcnt(10)\n"
    "v_max3_u32 v15, v15, v211, v212\n"
    "v_max3_u32 v15, v15, v213, v214\n"
    "v_max_u32 v15, v15, v215\n"
    "v_cmp_eq_u32 vcc, -1, v15\n"
    "v_mfma_f32_32x32x16_bf16 v[80:95], v[60:63], v[204:207], v[80:95]\n"
    "buffer_load_dwordx4 v[252:255], v0, s[24:27], s42 offen offset:3072 sc1\n"
    "ds_read_b128 v[56:59], v1 offset:21504\n"
    "s_cbranch_vccnz Lre250_%=\n"
    "Lok251_%=:\n"
    "s_waitcnt lgkmcnt(3)\n"
    "v_mfma_f32_32x32x16_bf16 v[64:79], v[40:43], v[208:211], v[64:79]\n"
    "ds_read_b128 v[60:63], v0 offset:22528\n"
    "v_mfma_f32_32x32x16_bf16 v[80:95], v[44:47], v[208:211], v[80:95]\n"
    "v_lshlrev_b32 v18, 1, v2\n"
    "buffer_store_dwordx2 v[36:37], v18, s[24:27], s54 offen sc1\n"
    "ds_read_b128 v[40:43], v1 offset:22528\n"
    "s_waitcnt vmcnt(10)\n"
    "v_max3_u32 v32, v119, v113, v114\n"
    "s_waitcnt lgkmcnt(3)\n"
    "v_mfma_f32_32x32x16_bf16 v[96:111], v[48:51], v[160:163], v[96:111]\n"
    "ds_read_b128 v[44:47], v0 offset:41984\n"
    "v_max3_u32 v15, v216, v217, v218\n"
    "v_max3_u32 v32, v32, v112, v115\n"
    "v_max3_u32 v32, v32, v116, v117\n"
    "v_max_u32 v32, v32, v118\n"
    "v_mfma_f32_32x32x16_bf16 v[64:79], v[52:55], v[212:215], v[64:79]\n"
    "ds_read_b128 v[48:51], v0 offset:23552\n"
    "s_waitcnt vmcnt(9)\n"
    "v_max3_u32 v15, v15, v219, v220\n"
    "v_max3_u32 v15, v15, v221, v222\n"
    "v_max_u32 v15, v15, v223\n"
    "v_cmp_eq_u32 vcc, -1, v15\n"
    "s_waitcnt lgkmcnt(3)\n"
    "v_mfma_f32_32x32x16_bf16 v[80:95], v[56:59], v[212:215], v[80:95]\n"
    "buffer_store_dwordx4 v[36:39], v14, s[24:27], s55 offen sc1\n"
    "ds_read_b128 v[52:55], v1 offset:23552\n"
    "s_cbranch_vccnz Lre252_%=\n"
    "Lok253_%=:\n"
    "v_cmp_eq_u32 vcc, -1, v32\n"
    "v_mfma_f32_32x32x16_bf16 v[64:79], v[60:63], v[216:219], v[64:79]\n"
    "ds_read_b128 v[56:59], v0 offset:24576\n"
    "s_cbranch_vccnz Lre262_%=\n"
    "Lok263_%=:\n"
    "v_add_f32 v24, v112, v116\n"
    "v_add_f32 v25, v113, v117\n"
    "v_add_f32 v26, v114, v118\n"
    "s_waitcnt lgkmcnt(3)\n"
    "v_mfma_f32_32x32x16_bf16 v[80:95], v[40:43], v[216:219], v[80:95]\n"
    "s_add_u32 m0, s78, 0x18000\n"
    "s_add_u32 s47, s68, 0x20800\n"
    "buffer_load_dwordx4 v0, s[24:27], s47 offen sc1 lds\n"
    "ds_read_b128 v[60:63], v1 offset:24576\n"
    "v_add_f32 v27, v115, v119\n"
    "v_mfma_f32_32x32x16_bf16 v[96:111], v[44:47], v[164:167], v[96:111]\n"
    "ds_read_b128 v[40:43], v0 offset:25600\n"
    "s_waitcnt vmcnt(10)\n"
    "v_max3_u32 v15, v224, v225, v226\n"
    "v_permlane32_swap_b32 v24, v25\n"
    "v_permlane32_swap_b32 v26, v27\n"
    "s_waitcnt lgkmcnt(3)\n"
    "v_mfma_f32_32x32x16_bf16 v[64:79], v[48:51], v[220:223], v[64:79]\n"
    "ds_read_b128 v[44:47], v1 offset:25600\n"
    "s_waitcnt vmcnt(9)\n"
    "v_max3_u32 v15, v15, v227, v228\n"
    "v_max3_u32 v15, v15, v229, v230\n"
    "v_max_u32 v15, v15, v231\n"
    "v_cmp_eq_u32 vcc, -1, v15\n"
    "v_mfma_f32_32x32x16_bf16 v[80:95], v[52:55], v[220:223], v[80:95]\n"
    "buffer_load_dwordx4 v0, s[24:27], s47 offen offset:1024 sc1 lds\n"
    "ds_read_b128 v[48:51], v0 offset:43008\n"
    "s_cbranch_vccnz Lre254_%=\n"
    "Lok255_%=:\n"
    "v_add_f32 v24, v24, v25\n"
    "s_waitcnt lgkmcnt(3)\n"
    "v_mfma_f32_32x32x16_bf16 v[64:79], v[56:59], v[224:227], v[64:79]\n"
    "ds_read_b128 v[52:55], v0 offset:26624\n"
    "v_add_f32 v26, v26, v27\n"
    "s_nop 1\n"
    "v_permlane16_swap_b32 v24, v26\n"
    "v_add_f32 v24, v24, v26\n"
    "ds_bpermute_b32 v28, v13, v24\n"
    "ds_bpermute_b32 v29, v13, v24 offset:32\n"
    "v_mfma_f32_32x32x16_bf16 v[80:95], v[60:63], v[224:227], v[80:95]\n"
    "buffer_load_dwordx4 v0, s[24:27], s47 offen offset:2048 sc1 lds\n"
    "ds_read_b128 v[56:59], v1 offset:26624\n"
    "s_waitcnt lgkmcnt(5)\n"
    "v_mfma_f32_32x32x16_bf16 v[64:79], v[40:43], v[228:231], v[64:79]\n"
    "ds_read_b128 v[60:63], v0 offset:27648\n"
    "s_waitcnt vmcnt(10)\n"
    "v_max3_u32 v15, v232, v233, v234\n"
    "s_waitcnt lgkmcnt(2)\n"
    "v_mul_f32 v28, 0xbb000000, v28\n"
    "v_mul_f32 v29, 0x3b000000, v29\n"
    "v_mfma_f32_32x32x16_bf16 v[80:95], v[44:47], v[228:231], v[80:95]\n"
    "ds_read_b128 v[40:43], v1 offset:27648\n"
    "s_waitcnt vmcnt(9)\n"
    "v_max3_u32 v15, v15, v235, v236\n"
    "v_max3_u32 v15, v15, v237, v238\n"
    "v_max_u32 v15, v15, v239\n"
    "v_cmp_eq_u32 vcc, -1, v15\n"
    "v_mfma_f32_32x32x16_bf16 v[96:111], v[48:51], v[168:171], v[96:111]\n"
    "buffer_load_dwordx4 v0, s[24:27], s47 offen offset:3072 sc1 lds\n"
    "ds_read_b128 v[44:47], v0 offset:44032\n"
    "s_cbranch_vccnz Lre256_%=\n"
    "Lok257_%=:\n"
    "v_cvt_pk_bf16_f32 v30, v28, v28\n"
    "v_fma_f32 v29, -v28, v28, v29\n"
    "v_mfma_f32_32x32x16_bf16 v[64:79], v[52:55], v[232:235], v[64:79]\n"
    "ds_read_b128 v[48:51], v0 offset:28672\n"
    "v_lshlrev_b32 v31, 16, v30\n"
    "v_max_f32 v29, 0, v29\n"
    "v_sub_f32 v31, v28, v31\n"
    "v_add_f32 v29, 0x3727c5ac, v29\n"
    "s_waitcnt lgkmcnt(3)\n"
    "v_mfma_f32_32x32x16_bf16 v[80:95], v[56:59], v[232:235], v[80:95]\n"
    "s_add_u32 m0, s78, 0x19000\n"
    "s_add_u32 s47, s69, 0x20800\n"
    "buffer_load_dwordx4 v0, s[24:27], s47 offen sc1 lds\n"
    "ds_read_b128 v[52:55], v1 offset:28672\n"
    "v_cvt_pk_bf16_f32 v31, v31, v31\n"
    "v_mfma_f32_32x32x16_bf16 v[64:79], v[60:63], v[236:239], v[64:79]\n"
    "ds_read_b128 v[56:59], v0 offset:29696\n"
    "s_waitcnt vmcnt(10)\n"
    "v_max3_u32 v15, v240, v241, v242\n"
    "v_rsq_f32 v16, v29\n"
    "s_waitcnt lgkmcnt(3)\n"
    "v_mfma_f32_32x32x16_bf16 v[80:95], v[40:43], v[236:239], v[80:95]\n"
    "ds_read_b128 v[60:63], v1 offset:29696\n"
    "s_waitcnt vmcnt(9)\n"
    "v_max3_u32 v15, v15, v243, v244\n"
    "v_max3_u32 v15, v15, v245, v246\n"
    "v_max_u32 v15, v15, v247\n"
    "v_cmp_eq_u32 vcc, -1, v15\n"
    "v_mfma_f32_32x32x16_bf16 v[96:111], v[44:47], v[172:175], v[96:111]\n"
    "buffer_load_dwordx4 v0, s[24:27], s47 offen offset:1024 sc1 lds\n"
    "ds_read_b128 v[40:43], v0 offset:45056\n"
    "s_cbranch_vccnz Lre258_%=\n"
    "Lok259_%=:\n"
    "v_permlane32_swap_b32 v30, v31\n"
    "s_waitcnt lgkmcnt(3)\n"
    "v_mfma_f32_32x32x16_bf16 v[64:79], v[48:51], v[240:243], v[64:79]\n"
    "ds_read_b128 v[44:47], v0 offset:30720\n"
    "v_mul_f32 v17, 0x3fb8aa3b, v16\n"
    "v_mov_b32 v20, v30\n"
    "v_mov_b32 v21, v30\n"
    "v_mov_b32 v22, v30\n"
    "v_mov_b32 v23, v30\n"
    "v_mfma_f32_32x32x16_bf16 v[80:95], v[52:55], v[240:243], v[80:95]\n"
    "buffer_load_dwordx4 v0, s[24:27], s47 offen offset:2048 sc1 lds\n"
    "ds_read_b128 v[48:51], v1 offset:30720\n"
    "s_waitcnt lgkmcnt(3)\n"
    "v_mfma_f32_32x32x16_bf16 v[64:79], v[56:59], v[244:247], v[64:79]\n"
    "ds_read_b128 v[52:55], v0 offset:31744\n"
    "s_waitcnt vmcnt(10)\n"
    "v_max3_u32 v15, v248, v249, v250\n"
    "v_mfma_f32_32x32x16_bf16 v[80:95], v[60:63], v[244:247], v[80:95]\n"
    "ds_read_b128 v[56:59], v1 offset:31744\n"
    "s_waitcnt vmcnt(9)\n"
    "v_max3_u32 v15, v15, v251, v252\n"
    "v_max3_u32 v15, v15, v253, v254\n"
    "v_max_u32 v15, v15, v255\n"
    "v_cmp_eq_u32 vcc, -1, v15\n"
    "s_waitcnt lgkmcnt(3)\n"
    "v_mfma_f32_32x32x16_bf16 v[96:111], v[40:43], v[176:179], v[96:111]\n"
    "buffer_load_dwordx4 v0, s[24:27], s47 offen offset:3072 sc1 lds\n"
    "ds_read_b128 v[60:63], v0 offset:46080\n"
    "s_cbranch_vccnz Lre260_%=\n"
    "Lok261_%=:\n"
    "v_mfma_f32_32x32x16_bf16 v[64:79], v[44:47], v[248:251], v[64:79]\n"
    "ds_read_b128 v[40:43], v4 offset:0\n"
    "s_waitcnt lgkmcnt(3)\n"
    "v_mfma_f32_32x32x16_bf16 v[80:95], v[48:51], v[248:251], v[80:95]\n"
    "s_add_u32 m0, s86, 0x600\n"
    "s_nop 0\n"
    "buffer_load_dword v3, s[24:27], s47 offen sc1 lds\n"
    "ds_read_b128 v[44:47], v4 offset:1024\n"
    "v_mfma_f32_32x32x16_bf16 v[64:79], v[52:55], v[252:255], v[64:79]\n"
    "s_waitcnt lgkmcnt(2)\n"
    "v_mfma_f32_32x32x16_bf16 v[80:95], v[56:59], v[252:255], v[80:95]\n"
    "v_mfma_f32_32x32x16_bf16 v[96:111], v[60:63], v[180:183], v[96:111]\n"
    "s_waitcnt lgkmcnt(0)\n"
    "v_mfma_f32_32x32x16_bf16 v[64:79], v[40:43], v[20:23], v[64:79]\n"
    "v_mfma_f32_32x32x16_bf16 v[80:95], v[44:47], v[20:23], v[80:95]\n"
    "s_add_u32 s51, s51, 0x240000\n"
    "v_and_b32 v15, 0x180, v0\n"
    "s_mov_b32 m0, 162528\n"
    "s_nop 0\n"
    "buffer_load_dword v15, s[28:31], s87 offen lds\n"
    "buffer_store_dwordx4 v[36:39], v0, s[24:27], s57 offen sc1\n"
    "s_nop 4\n"
    "v_cvt_pk_bf16_f32 v40, v64, v65\n"
    "v_cvt_pk_bf16_f32 v41, v66, v67\n"
    "v_cvt_pk_bf16_f32 v42, v68, v69\n"
    "v_cvt_pk_bf16_f32 v43, v70, v71\n"
    "v_cvt_pk_bf16_f32 v44, v72, v73\n"
    "v_cvt_pk_bf16_f32 v45, v74, v75\n"
    "v_cvt_pk_bf16_f32 v46, v76, v77\n"
    "v_cvt_pk_bf16_f32 v47, v78, v79\n"
    "v_mfma_f32_32x32x16_bf16 v[128:143], a[240:243], v[40:43], 0\n"
    "s_nop 0\n"
    "v_mfma_f32_32x32x16_bf16 v[128:143], a[244:247], v[44:47], v[128:143]\n"
    "v_mul_f32 v80, v16, v80\n"
    "v_mul_f32 v81, v16, v81\n"
    "v_mul_f32 v82, v16, v82\n"
    "v_mul_f32 v83, v16, v83\n"
    "v_mul_f32 v84, v16, v84\n"
    "v_mul_f32 v85, v16, v85\n"
    "v_mul_f32 v86, v16, v86\n"
    "v_mul_f32 v87, v16, v87\n"
    "v_mul_f32 v88, v16, v88\n"
    "v_mul_f32 v89, v16, v89\n"
    "v_mul_f32 v90, v16, v90\n"
    "v_mul_f32 v91, v16, v91\n"
    "v_mul_f32 v92, v16, v92\n"
    "v_mul_f32 v93, v16, v93\n"
    "v_mul_f32 v94, v16, v94\n"
    "s_add_u32 s44, s57, 0x1000\n"
    "buffer_store_dwordx4 v[36:39], v0, s[24:27], s44 offen sc1\n"
    "v_mul_f32 v95, v16, v95\n"
    "v_cvt_pk_bf16_f32 v80, v80, v81\n"
    "v_cvt_pk_bf16_f32 v81, v82, v83\n"
    "v_cvt_pk_bf16_f32 v82, v84, v85\n"
    "v_cvt_pk_bf16_f32 v83, v86, v87\n"
    "v_cvt_pk_bf16_f32 v84, v88, v89\n"
    "v_cvt_pk_bf16_f32 v85, v90, v91\n"
    "v_cvt_pk_bf16_f32 v86, v92, v93\n"
    "v_cvt_pk_bf16_f32 v87, v94, v95\n"
    "ds_write_b128 v6, v[80:83] offset:128\n"
    "ds_write_b128 v6, v[84:87] offset:144\n"
    "v_max3_f32 v24, v128, v129, v130\n"
    "v_min3_f32 v25, v128, v129, v130\n"
    "v_max3_f32 v24, v24, v131, v132\n"
    "v_min3_f32 v25, v25, v131, v132\n"
    "v_max3_f32 v24, v24, v133, v134\n"
    "v_min3_f32 v25, v25, v133, v134\n"
    "v_max3_f32 v24, v24, v135, v136\n"
    "v_min3_f32 v25, v25, v135, v136\n"
    "v_max3_f32 v24, v24, v137, v138\n"
    "v_min3_f32 v25, v25, v137, v138\n"
    "v_max3_f32 v24, v24, v139, v140\n"
    "v_min3_f32 v25, v25, v139, v140\n"
    "v_max3_f32 v24, v24, v141, v142\n"
    "v_min3_f32 v25, v25, v141, v142\n"
    "buffer_store_dwordx4 v[36:39], v0, s[24:27], s52 offen sc1\n"
    "v_max_f32 v24, v24, v143\n"
    "v_min_f32 v25, v25, v143\n"
    "v_mul_f32 v26, v17, v24\n"
    "v_mul_f32 v28, v17, v25\n"
    "v_pk_fma_f32 v[64:65], v[128:129], v[16:17], v[26:27] op_sel:[0,1,0] op_sel_hi:[1,1,0] neg_lo:[0,0,1] neg_hi:[0,0,1]\n"
    "v_pk_fma_f32 v[112:113], v[128:129], v[16:17], v[28:29] op_sel:[0,1,0] op_sel_hi:[1,1,0] neg_lo:[1,0,0] neg_hi:[1,0,0]\n"
    "v_pk_fma_f32 v[66:67], v[130:131], v[16:17], v[26:27] op_sel:[0,1,0] op_sel_hi:[1,1,0] neg_lo:[0,0,1] neg_hi:[0,0,1]\n"
    "v_pk_fma_f32 v[114:115], v[130:131], v[16:17], v[28:29] op_sel:[0,1,0] op_sel_hi:[1,1,0] neg_lo:[1,0,0] neg_hi:[1,0,0]\n"
    "v_pk_fma_f32 v[68:69], v[132:133], v[16:17], v[26:27] op_sel:[0,1,0] op_sel_hi:[1,1,0] neg_lo:[0,0,1] neg_hi:[0,0,1]\n"
    "v_pk_fma_f32 v[116:117], v[132:133], v[16:17], v[28:29] op_sel:[0,1,0] op_sel_hi:[1,1,0] neg_lo:[1,0,0] neg_hi:[1,0,0]\n"
    "v_pk_fma_f32 v[70:71], v[134:135], v[16:17], v[26:27] op_sel:[0,1,0] op_sel_hi:[1,1,0] neg_lo:[0,0,1] neg_hi:[0,0,1]\n"
    "v_pk_fma_f32 v[118:119], v[134:135], v[16:17], v[28:29] op_sel:[0,1,0] op_sel_hi:[1,1,0] neg_lo:[1,0,0] neg_hi:[1,0,0]\n"
    "v_pk_fma_f32 v[72:73], v[136:137], v[16:17], v[26:27] op_sel:[0,1,0] op_sel_hi:[1,1,0] neg_lo:[0,0,1] neg_hi:[0,0,1]\n"
    "v_pk_fma_f32 v[120:121], v[136:137], v[16:17], v[28:29] op_sel:[0,1,0] op_sel_hi:[1,1,0] neg_lo:[1,0,0] neg_hi:[1,0,0]\n"
    "v_pk_fma_f32 v[74:75], v[138:139], v[16:17], v[26:27] op_sel:[0,1,0] op_sel_hi:[1,1,0] neg_lo:[0,0,1] neg_hi:[0,0,1]\n"
    "v_pk_fma_f32 v[122:123], v[138:139], v[16:17], v[28:29] op_sel:[0,1,0] op_sel_hi:[1,1,0] neg_lo:[1,0,0] neg_hi:[1,0,0]\n"
    "v_pk_fma_f32 v[76:77], v[140:141], v[16:17], v[26:27] op_sel:[0,1,0] op_sel_hi:[1,1,0] neg_lo:[0,0,1] neg_hi:[0,0,1]\n"
    "v_pk_fma_f32 v[124:125], v[140:141], v[16:17], v[28:29] op_sel:[0,1,0] op_sel_hi:[1,1,0] neg_lo:[1,0,0] neg_hi:[1,0,0]\n"
    "v_pk_fma_f32 v[78:79], v[142:143], v[16:17], v[26:27] op_sel:[0,1,0] op_sel_hi:[1,1,0] neg_lo:[0,0,1] neg_hi:[0,0,1]\n"
    "v_pk_fma_f32 v[126:127], v[142:143], v[16:17], v[28:29] op_sel:[0,1,0] op_sel_hi:[1,1,0] neg_lo:[1,0,0] neg_hi:[1,0,0]\n"
    "v_exp_f32 v64, v64\n"
    "v_exp_f32 v65, v65\n"
    "v_exp_f32 v66, v66\n"
    "v_exp_f32 v67, v67\n"
    "v_exp_f32 v68, v68\n"
    "buffer_store_dwordx4 v[36:39], v0, s[24:27], s52 offen offset:1024 sc1\n"
    "v_exp_f32 v69, v69\n"
    "v_exp_f32 v70, v70\n"
    "v_exp_f32 v71, v71\n"
    "v_exp_f32 v72, v72\n"
    "v_exp_f32 v73, v73\n"
    "v_exp_f32 v74, v74\n"
    "v_exp_f32 v75, v75\n"
    "v_exp_f32 v76, v76\n"
    "v_exp_f32 v77, v77\n"
    "v_exp_f32 v78, v78\n"
    "v_exp_f32 v79, v79\n"
    "v_pk_add_f32 v[24:25], v[64:65], v[66:67]\n"
    "v_pk_add_f32 v[26:27], v[68:69], v[70:71]\n"
    "v_pk_add_f32 v[24:25], v[24:25], v[72:73]\n"
    "v_pk_add_f32 v[26:27], v[26:27], v[74:75]\n"
    "v_pk_add_f32 v[24:25], v[24:25], v[76:77]\n"
    "v_pk_add_f32 v[26:27], v[26:27], v[78:79]\n"
    "v_pk_add_f32 v[24:25], v[24:25], v[26:27]\n"
    "v_add_f32 v24, v24, v25\n"
    "v_rcp_f32 v24, v24\n"
    "s_nop 0\n"
    "v_pk_mul_f32 v[64:65], v[64:65], v[24:25] op_sel_hi:[1,0]\n"
    "v_pk_mul_f32 v[66:67], v[66:67], v[24:25] op_sel_hi:[1,0]\n"
    "v_pk_mul_f32 v[68:69], v[68:69], v[24:25] op_sel_hi:[1,0]\n"
    "v_pk_mul_f32 v[70:71], v[70:71], v[24:25] op_sel_hi:[1,0]\n"
    "buffer_store_dword v36, v2, s[24:27], s53 offen sc1\n"
    "v_cvt_pk_bf16_f32 v128, v64, v65\n"
    "v_cvt_pk_bf16_f32 v129, v66, v67\n"
    "v_cvt_pk_bf16_f32 v130, v68, v69\n"
    "v_cvt_pk_bf16_f32 v131, v70, v71\n"
    "ds_write_b128 v5, v[128:131]\n"
    "v_pk_mul_f32 v[72:73], v[72:73], v[24:25] op_sel_hi:[1,0]\n"
    "v_pk_mul_f32 v[74:75], v[74:75], v[24:25] op_sel_hi:[1,0]\n"
    "v_pk_mul_f32 v[76:77], v[76:77], v[24:25] op_sel_hi:[1,0]\n"
    "v_pk_mul_f32 v[78:79], v[78:79], v[24:25] op_sel_hi:[1,0]\n"
    "v_cvt_pk_bf16_f32 v132, v72, v73\n"
    "v_cvt_pk_bf16_f32 v133, v74, v75\n"
    "v_cvt_pk_bf16_f32 v134, v76, v77\n"
    "v_cvt_pk_bf16_f32 v135, v78, v79\n"
    "ds_write_b128 v5, v[132:135] offset:16\n"
    "v_exp_f32 v112, v112\n"
    "v_exp_f32 v113, v113\n"
    "v_exp_f32 v114, v114\n"
    "v_exp_f32 v115, v115\n"
    "v_exp_f32 v116, v116\n"
    "v_exp_f32 v117, v117\n"
    "v_exp_f32 v118, v118\n"
    "v_exp_f32 v119, v119\n"
    "v_exp_f32 v120, v120\n"
    "v_exp_f32 v121, v121\n"
    "v_exp_f32 v122, v122\n"
    "s_add_u32 s44, s60, 0x4000\n"
    "buffer_store_dwordx4 v[36:39], v0, s[24:27], s44 offen sc1\n"
    "v_exp_f32 v123, v123\n"
    "v_exp_f32 v124, v124\n"
    "v_exp_f32 v125, v125\n"
    "v_exp_f32 v126, v126\n"
    "v_exp_f32 v127, v127\n"
    "v_pk_add_f32 v[30:31], v[112:113], v[114:115]\n"
    "v_pk_add_f32 v[26:27], v[116:117], v[118:119]\n"
    "v_pk_add_f32 v[30:31], v[30:31], v[120:121]\n"
    "v_pk_add_f32 v[26:27], v[26:27], v[122:123]\n"
    "v_pk_add_f32 v[30:31], v[30:31], v[124:125]\n"
    "v_pk_add_f32 v[26:27], v[26:27], v[126:127]\n"
    "v_pk_add_f32 v[30:31], v[30:31], v[26:27]\n"
    "v_add_f32 v30, v30, v31\n"
    "v_rcp_f32 v30, v30\n"
    "s_nop 0\n"
    "v_pk_mul_f32 v[112:113], v[112:113], v[30:31] op_sel_hi:[1,0]\n"
    "v_pk_mul_f32 v[114:115], v[114:115], v[30:31] op_sel_hi:[1,0]\n"
    "v_pk_mul_f32 v[116:117], v[116:117], v[30:31] op_sel_hi:[1,0]\n"
    "v_pk_mul_f32 v[118:119], v[118:119], v[30:31] op_sel_hi:[1,0]\n"
    "v_cvt_pk_bf16_f32 v136, v112, v113\n"
    "v_cvt_pk_bf16_f32 v137, v114, v115\n"
    "v_cvt_pk_bf16_f32 v138, v116, v117\n"
    "v_cvt_pk_bf16_f32 v139, v118, v119\n"
    "ds_write_b128 v5, v[136:139] offset:32\n"
    "v_pk_mul_f32 v[120:121], v[120:121], v[30:31] op_sel_hi:[1,0]\n"
    "v_pk_mul_f32 v[122:123], v[122:123], v[30:31] op_sel_hi:[1,0]\n"
    "v_pk_mul_f32 v[124:125], v[124:125], v[30:31] op_sel_hi:[1,0]\n"
    "v_pk_mul_f32 v[126:127], v[126:127], v[30:31] op_sel_hi:[1,0]\n"
    "v_cvt_pk_bf16_f32 v140, v120, v121\n"
    "v_cvt_pk_bf16_f32 v141, v122, v123\n"
    "v_cvt_pk_bf16_f32 v142, v124, v125\n"
    "v_cvt_pk_bf16_f32 v143, v126, v127\n"
    "ds_write_b128 v5, v[140:143] offset:48\n"
    "v_mov_b32 v24, 0x3f803f80\n"
    "v_mov_b32 v25, 0x3f803f80\n"
    "v_mov_b32 v26, 0x3f803f80\n"
    "v_mov_b32 v27, 0x3f803f80\n"
    "s_waitcnt vmcnt(16)\n"
    "s_waitcnt lgkmcnt(0)\n"
    "s_barrier\n"
    "ds_read_b64_tr_b16 v[64:65], v7\n"
    "ds_read_b64_tr_b16 v[66:67], v7 offset:832\n"
    "ds_read_b64_tr_b16 v[68:69], v8\n"
    "ds_read_b64_tr_b16 v[70:71], v8 offset:832\n"
    "ds_read_b64_tr_b16 v[72:73], v7 offset:6656\n"
    "ds_read_b64_tr_b16 v[74:75], v7 offset:7488\n"
    "ds_read_b64_tr_b16 v[76:77], v8 offset:6656\n"
    "ds_read_b64_tr_b16 v[78:79], v8 offset:7488\n"
    "ds_read_b64_tr_b16 v[80:81], v7 offset:13312\n"
    "ds_read_b64_tr_b16 v[82:83], v7 offset:14144\n"
    "ds_read_b64_tr_b16 v[84:85], v8 offset:13312\n"
    "ds_read_b64_tr_b16 v[86:87], v8 offset:14144\n"
    "ds_read_b64_tr_b16 v[88:89], v7 offset:19968\n"
    "ds_read_b64_tr_b16 v[90:91], v7 offset:20800\n"
    "ds_read_b64_tr_b16 v[92:93], v8 offset:19968\n"
    "ds_read_b64_tr_b16 v[94:95], v8 offset:20800\n"
    "s_waitcnt lgkmcnt(12)\n"
    "v_mfma_f32_16x16x32_bf16 v[112:115], v[64:67], v[68:71], 0\n"
    "v_mfma_f32_16x16x32_bf16 v[116:119], v[64:67], v[24:27], 0\n"
    "s_waitcnt lgkmcnt(8)\n"
    "v_mfma_f32_16x16x32_bf16 v[112:115], v[72:75], v[76:79], v[112:115]\n"
    "v_mfma_f32_16x16x32_bf16 v[116:119], v[72:75], v[24:27], v[116:119]\n"
    "s_waitcnt lgkmcnt(4)\n"
    "v_mfma_f32_16x16x32_bf16 v[112:115], v[80:83], v[84:87], v[112:115]\n"
    "v_mfma_f32_16x16x32_bf16 v[116:119], v[80:83], v[24:27], v[116:119]\n"
    "s_waitcnt lgkmcnt(0)\n"
    "v_mfma_f32_16x16x32_bf16 v[112:115], v[88:91], v[92:95], v[112:115]\n"
    "v_mfma_f32_16x16x32_bf16 v[116:119], v[88:91], v[24:27], v[116:119]\n"
    "v_lshlrev_b32 v33, 7, v0\n"
    "v_mov_b32 v34, 1\n"
    "s_mov_b64 exec, s[18:19]\n"
    "buffer_atomic_add v34, v33, s[24:27], s89 offen offset:20 sc1\n"
    "s_mov_b64 exec, -1\n"
    "ds_read_b128 v[40:43], v0 offset:47104\n"
    "ds_read_b128 v[44:47], v0 offset:48128\n"
    "ds_read_b128 v[48:51], v0 offset:49152\n"
    "ds_read_b128 v[52:55], v0 offset:50176\n"
    "ds_read_b128 v[56:59], v0 offset:51200\n"
    "v_cvt_pk_bf16_f32 v112, v112, v113\n"
    "v_cvt_pk_bf16_f32 v113, v114, v115\n"
    "buffer_store_dwordx2 v[112:113], v18, s[24:27], s54 offen sc1\n"
    "buffer_store_dwordx4 v[116:119], v14, s[24:27], s55 offen sc1\n"
    "s_waitcnt lgkmcnt(3)\n"
    "v_mfma_f32_32x32x16_bf16 v[96:111], v[40:43], v[184:187], v[96:111]\n"
    "ds_read_b128 v[60:63], v0 offset:52224\n"
    "v_and_b32 v32, 0x3c0, v0\n"
    "v_add_u32 v32, v32, v0\n"
    "v_and_b32 v168, 0x300, v0\n"
    "v_add_u32 v168, v168, v0\n"
    "v_bfe_u32 v169, v0, 5, 3\n"
    "v_mfma_f32_32x32x16_bf16 v[96:111], v[44:47], v[188:191], v[96:111]\n"
    "s_add_u32 m0, s78, 0x0\n"
    "s_add_u32 s47, s68, 0x82000\n"
    "buffer_load_dwordx4 v0, s[24:27], s47 offen sc1 lds\n"
    "ds_read_b128 v[40:43], v0 offset:53248\n"
    "s_waitcnt lgkmcnt(3)\n"
    "v_mfma_f32_32x32x16_bf16 v[96:111], v[48:51], v[192:195], v[96:111]\n"
    "v_mov_b32 v33, 0\n"
    "s_mov_b64 exec, s[16:17]\n"
    "buffer_atomic_add v33, off, s[24:27], s91 offset:20 sc0 sc1\n"
    "s_mov_b64 exec, -1\n"
    "ds_read_b128 v[44:47], v0 offset:54272\n"
    "v_mfma_f32_32x32x16_bf16 v[96:111], v[52:55], v[196:199], v[96:111]\n"
    "buffer_load_dwordx4 v0, s[24:27], s47 offen offset:1024 sc1 lds\n"
    "ds_read_b128 v[48:51], v0 offset:55296\n"
    "v_and_b32 v170, 16, v0\n"
    "v_mov_b32 v171, 0x3f80\n"
    "s_waitcnt lgkmcnt(3)\n"
    "v_mfma_f32_32x32x16_bf16 v[96:111], v[56:59], v[200:203], v[96:111]\n"
    "ds_read_b128 v[52:55], v0 offset:56320\n"
    "v_lshlrev_b32 v170, v170, v171\n"
    "v_cmp_eq_u32 vcc, 0, v169\n"
    "s_nop 1\n"
    "v_cndmask_b32 v160, 0, v170, vcc\n"
    "v_mfma_f32_32x32x16_bf16 v[96:111], v[60:63], v[204:207], v[96:111]\n"
    "buffer_load_dwordx4 v0, s[24:27], s47 offen offset:2048 sc1 lds\n"
    "ds_read_b128 v[56:59], v0 offset:57344\n"
    "s_waitcnt lgkmcnt(3)\n"
    "v_mfma_f32_32x32x16_bf16 v[96:111], v[40:43], v[208:211], v[96:111]\n"
    "ds_read_b128 v[60:63], v0 offset:58368\n"
    "v_cmp_eq_u32 vcc, 1, v169\n"
    "s_nop 1\n"
    "v_cndmask_b32 v161, 0, v170, vcc\n"
    "v_mfma_f32_32x32x16_bf16 v[96:111], v[44:47], v[212:215], v[96:111]\n"
    "buffer_load_dwordx4 v0, s[24:27], s47 offen offset:3072 sc1 lds\n"
    "ds_read_b128 v[40:43], v0 offset:59392\n"
    "s_waitcnt lgkmcnt(3)\n"
    "v_mfma_f32_32x32x16_bf16 v[96:111], v[48:51], v[216:219], v[96:111]\n"
    "ds_read_b128 v[44:47], v0 offset:60416\n"
    "v_cmp_eq_u32 vcc, 2, v169\n"
    "s_nop 1\n"
    "v_cndmask_b32 v162, 0, v170, vcc\n"
    "v_mfma_f32_32x32x16_bf16 v[96:111], v[52:55], v[220:223], v[96:111]\n"
    "ds_read_b128 v[48:51], v0 offset:61440\n"
    "v_cmp_eq_u32 vcc, 3, v169\n"
    "s_nop 1\n"
    "v_cndmask_b32 v163, 0, v170, vcc\n"
    "s_waitcnt lgkmcnt(3)\n"
    "v_mfma_f32_32x32x16_bf16 v[96:111], v[56:59], v[224:227], v[96:111]\n"
    "v_mov_b32 v34, 0\n"
    "s_mov_b64 exec, s[16:17]\n"
    "buffer_atomic_add v34, off, s[24:27], s91 offset:20 sc0 sc1\n"
    "s_mov_b64 exec, -1\n"
    "ds_read_b128 v[52:55], v0 offset:62464\n"
    "v_mfma_f32_32x32x16_bf16 v[96:111], v[60:63], v[228:231], v[96:111]\n"
    "ds_read_b128 v[56:59], v0 offset:63488\n"
    "v_cmp_eq_u32 vcc, 4, v169\n"
    "s_nop 1\n"
    "v_cndmask_b32 v164, 0, v170, vcc\n"
    "s_waitcnt lgkmcnt(3)\n"
    "v_mfma_f32_32x32x16_bf16 v[96:111], v[40:43], v[232:235], v[96:111]\n"
    "ds_read_b128 v[60:63], v0 offset:64512\n"
    "v_cmp_eq_u32 vcc, 5, v169\n"
    "s_nop 1\n"
    "v_cndmask_b32 v165, 0, v170, vcc\n"
    "v_mfma_f32_32x32x16_bf16 v[96:111], v[44:47], v[236:239], v[96:111]\n"
    "ds_read_b128 v[40:43], v4 offset:512\n"
    "v_cmp_eq_u32 vcc, 6, v169\n"
    "s_nop 1\n"
    "v_cndmask_b32 v166, 0, v170, vcc\n"
    "s_waitcnt lgkmcnt(3)\n"
    "v_mfma_f32_32x32x16_bf16 v[96:111], v[48:51], v[240:243], v[96:111]\n"
    "v_cmp_eq_u32 vcc, 7, v169\n"
    "s_nop 1\n"
    "v_cndmask_b32 v167, 0, v170, vcc\n"
    "v_mfma_f32_32x32x16_bf16 v[96:111], v[52:55], v[244:247], v[96:111]\n"
    "s_waitcnt lgkmcnt(1)\n"
    "v_mfma_f32_32x32x16_bf16 v[96:111], v[56:59], v[248:251], v[96:111]\n"
    "v_mfma_f32_32x32x16_bf16 v[96:111], v[60:63], v[252:255], v[96:111]\n"
    "s_waitcnt lgkmcnt(0)\n"
    "v_mfma_f32_32x32x16_bf16 v[96:111], v[40:43], v[20:23], v[96:111]\n"
    "s_cmp_lg_u32 s37, 0\n"
    "s_cbranch_scc1 Lhs266_%=\n"
    "s_mov_b64 exec, 1\n"
    "v_mov_b32 v35, 0\n"
    "buffer_atomic_add v35, off, s[24:27], s91 offset:20 sc0 sc1\n"
    "s_waitcnt vmcnt(5)\n"
    "v_cmp_eq_u32 vcc, 16, v33\n"
    "s_cbranch_vccnz Lhd265_%=\n"
    "v_mov_b32 v33, 0\n"
    "buffer_atomic_add v33, off, s[24:27], s91 offset:20 sc0 sc1\n"
    "s_waitcnt vmcnt(2)\n"
    "v_cmp_eq_u32 vcc, 16, v34\n"
    "s_cbranch_vccnz Lhd265_%=\n"
    "v_mov_b32 v34, 0\n"
    "buffer_atomic_add v34, off, s[24:27], s91 offset:20 sc0 sc1\n"
    "Lhp264_%=:\n"
    "s_waitcnt vmcnt(2)\n"
    "v_cmp_eq_u32 vcc, 16, v35\n"
    "s_cbranch_vccnz Lhd265_%=\n"
    "v_mov_b32 v35, 0\n"
    "buffer_atomic_add v35, off, s[24:27], s91 offset:20 sc0 sc1\n"
    "s_waitcnt vmcnt(2)\n"
    "v_cmp_eq_u32 vcc, 16, v33\n"
    "s_cbranch_vccnz Lhd265_%=\n"
    "v_mov_b32 v33, 0\n"
    "buffer_atomic_add v33, off, s[24:27], s91 offset:20 sc0 sc1\n"
    "s_waitcnt vmcnt(2)\n"
    "v_cmp_eq_u32 vcc, 16, v34\n"
    "s_cbranch_vccnz Lhd265_%=\n"
    "v_mov_b32 v34, 0\n"
    "buffer_atomic_add v34, off, s[24:27], s91 offset:20 sc0 sc1\n"
    "s_sub_u32 s50, s50, 1\n"
    "s_cbranch_scc0 Lhp264_%=\n"
    "s_branch Lfail_%=\n"
    "Lhd265_%=:\n"
    "s_mov_b64 exec, -1\n"
    "Lhs266_%=:\n"
    "s_barrier\n"
    "s_add_u32 s42, s56, 0x2000\n"
    "buffer_load_dwordx4 v[192:195], v32, s[24:27], s42 offen sc1\n"
    "s_add_u32 s42, s56, s74\n"
    "buffer_load_dwordx4 v[128:131], v168, s[24:27], s42 offen sc1\n"
    "buffer_load_dwordx4 v[132:135], v168, s[24:27], s42 offen offset:256 sc1\n"
    "s_nop 3\n"
    "v_cvt_pk_bf16_f32 v40, v96, v97\n"
    "v_cvt_pk_bf16_f32 v41, v98, v99\n"
    "v_cvt_pk_bf16_f32 v42, v100, v101\n"
    "v_cvt_pk_bf16_f32 v43, v102, v103\n"
    "v_cvt_pk_bf16_f32 v44, v104, v105\n"
    "v_cvt_pk_bf16_f32 v45, v106, v107\n"
    "v_cvt_pk_bf16_f32 v46, v108, v109\n"
    "v_cvt_pk_bf16_f32 v47, v110, v111\n"
    "v_mfma_f32_32x32x16_bf16 v[112:127], a[240:243], v[40:43], 0\n"
    "s_nop 0\n"
    "v_mfma_f32_32x32x16_bf16 v[112:127], a[244:247], v[44:47], v[112:127]\n"
    "s_add_u32 s42, s56, s75\n"
    "buffer_load_dwordx4 v[136:139], v168, s[24:27], s42 offen sc1\n"
    "buffer_load_dwordx4 v[140:143], v168, s[24:27], s42 offen offset:256 sc1\n"
    "s_nop 8\n"
    "v_max3_f32 v24, v112, v113, v114\n"
    "v_min3_f32 v25, v112, v113, v114\n"
    "s_add_u32 s42, s56, s76\n"
    "buffer_load_dwordx4 v[144:147], v168, s[24:27], s42 offen sc1\n"
    "v_max3_f32 v24, v24, v115, v116\n"
    "v_min3_f32 v25, v25, v115, v116\n"
    "v_max3_f32 v24, v24, v117, v118\n"
    "v_min3_f32 v25, v25, v117, v118\n"
    "v_max3_f32 v24, v24, v119, v120\n"
    "v_min3_f32 v25, v25, v119, v120\n"
    "v_max3_f32 v24, v24, v121, v122\n"
    "v_min3_f32 v25, v25, v121, v122\n"
    "v_max3_f32 v24, v24, v123, v124\n"
    "buffer_load_dwordx4 v[148:151], v168, s[24:27], s42 offen offset:256 sc1\n"
    "v_min3_f32 v25, v25, v123, v124\n"
    "v_max3_f32 v24, v24, v125, v126\n"
    "v_min3_f32 v25, v25, v125, v126\n"
    "v_max_f32 v24, v24, v127\n"
    "v_min_f32 v25, v25, v127\n"
    "v_mul_f32 v26, v17, v24\n"
    "v_mul_f32 v28, v17, v25\n"
    "v_pk_fma_f32 v[64:65], v[112:113], v[16:17], v[26:27] op_sel:[0,1,0] op_sel_hi:[1,1,0] neg_lo:[0,0,1] neg_hi:[0,0,1]\n"
    "v_pk_fma_f32 v[80:81], v[112:113], v[16:17], v[28:29] op_sel:[0,1,0] op_sel_hi:[1,1,0] neg_lo:[1,0,0] neg_hi:[1,0,0]\n"
    "s_add_u32 s42, s56, s77\n"
    "buffer_load_dwordx4 v[152:155], v168, s[24:27], s42 offen sc1\n"
    "v_pk_fma_f32 v[66:67], v[114:115], v[16:17], v[26:27] op_sel:[0,1,0] op_sel_hi:[1,1,0] neg_lo:[0,0,1] neg_hi:[0,0,1]\n"
    "v_pk_fma_f32 v[82:83], v[114:115], v[16:17], v[28:29] op_sel:[0,1,0] op_sel_hi:[1,1,0] neg_lo:[1,0,0] neg_hi:[1,0,0]\n"
    "v_pk_fma_f32 v[68:69], v[116:117], v[16:17], v[26:27] op_sel:[0,1,0] op_sel_hi:[1,1,0] neg_lo:[0,0,1] neg_hi:[0,0,1]\n"
    "v_pk_fma_f32 v[84:85], v[116:117], v[16:17], v[28:29] op_sel:[0,1,0] op_sel_hi:[1,1,0] neg_lo:[1,0,0] neg_hi:[1,0,0]\n"
    "v_pk_fma_f32 v[70:71], v[118:119], v[16:17], v[26:27] op_sel:[0,1,0] op_sel_hi:[1,1,0] neg_lo:[0,0,1] neg_hi:[0,0,1]\n"
    "v_pk_fma_f32 v[86:87], v[118:119], v[16:17], v[28:29] op_sel:[0,1,0] op_sel_hi:[1,1,0] neg_lo:[1,0,0] neg_hi:[1,0,0]\n"
    "v_pk_fma_f32 v[72:73], v[120:121], v[16:17], v[26:27] op_sel:[0,1,0] op_sel_hi:[1,1,0] neg_lo:[0,0,1] neg_hi:[0,0,1]\n"
    "v_pk_fma_f32 v[88:89], v[120:121], v[16:17], v[28:29] op_sel:[0,1,0] op_sel_hi:[1,1,0] neg_lo:[1,0,0] neg_hi:[1,0,0]\n"
    "buffer_load_dwordx4 v[156:159], v168, s[24:27], s42 offen offset:256 sc1\n"
    "v_pk_fma_f32 v[74:75], v[122:123], v[16:17], v[26:27] op_sel:[0,1,0] op_sel_hi:[1,1,0] neg_lo:[0,0,1] neg_hi:[0,0,1]\n"
    "v_pk_fma_f32 v[90:91], v[122:123], v[16:17], v[28:29] op_sel:[0,1,0] op_sel_hi:[1,1,0] neg_lo:[1,0,0] neg_hi:[1,0,0]\n"
    "v_pk_fma_f32 v[76:77], v[124:125], v[16:17], v[26:27] op_sel:[0,1,0] op_sel_hi:[1,1,0] neg_lo:[0,0,1] neg_hi:[0,0,1]\n"
    "v_pk_fma_f32 v[92:93], v[124:125], v[16:17], v[28:29] op_sel:[0,1,0] op_sel_hi:[1,1,0] neg_lo:[1,0,0] neg_hi:[1,0,0]\n"
    "v_pk_fma_f32 v[78:79], v[126:127], v[16:17], v[26:27] op_sel:[0,1,0] op_sel_hi:[1,1,0] neg_lo:[0,0,1] neg_hi:[0,0,1]\n"
    "v_pk_fma_f32 v[94:95], v[126:127], v[16:17], v[28:29] op_sel:[0,1,0] op_sel_hi:[1,1,0] neg_lo:[1,0,0] neg_hi:[1,0,0]\n"
    "v_exp_f32 v64, v64\n"
    "v_exp_f32 v65, v65\n"
    "v_exp_f32 v66, v66\n"
    "v_exp_f32 v67, v67\n"
    "v_exp_f32 v68, v68\n"
    "v_exp_f32 v69, v69\n"
    "v_exp_f32 v70, v70\n"
    "v_exp_f32 v71, v71\n"
    "v_exp_f32 v72, v72\n"
    "v_exp_f32 v73, v73\n"
    "v_exp_f32 v74, v74\n"
    "v_exp_f32 v75, v75\n"
    "s_add_u32 m0, s78, 0x1000\n"
    "s_add_u32 s47, s69, 0x82000\n"
    "buffer_load_dwordx4 v0, s[24:27], s47 offen sc1 lds\n"
    "v_exp_f32 v76, v76\n"
    "v_exp_f32 v77, v77\n"
    "v_exp_f32 v78, v78\n"
    "v_exp_f32 v79, v79\n"
    "v_pk_add_f32 v[24:25], v[64:65], v[66:67]\n"
    "v_pk_add_f32 v[26:27], v[68:69], v[70:71]\n"
    "v_pk_add_f32 v[24:25], v[24:25], v[72:73]\n"
    "v_pk_add_f32 v[26:27], v[26:27], v[74:75]\n"
    "v_pk_add_f32 v[24:25], v[24:25], v[76:77]\n"
    "buffer_load_dwordx4 v0, s[24:27], s47 offen offset:1024 sc1 lds\n"
    "v_pk_add_f32 v[26:27], v[26:27], v[78:79]\n"
    "v_pk_add_f32 v[24:25], v[24:25], v[26:27]\n"
    "v_add_f32 v24, v24, v25\n"
    "v_rcp_f32 v24, v24\n"
    "s_nop 0\n"
    "v_pk_mul_f32 v[64:65], v[64:65], v[24:25] op_sel_hi:[1,0]\n"
    "v_pk_mul_f32 v[66:67], v[66:67], v[24:25] op_sel_hi:[1,0]\n"
    "v_pk_mul_f32 v[68:69], v[68:69], v[24:25] op_sel_hi:[1,0]\n"
    "v_pk_mul_f32 v[70:71], v[70:71], v[24:25] op_sel_hi:[1,0]\n"
    "v_cvt_pk_bf16_f32 v112, v64, v65\n"
    "buffer_load_dwordx4 v0, s[24:27], s47 offen offset:2048 sc1 lds\n"
    "v_cvt_pk_bf16_f32 v113, v66, v67\n"
    "v_cvt_pk_bf16_f32 v114, v68, v69\n"
    "v_cvt_pk_bf16_f32 v115, v70, v71\n"
    "v_pk_mul_f32 v[72:73], v[72:73], v[24:25] op_sel_hi:[1,0]\n"
    "v_pk_mul_f32 v[74:75], v[74:75], v[24:25] op_sel_hi:[1,0]\n"
    "v_pk_mul_f32 v[76:77], v[76:77], v[24:25] op_sel_hi:[1,0]\n"
    "v_pk_mul_f32 v[78:79], v[78:79], v[24:25] op_sel_hi:[1,0]\n"
    "v_cvt_pk_bf16_f32 v116, v72, v73\n"
    "buffer_load_dwordx4 v0, s[24:27], s47 offen offset:3072 sc1 lds\n"
    "v_cvt_pk_bf16_f32 v117, v74, v75\n"
    "v_cvt_pk_bf16_f32 v118, v76, v77\n"
    "v_cvt_pk_bf16_f32 v119, v78, v79\n"
    "v_exp_f32 v80, v80\n"
    "v_exp_f32 v81, v81\n"
    "v_exp_f32 v82, v82\n"
    "v_exp_f32 v83, v83\n"
    "v_exp_f32 v84, v84\n"
    "v_exp_f32 v85, v85\n"
    "s_add_u32 m0, s86, 0x0\n"
    "s_nop 0\n"
    "buffer_load_dword v3, s[24:27], s47 offen sc1 lds\n"
    "v_exp_f32 v86, v86\n"
    "v_exp_f32 v87, v87\n"
    "v_exp_f32 v88, v88\n"
    "v_exp_f32 v89, v89\n"
    "v_exp_f32 v90, v90\n"
    "v_exp_f32 v91, v91\n"
    "v_exp_f32 v92, v92\n"
    "v_exp_f32 v93, v93\n"
    "s_add_u32 m0, s78, 0x8000\n"
    "s_add_u32 s47, s68, 0x61800\n"
    "buffer_load_dwordx4 v0, s[24:27], s47 offen sc1 lds\n"
    "v_exp_f32 v94, v94\n"
    "v_exp_f32 v95, v95\n"
    "v_pk_add_f32 v[30:31], v[80:81], v[82:83]\n"
    "v_pk_add_f32 v[26:27], v[84:85], v[86:87]\n"
    "v_pk_add_f32 v[30:31], v[30:31], v[88:89]\n"
    "v_pk_add_f32 v[26:27], v[26:27], v[90:91]\n"
    "v_pk_add_f32 v[30:31], v[30:31], v[92:93]\n"
    "v_pk_add_f32 v[26:27], v[26:27], v[94:95]\n"
    "v_pk_add_f32 v[30:31], v[30:31], v[26:27]\n"
    "buffer_load_dwordx4 v0, s[24:27], s47 offen offset:1024 sc1 lds\n"
    "v_add_f32 v30, v30, v31\n"
    "v_rcp_f32 v30, v30\n"
    "s_nop 0\n"
    "v_pk_mul_f32 v[80:81], v[80:81], v[30:31] op_sel_hi:[1,0]\n"
    "v_pk_mul_f32 v[82:83], v[82:83], v[30:31] op_sel_hi:[1,0]\n"
    "v_pk_mul_f32 v[84:85], v[84:85], v[30:31] op_sel_hi:[1,0]\n"
    "v_pk_mul_f32 v[86:87], v[86:87], v[30:31] op_sel_hi:[1,0]\n"
    "v_cvt_pk_bf16_f32 v120, v80, v81\n"
    "v_cvt_pk_bf16_f32 v121, v82, v83\n"
    "v_cvt_pk_bf16_f32 v122, v84, v85\n"
    "buffer_load_dwordx4 v0, s[24:27], s47 offen offset:2048 sc1 lds\n"
    "v_cvt_pk_bf16_f32 v123, v86, v87\n"
    "v_pk_mul_f32 v[88:89], v[88:89], v[30:31] op_sel_hi:[1,0]\n"
    "v_pk_mul_f32 v[90:91], v[90:91], v[30:31] op_sel_hi:[1,0]\n"
    "v_pk_mul_f32 v[92:93], v[92:93], v[30:31] op_sel_hi:[1,0]\n"
    "v_pk_mul_f32 v[94:95], v[94:95], v[30:31] op_sel_hi:[1,0]\n"
    "v_cvt_pk_bf16_f32 v124, v88, v89\n"
    "v_cvt_pk_bf16_f32 v125, v90, v91\n"
    "v_cvt_pk_bf16_f32 v126, v92, v93\n"
    "buffer_load_dwordx4 v0, s[24:27], s47 offen offset:3072 sc1 lds\n"
    "v_cvt_pk_bf16_f32 v127, v94, v95\n"
    "Lkv267_%=:\n"
    "s_waitcnt vmcnt(17)\n"
    "v_mov_b32 v44, v192\n"
    "v_mov_b32 v45, v193\n"
    "v_mov_b32 v46, v194\n"
    "v_mov_b32 v47, v195\n"
    "s_waitcnt vmcnt(16)\n"
    "v_mfma_f32_16x16x32_bf16 v[24:27], v[160:163], v[128:131], 0\n"
    "v_permlane32_swap_b32 v44, v45\n"
    "s_waitcnt vmcnt(15)\n"
    "v_mfma_f32_16x16x32_bf16 v[24:27], v[164:167], v[132:135], v[24:27]\n"
    "v_permlane32_swap_b32 v46, v47\n"
    "s_waitcnt vmcnt(14)\n"
    "v_mfma_f32_16x16x32_bf16 v[24:27], v[160:163], v[136:139], v[24:27]\n"
    "v_add_f32 v44, v44, v45\n"
    "s_waitcnt vmcnt(13)\n"
    "v_mfma_f32_16x16x32_bf16 v[24:27], v[164:167], v[140:143], v[24:27]\n"
    "v_add_f32 v46, v46, v47\n"
    "s_waitcnt vmcnt(12)\n"
    "v_mfma_f32_16x16x32_bf16 v[24:27], v[160:163], v[144:147], v[24:27]\n"
    "v_permlane16_swap_b32 v44, v46\n"
    "s_waitcnt vmcnt(11)\n"
    "v_mfma_f32_16x16x32_bf16 v[24:27], v[164:167], v[148:151], v[24:27]\n"
    "v_add_f32 v44, v44, v46\n"
    "s_waitcnt vmcnt(10)\n"
    "v_mfma_f32_16x16x32_bf16 v[24:27], v[160:163], v[152:155], v[24:27]\n"
    "v_add_f32_dpp v45, v44, v44 row_ror:4 row_mask:0xf bank_mask:0xf\n"
    "s_waitcnt vmcnt(9)\n"
    "v_mfma_f32_16x16x32_bf16 v[24:27], v[164:167], v[156:159], v[24:27]\n"
    "v_add_f32_dpp v44, v45, v45 row_ror:8 row_mask:0xf bank_mask:0xf\n"
    "v_cvt_pk_bf16_f32 v45, v44, v44\n"
    "ds_write_b16 v10, v45\n"
    "s_nop 4\n"
    "v_cvt_pk_bf16_f32 v28, v24, v25\n"
    "v_cvt_pk_bf16_f32 v29, v26, v27\n"
    "v_cmp_u_f32 vcc, v24, v44\n"
    "ds_write_b64 v9, v[28:29]\n"
    "s_waitcnt lgkmcnt(0)\n"
    "s_cbranch_vccnz Lre268_%=\n"
    "Lok269_%=:\n"
    "s_barrier\n"
    "v_lshlrev_b32 v60, 3, v0\n"
    "v_mov_b32 v61, 1\n"
    "s_mov_b64 exec, s[18:19]\n"
    "buffer_atomic_add v61, v60, s[24:27], s88 offen offset:24 sc1\n"
    "s_mov_b64 exec, -1\n"
    "ds_read_b128 v[128:131], v12\n"
    "ds_read_b128 v[132:135], v11\n"
    "ds_read_b128 v[136:139], v12 offset:16\n"
    "ds_read_b128 v[140:143], v11 offset:16\n"
    "ds_read_b128 v[144:147], v12 offset:32\n"
    "ds_read_b128 v[148:151], v11 offset:32\n"
    "ds_read_b128 v[152:155], v12 offset:48\n"
    "ds_read_b128 v[156:159], v11 offset:48\n"
    "s_waitcnt lgkmcnt(7)\n"
    "v_mfma_f32_32x32x16_bf16 v[80:95], v[128:131], v[112:115], 0\n"
    "s_waitcnt lgkmcnt(6)\n"
    "v_mfma_f32_32x32x16_bf16 v[64:79], v[132:135], v[112:115], 0\n"
    "s_add_u32 m0, s78, 0x9000\n"
    "s_add_u32 s47, s69, 0x61800\n"
    "buffer_load_dwordx4 v0, s[24:27], s47 offen sc1 lds\n"
    "s_waitcnt lgkmcnt(5)\n"
    "v_mfma_f32_32x32x16_bf16 v[80:95], v[136:139], v[116:119], v[80:95]\n"
    "s_waitcnt lgkmcnt(4)\n"
    "v_mfma_f32_32x32x16_bf16 v[64:79], v[140:143], v[116:119], v[64:79]\n"
    "buffer_load_dwordx4 v0, s[24:27], s47 offen offset:1024 sc1 lds\n"
    "s_waitcnt lgkmcnt(3)\n"
    "v_mfma_f32_32x32x16_bf16 v[80:95], v[144:147], v[120:123], v[80:95]\n"
    "s_waitcnt lgkmcnt(2)\n"
    "v_mfma_f32_32x32x16_bf16 v[64:79], v[148:151], v[120:123], v[64:79]\n"
    "buffer_load_dwordx4 v0, s[24:27], s47 offen offset:2048 sc1 lds\n"
    "s_waitcnt lgkmcnt(1)\n"
    "v_mfma_f32_32x32x16_bf16 v[80:95], v[152:155], v[124:127], v[80:95]\n"
    "s_waitcnt lgkmcnt(0)\n"
    "v_mfma_f32_32x32x16_bf16 v[64:79], v[156:159], v[124:127], v[64:79]\n"
    "v_mov_b32 v60, 0\n"
    "s_mov_b64 exec, s[16:17]\n"
    "buffer_atomic_add v60, off, s[24:27], s92 offset:24 sc0 sc1\n"
    "s_mov_b64 exec, -1\n"
    "s_nop 5\n"
    "v_add_f32 v32, 0x358637bd, v80\n"
    "v_rcp_f32 v32, v32\n"
    "s_nop 0\n"
    "v_pk_mul_f32 v[64:65], v[64:65], v[32:33] op_sel_hi:[1,0]\n"
    "v_pk_mul_f32 v[66:67], v[66:67], v[32:33] op_sel_hi:[1,0]\n"
    "v_pk_mul_f32 v[68:69], v[68:69], v[32:33] op_sel_hi:[1,0]\n"
    "v_pk_mul_f32 v[70:71], v[70:71], v[32:33] op_sel_hi:[1,0]\n"
    "v_cvt_pk_bf16_f32 v24, v64, v65\n"
    "v_cvt_pk_bf16_f32 v25, v66, v67\n"
    "v_cvt_pk_bf16_f32 v26, v68, v69\n"
    "v_cvt_pk_bf16_f32 v27, v70, v71\n"
    "buffer_store_dwordx4 v[24:27], v0, s[24:27], s57 offen sc1\n"
    "v_pk_mul_f32 v[72:73], v[72:73], v[32:33] op_sel_hi:[1,0]\n"
    "v_pk_mul_f32 v[74:75], v[74:75], v[32:33] op_sel_hi:[1,0]\n"
    "v_pk_mul_f32 v[76:77], v[76:77], v[32:33] op_sel_hi:[1,0]\n"
    "v_pk_mul_f32 v[78:79], v[78:79], v[32:33] op_sel_hi:[1,0]\n"
    "v_cvt_pk_bf16_f32 v28, v72, v73\n"
    "v_cvt_pk_bf16_f32 v29, v74, v75\n"
    "v_cvt_pk_bf16_f32 v30, v76, v77\n"
    "v_cvt_pk_bf16_f32 v31, v78, v79\n"
    "s_add_u32 s42, s57, 0x1000\n"
    "buffer_store_dwordx4 v[28:31], v0, s[24:27], s42 offen sc1\n"
    "v_mov_b32 v61, 0\n"
    "s_mov_b64 exec, s[16:17]\n"
    "buffer_atomic_add v61, off, s[24:27], s92 offset:24 sc0 sc1\n"
    "s_mov_b64 exec, -1\n"
    "v_accvgpr_read_b32 v65, a248\n"
    "v_accvgpr_read_b32 v67, a249\n"
    "v_accvgpr_read_b32 v69, a250\n"
    "v_accvgpr_read_b32 v71, a251\n"
    "v_accvgpr_read_b32 v73, a252\n"
    "v_accvgpr_read_b32 v75, a253\n"
    "v_accvgpr_read_b32 v77, a254\n"
    "v_accvgpr_read_b32 v79, a255\n"
    "v_lshlrev_b32 v64, 16, v65\n"
    "v_and_b32 v65, 0xffff0000, v65\n"
    "v_lshlrev_b32 v66, 16, v67\n"
    "v_and_b32 v67, 0xffff0000, v67\n"
    "v_lshlrev_b32 v68, 16, v69\n"
    "v_and_b32 v69, 0xffff0000, v69\n"
    "v_lshlrev_b32 v70, 16, v71\n"
    "v_and_b32 v71, 0xffff0000, v71\n"
    "v_lshlrev_b32 v72, 16, v73\n"
    "v_and_b32 v73, 0xffff0000, v73\n"
    "v_lshlrev_b32 v74, 16, v75\n"
    "v_and_b32 v75, 0xffff0000, v75\n"
    "v_lshlrev_b32 v76, 16, v77\n"
    "v_and_b32 v77, 0xffff0000, v77\n"
    "v_lshlrev_b32 v78, 16, v79\n"
    "v_and_b32 v79, 0xffff0000, v79\n"
    "s_cmp_lg_u32 s37, 0\n"
    "s_cbranch_scc1 Lhs273_%=\n"
    "s_mov_b64 exec, 1\n"
    "v_mov_b32 v62, 0\n"
    "buffer_atomic_add v62, off, s[24:27], s92 offset:24 sc0 sc1\n"
    "s_waitcnt vmcnt(4)\n"
    "v_cmp_eq_u32 vcc, 16, v60\n"
    "s_cbranch_vccnz Lhd272_%=\n"
    "v_mov_b32 v60, 0\n"
    "buffer_atomic_add v60, off, s[24:27], s92 offset:24 sc0 sc1\n"
    "s_waitcnt vmcnt(2)\n"
    "v_cmp_eq_u32 vcc, 16, v61\n"
    "s_cbranch_vccnz Lhd272_%=\n"
    "v_mov_b32 v61, 0\n"
    "buffer_atomic_add v61, off, s[24:27], s92 offset:24 sc0 sc1\n"
    "Lhp271_%=:\n"
    "s_waitcnt vmcnt(2)\n"
    "v_cmp_eq_u32 vcc, 16, v62\n"
    "s_cbranch_vccnz Lhd272_%=\n"
    "v_mov_b32 v62, 0\n"
    "buffer_atomic_add v62, off, s[24:27], s92 offset:24 sc0 sc1\n"
    "s_waitcnt vmcnt(2)\n"
    "v_cmp_eq_u32 vcc, 16, v60\n"
    "s_cbranch_vccnz Lhd272_%=\n"
    "v_mov_b32 v60, 0\n"
    "buffer_atomic_add v60, off, s[24:27], s92 offset:24 sc0 sc1\n"
    "s_waitcnt vmcnt(2)\n"
    "v_cmp_eq_u32 vcc, 16, v61\n"
    "s_cbranch_vccnz Lhd272_%=\n"
    "v_mov_b32 v61, 0\n"
    "buffer_atomic_add v61, off, s[24:27], s92 offset:24 sc0 sc1\n"
    "s_sub_u32 s50, s50, 1\n"
    "s_cbranch_scc0 Lhp271_%=\n"
    "s_branch Lfail_%=\n"
    "Lhd272_%=:\n"
    "s_mov_b64 exec, -1\n"
    "Lhs273_%=:\n"
    "s_barrier\n"
    "buffer_load_dwordx4 v[128:131], v0, s[24:27], s59 offen sc1\n"
    "s_add_u32 s42, s59, 0x1000\n"
    "buffer_load_dwordx4 v[132:135], v0, s[24:27], s42 offen sc1\n"
    "s_add_u32 s42, s59, 0x2000\n"
    "buffer_load_dwordx4 v[136:139], v0, s[24:27], s42 offen sc1\n"
    "s_add_u32 s42, s59, 0x3000\n"
    "buffer_load_dwordx4 v[140:143], v0, s[24:27], s42 offen sc1\n"
    "s_add_u32 s42, s59, 0x4000\n"
    "buffer_load_dwordx4 v[144:147], v0, s[24:27], s42 offen sc1\n"
    "s_add_u32 s42, s59, 0x5000\n"
    "buffer_load_dwordx4 v[148:151], v0, s[24:27], s42 offen sc1\n"
    "s_add_u32 s42, s59, 0x6000\n"
    "buffer_load_dwordx4 v[152:155], v0, s[24:27], s42 offen sc1\n"
    "s_add_u32 s42, s59, 0x7000\n"
    "buffer_load_dwordx4 v[156:159], v0, s[24:27], s42 offen sc1\n"
    "buffer_load_dwordx4 v[160:163], v0, s[24:27], s58 offen sc1\n"
    "s_add_u32 s42, s58, 0x1000\n"
    "buffer_load_dwordx4 v[164:167], v0, s[24:27], s42 offen sc1\n"
    "s_add_u32 s42, s58, 0x2000\n"
    "buffer_load_dwordx4 v[168:171], v0, s[24:27], s42 offen sc1\n"
    "s_add_u32 s42, s58, 0x3000\n"
    "buffer_load_dwordx4 v[172:175], v0, s[24:27], s42 offen sc1\n"
    "s_waitcnt vmcnt(10)\n"
    "v_max3_u32 v15, v135, v129, v130\n"
    "v_max3_u32 v15, v15, v128, v131\n"
    "v_max3_u32 v15, v15, v132, v133\n"
    "v_max_u32 v15, v15, v134\n"
    "v_cmp_eq_u32 vcc, -1, v15\n"
    "s_nop 0\n"
    "s_cbranch_vccnz Lre274_%=\n"
    "Lok275_%=:\n"
    "v_mfma_f32_32x32x16_bf16 v[112:127], a[208:211], v[128:131], 0\n"
    "v_mfma_f32_32x32x16_bf16 v[96:111], a[176:179], v[128:131], 0\n"
    "s_add_u32 s42, s58, 0x4000\n"
    "buffer_load_dwordx4 v[176:179], v0, s[24:27], s42 offen sc1\n"
    "s_waitcnt vmcnt(10)\n"
    "v_max3_u32 v15, v136, v137, v138\n"
    "v_mfma_f32_32x32x16_bf16 v[112:127], a[212:215], v[132:135], v[112:127]\n"
    "s_waitcnt vmcnt(9)\n"
    "v_max3_u32 v15, v15, v139, v140\n"
    "v_max3_u32 v15, v15, v141, v142\n"
    "v_max_u32 v15, v15, v143\n"
    "v_cmp_eq_u32 vcc, -1, v15\n"
    "v_mfma_f32_32x32x16_bf16 v[96:111], a[180:183], v[132:135], v[96:111]\n"
    "s_add_u32 s42, s58, 0x5000\n"
    "buffer_load_dwordx4 v[180:183], v0, s[24:27], s42 offen sc1\n"
    "s_cbranch_vccnz Lre276_%=\n"
    "Lok277_%=:\n"
    "v_mfma_f32_32x32x16_bf16 v[112:127], a[216:219], v[136:139], v[112:127]\n"
    "v_mfma_f32_32x32x16_bf16 v[96:111], a[184:187], v[136:139], v[96:111]\n"
    "s_add_u32 s42, s58, 0x6000\n"
    "buffer_load_dwordx4 v[184:187], v0, s[24:27], s42 offen sc1\n"
    "s_waitcnt vmcnt(10)\n"
    "v_max3_u32 v15, v144, v145, v146\n"
    "v_mfma_f32_32x32x16_bf16 v[112:127], a[220:223], v[140:143], v[112:127]\n"
    "s_waitcnt vmcnt(9)\n"
    "v_max3_u32 v15, v15, v147, v148\n"
    "v_max3_u32 v15, v15, v149, v150\n"
    "v_max_u32 v15, v15, v151\n"
    "v_cmp_eq_u32 vcc, -1, v15\n"
    "v_mfma_f32_32x32x16_bf16 v[96:111], a[188:191], v[140:143], v[96:111]\n"
    "s_add_u32 s42, s58, 0x7000\n"
    "buffer_load_dwordx4 v[188:191], v0, s[24:27], s42 offen sc1\n"
    "s_cbranch_vccnz Lre278_%=\n"
    "Lok279_%=:\n"
    "v_mfma_f32_32x32x16_bf16 v[112:127], a[224:227], v[144:147], v[112:127]\n"
    "v_mfma_f32_32x32x16_bf16 v[96:111], a[192:195], v[144:147], v[96:111]\n"
    "buffer_load_dwordx4 v0, s[24:27], s47 offen offset:3072 sc1 lds\n"
    "s_waitcnt vmcnt(10)\n"
    "v_max3_u32 v15, v152, v153, v154\n"
    "v_mfma_f32_32x32x16_bf16 v[112:127], a[228:231], v[148:151], v[112:127]\n"
    "s_waitcnt vmcnt(9)\n"
    "v_max3_u32 v15, v15, v155, v156\n"
    "v_max3_u32 v15, v15, v157, v158\n"
    "v_max_u32 v15, v15, v159\n"
    "v_cmp_eq_u32 vcc, -1, v15\n"
    "v_mfma_f32_32x32x16_bf16 v[96:111], a[196:199], v[148:151], v[96:111]\n"
    "s_add_u32 m0, s86, 0x200\n"
    "s_nop 0\n"
    "buffer_load_dword v3, s[24:27], s47 offen sc1 lds\n"
    "s_cbranch_vccnz Lre280_%=\n"
    "Lok281_%=:\n"
    "v_mfma_f32_32x32x16_bf16 v[112:127], a[232:235], v[152:155], v[112:127]\n"
    "v_mfma_f32_32x32x16_bf16 v[96:111], a[200:203], v[152:155], v[96:111]\n"
    "s_waitcnt vmcnt(9)\n"
    "v_max3_u32 v15, v160, v161, v162\n"
    "v_mfma_f32_32x32x16_bf16 v[112:127], a[236:239], v[156:159], v[112:127]\n"
    "s_waitcnt vmcnt(8)\n"
    "v_max3_u32 v15, v15, v163, v164\n"
    "v_max3_u32 v15, v15, v165, v166\n"
    "v_max_u32 v15, v15, v167\n"
    "v_cmp_eq_u32 vcc, -1, v15\n"
    "v_mfma_f32_32x32x16_bf16 v[96:111], a[204:207], v[156:159], v[96:111]\n"
    "s_cbranch_vccnz Lre282_%=\n"
    "Lok283_%=:\n"
    "v_mfma_f32_32x32x16_bf16 v[80:95], a[208:211], v[160:163], 0\n"
    "v_add_u32 v39, s81, v0\n"
    "v_add_u32 v38, s80, v0\n"
    "v_add_u32 v37, s79, v0\n"
    "v_mfma_f32_32x32x16_bf16 v[64:79], a[176:179], v[160:163], v[64:79]\n"
    "s_waitcnt vmcnt(7)\n"
    "v_max3_u32 v15, v168, v169, v170\n"
    "ds_write_b128 v39, v[112:115]\n"
    "v_mfma_f32_32x32x16_bf16 v[80:95], a[212:215], v[164:167], v[80:95]\n"
    "s_waitcnt vmcnt(6)\n"
    "v_max3_u32 v15, v15, v171, v172\n"
    "v_max3_u32 v15, v15, v173, v174\n"
    "v_max_u32 v15, v15, v175\n"
    "v_cmp_eq_u32 vcc, -1, v15\n"
    "ds_write_b128 v39, v[116:119] offset:1024\n"
    "v_mfma_f32_32x32x16_bf16 v[64:79], a[180:183], v[164:167], v[64:79]\n"
    "s_cbranch_vccnz Lre284_%=\n"
    "Lok285_%=:\n"
    "v_mfma_f32_32x32x16_bf16 v[80:95], a[216:219], v[168:171], v[80:95]\n"
    "ds_write_b128 v39, v[120:123] offset:2048\n"
    "v_mfma_f32_32x32x16_bf16 v[64:79], a[184:187], v[168:171], v[64:79]\n"
    "s_waitcnt vmcnt(5)\n"
    "v_max3_u32 v15, v176, v177, v178\n"
    "v_mfma_f32_32x32x16_bf16 v[80:95], a[220:223], v[172:175], v[80:95]\n"
    "s_waitcnt vmcnt(4)\n"
    "v_max3_u32 v15, v15, v179, v180\n"
    "v_max3_u32 v15, v15, v181, v182\n"
    "v_max_u32 v15, v15, v183\n"
    "v_cmp_eq_u32 vcc, -1, v15\n"
    "ds_write_b128 v39, v[124:127] offset:3072\n"
    "v_mfma_f32_32x32x16_bf16 v[64:79], a[188:191], v[172:175], v[64:79]\n"
    "s_cbranch_vccnz Lre286_%=\n"
    "Lok287_%=:\n"
    "v_mfma_f32_32x32x16_bf16 v[80:95], a[224:227], v[176:179], v[80:95]\n"
    "ds_write_b128 v38, v[96:99]\n"
    "v_mfma_f32_32x32x16_bf16 v[64:79], a[192:195], v[176:179], v[64:79]\n"
    "s_waitcnt vmcnt(3)\n"
    "v_max3_u32 v15, v184, v185, v186\n"
    "ds_write_b128 v38, v[100:103] offset:1024\n"
    "v_mfma_f32_32x32x16_bf16 v[80:95], a[228:231], v[180:183], v[80:95]\n"
    "s_waitcnt vmcnt(2)\n"
    "v_max3_u32 v15, v15, v187, v188\n"
    "v_max3_u32 v15, v15, v189, v190\n"
    "v_max_u32 v15, v15, v191\n"
    "v_cmp_eq_u32 vcc, -1, v15\n"
    "v_mfma_f32_32x32x16_bf16 v[64:79], a[196:199], v[180:183], v[64:79]\n"
    "s_cbranch_vccnz Lre288_%=\n"
    "Lok289_%=:\n"
    "ds_write_b128 v38, v[104:107] offset:2048\n"
    "v_mfma_f32_32x32x16_bf16 v[80:95], a[232:235], v[184:187], v[80:95]\n"
    "v_mfma_f32_32x32x16_bf16 v[64:79], a[200:203], v[184:187], v[64:79]\n"
    "ds_write_b128 v38, v[108:111] offset:3072\n"
    "v_mfma_f32_32x32x16_bf16 v[80:95], a[236:239], v[188:191], v[80:95]\n"
    "v_mfma_f32_32x32x16_bf16 v[64:79], a[204:207], v[188:191], v[64:79]\n"
    "s_nop 10\n"
    "ds_write_b128 v37, v[80:83]\n"
    "ds_write_b128 v37, v[84:87] offset:1024\n"
    "ds_write_b128 v37, v[88:91] offset:2048\n"
    "ds_write_b128 v37, v[92:95] offset:3072\n"
    "v_add_u32 v32, s82, v0\n"
    "v_add_u32 v33, s83, v0\n"
    "s_waitcnt lgkmcnt(0)\n"
    "s_barrier\n"
    "v_lshlrev_b32 v60, 3, v0\n"
    "v_mov_b32 v61, 1\n"
    "s_mov_b64 exec, s[18:19]\n"
    "buffer_atomic_add v61, v60, s[24:27], s90 offen offset:28 sc1\n"
    "s_mov_b64 exec, -1\n"
    "ds_read_b128 v[80:83], v32\n"
    "ds_read_b128 v[96:99], v32 offset:4096\n"
    "ds_read_b128 v[112:115], v33\n"
    "ds_read_b128 v[84:87], v32 offset:1024\n"
    "ds_read_b128 v[100:103], v32 offset:5120\n"
    "ds_read_b128 v[116:119], v33 offset:1024\n"
    "ds_read_b128 v[88:91], v32 offset:2048\n"
    "ds_read_b128 v[104:107], v32 offset:6144\n"
    "ds_read_b128 v[120:123], v33 offset:2048\n"
    "ds_read_b128 v[92:95], v32 offset:3072\n"
    "ds_read_b128 v[108:111], v32 offset:7168\n"
    "ds_read_b128 v[124:127], v33 offset:3072\n"
    "s_waitcnt lgkmcnt(9)\n"
    "v_pk_add_f32 v[64:65], v[64:65], v[80:81]\n"
    "v_pk_add_f32 v[66:67], v[66:67], v[82:83]\n"
    "v_pk_add_f32 v[64:65], v[64:65], v[96:97]\n"
    "v_pk_add_f32 v[66:67], v[66:67], v[98:99]\n"
    "v_pk_add_f32 v[64:65], v[64:65], v[112:113]\n"
    "v_pk_add_f32 v[66:67], v[66:67], v[114:115]\n"
    "v_cvt_pk_bf16_f32 v24, v64, v66\n"
    "v_cvt_pk_bf16_f32 v28, v65, v67\n"
    "v_mov_b32 v60, 0\n"
    "s_mov_b64 exec, s[16:17]\n"
    "buffer_atomic_add v60, off, s[24:27], s91 offset:28 sc0 sc1\n"
    "s_mov_b64 exec, -1\n"
    "s_waitcnt lgkmcnt(6)\n"
    "v_pk_add_f32 v[68:69], v[68:69], v[84:85]\n"
    "v_pk_add_f32 v[70:71], v[70:71], v[86:87]\n"
    "v_pk_add_f32 v[68:69], v[68:69], v[100:101]\n"
    "v_pk_add_f32 v[70:71], v[70:71], v[102:103]\n"
    "v_pk_add_f32 v[68:69], v[68:69], v[116:117]\n"
    "v_pk_add_f32 v[70:71], v[70:71], v[118:119]\n"
    "v_cvt_pk_bf16_f32 v25, v68, v70\n"
    "v_cvt_pk_bf16_f32 v29, v69, v71\n"
    "s_waitcnt lgkmcnt(3)\n"
    "v_pk_add_f32 v[72:73], v[72:73], v[88:89]\n"
    "v_pk_add_f32 v[74:75], v[74:75], v[90:91]\n"
    "v_pk_add_f32 v[72:73], v[72:73], v[104:105]\n"
    "v_pk_add_f32 v[74:75], v[74:75], v[106:107]\n"
    "v_pk_add_f32 v[72:73], v[72:73], v[120:121]\n"
    "v_pk_add_f32 v[74:75], v[74:75], v[122:123]\n"
    "v_cvt_pk_bf16_f32 v26, v72, v74\n"
    "v_cvt_pk_bf16_f32 v30, v73, v75\n"
    "s_waitcnt lgkmcnt(0)\n"
    "v_pk_add_f32 v[76:77], v[76:77], v[92:93]\n"
    "v_pk_add_f32 v[78:79], v[78:79], v[94:95]\n"
    "v_pk_add_f32 v[76:77], v[76:77], v[108:109]\n"
    "v_pk_add_f32 v[78:79], v[78:79], v[110:111]\n"
    "v_pk_add_f32 v[76:77], v[76:77], v[124:125]\n"
    "v_pk_add_f32 v[78:79], v[78:79], v[126:127]\n"
    "v_cvt_pk_bf16_f32 v27, v76, v78\n"
    "v_cvt_pk_bf16_f32 v31, v77, v79\n"
    "buffer_store_dwordx4 v[24:27], v0, s[24:27], s52 offen sc1\n"
    "buffer_store_dwordx4 v[28:31], v0, s[24:27], s52 offen offset:1024 sc1\n"
    "v_mov_b32 v61, 0\n"
    "s_mov_b64 exec, s[16:17]\n"
    "buffer_atomic_add v61, off, s[24:27], s91 offset:28 sc0 sc1\n"
    "s_mov_b64 exec, -1\n"
    "v_mov_b32 v32, 0\n"
    "v_mov_b32 v33, 0\n"
    "v_mov_b32 v34, 0x3f803f80\n"
    "s_lshl_b32 s44, s37, 8\n"
    "s_add_u32 s44, s44, 0x27000\n"
    "v_add_u32 v35, s44, v2\n"
    "v_dot2c_f32_bf16 v32, v24, v34\n"
    "v_dot2c_f32_bf16 v33, v24, v24\n"
    "v_dot2c_f32_bf16 v32, v25, v34\n"
    "v_dot2c_f32_bf16 v33, v25, v25\n"
    "v_dot2c_f32_bf16 v32, v26, v34\n"
    "v_dot2c_f32_bf16 v33, v26, v26\n"
    "v_dot2c_f32_bf16 v32, v27, v34\n"
    "v_dot2c_f32_bf16 v33, v27, v27\n"
    "v_dot2c_f32_bf16 v32, v28, v34\n"
    "v_dot2c_f32_bf16 v33, v28, v28\n"
    "v_dot2c_f32_bf16 v32, v29, v34\n"
    "v_dot2c_f32_bf16 v33, v29, v29\n"
    "v_dot2c_f32_bf16 v32, v30, v34\n"
    "v_dot2c_f32_bf16 v33, v30, v30\n"
    "v_dot2c_f32_bf16 v32, v31, v34\n"
    "v_dot2c_f32_bf16 v33, v31, v31\n"
    "v_accvgpr_write_b32 a248, v24\n"
    "v_accvgpr_write_b32 a249, v25\n"
    "v_accvgpr_write_b32 a250, v26\n"
    "v_accvgpr_write_b32 a251, v27\n"
    "v_permlane32_swap_b32 v32, v33\n"
    "v_add_f32 v32, v32, v33\n"
    "ds_write_b32 v35, v32\n"
    "v_xor_b32 v35, 0x100, v35\n"
    "s_waitcnt lgkmcnt(0)\n"
    "s_barrier\n"
    "ds_read_b32 v33, v35\n"
    "v_accvgpr_write_b32 a252, v28\n"
    "v_accvgpr_write_b32 a253, v29\n"
    "v_accvgpr_write_b32 a254, v30\n"
    "v_accvgpr_write_b32 a255, v31\n"
    "s_waitcnt lgkmcnt(0)\n"
    "v_add_f32 v32, v32, v33\n"
    "buffer_store_dword v32, v2, s[24:27], s53 offen sc1\n"
    "s_add_u32 s52, s52, 0x240000\n"
    "s_add_u32 s53, s53, 0x240000\n"
    "v_and_b32 v15, 0x180, v0\n"
    "s_mov_b32 m0, 162528\n"
    "s_nop 0\n"
    "buffer_load_dword v15, s[28:31], s87 offen lds\n"
    "s_cmp_lg_u32 s37, 0\n"
    "s_cbranch_scc1 Lhs292_%=\n"
    "s_mov_b64 exec, 1\n"
    "v_mov_b32 v62, 0\n"
    "buffer_atomic_add v62, off, s[24:27], s91 offset:28 sc0 sc1\n"
    "s_waitcnt vmcnt(6)\n"
    "v_cmp_eq_u32 vcc, 16, v60\n"
    "s_cbranch_vccnz Lhd291_%=\n"
    "v_mov_b32 v60, 0\n"
    "buffer_atomic_add v60, off, s[24:27], s91 offset:28 sc0 sc1\n"
    "s_waitcnt vmcnt(4)\n"
    "v_cmp_eq_u32 vcc, 16, v61\n"
    "s_cbranch_vccnz Lhd291_%=\n"
    "v_mov_b32 v61, 0\n"
    "buffer_atomic_add v61, off, s[24:27], s91 offset:28 sc0 sc1\n"
    "Lhp290_%=:\n"
    "s_waitcnt vmcnt(2)\n"
    "v_cmp_eq_u32 vcc, 16, v62\n"
    "s_cbranch_vccnz Lhd291_%=\n"
    "v_mov_b32 v62, 0\n"
    "buffer_atomic_add v62, off, s[24:27], s91 offset:28 sc0 sc1\n"
    "s_waitcnt vmcnt(2)\n"
    "v_cmp_eq_u32 vcc, 16, v60\n"
    "s_cbranch_vccnz Lhd291_%=\n"
    "v_mov_b32 v60, 0\n"
    "buffer_atomic_add v60, off, s[24:27], s91 offset:28 sc0 sc1\n"
    "s_waitcnt vmcnt(2)\n"
    "v_cmp_eq_u32 vcc, 16, v61\n"
    "s_cbranch_vccnz Lhd291_%=\n"
    "v_mov_b32 v61, 0\n"
    "buffer_atomic_add v61, off, s[24:27], s91 offset:28 sc0 sc1\n"
    "s_sub_u32 s50, s50, 1\n"
    "s_cbranch_scc0 Lhp290_%=\n"
    "s_branch Lfail_%=\n"
    "Lhd291_%=:\n"
    "s_mov_b64 exec, -1\n"
    "Lhs292_%=:\n"
    "s_barrier\n"
    "buffer_load_dwordx4 v[128:131], v0, s[24:27], s51 offen sc1\n"
    "buffer_load_dwordx4 v[132:135], v0, s[24:27], s51 offen offset:1024 sc1\n"
    "buffer_load_dwordx4 v[136:139], v0, s[24:27], s51 offen offset:2048 sc1\n"
    "buffer_load_dwordx4 v[140:143], v0, s[24:27], s51 offen offset:3072 sc1\n"
    "s_add_u32 s42, s51, 0x1000\n"
    "buffer_load_dwordx4 v[144:147], v0, s[24:27], s42 offen sc1\n"
    "buffer_load_dwordx4 v[148:151], v0, s[24:27], s42 offen offset:1024 sc1\n"
    "buffer_load_dwordx4 v[152:155], v0, s[24:27], s42 offen offset:2048 sc1\n"
    "buffer_load_dwordx4 v[156:159], v0, s[24:27], s42 offen offset:3072 sc1\n"
    "s_add_u32 s42, s51, 0x2000\n"
    "buffer_load_dwordx4 v[160:163], v0, s[24:27], s42 offen sc1\n"
    "buffer_load_dwordx4 v[164:167], v0, s[24:27], s42 offen offset:1024 sc1\n"
    "buffer_load_dwordx4 v[168:171], v0, s[24:27], s42 offen offset:2048 sc1\n"
    "buffer_load_dwordx4 v[172:175], v0, s[24:27], s42 offen offset:3072 sc1\n"
    "s_waitcnt vmcnt(16)\n"
    "s_barrier\n"
    "ds_read_b128 v[40:43], v0 offset:32768\n"
    "ds_read_b128 v[44:47], v1 offset:32768\n"
    "ds_read_b128 v[48:51], v0 offset:33792\n"
    "ds_read_b128 v[52:55], v1 offset:33792\n"
    "ds_read_b128 v[56:59], v0 offset:34816\n"
    "s_waitcnt vmcnt(10)\n"
    "v_max3_u32 v15, v135, v129, v130\n"
    "v_max3_u32 v15, v15, v128, v131\n"
    "v_max3_u32 v15, v15, v132, v133\n"
    "v_max_u32 v15, v15, v134\n"
    "v_cmp_eq_u32 vcc, -1, v15\n"
    "s_waitcnt lgkmcnt(3)\n"
    "s_cbranch_vccnz Lre293_%=\n"
    "Lok294_%=:\n"
    "v_mfma_f32_32x32x16_bf16 v[64:79], v[40:43], v[128:131], 0\n"
    "s_add_u32 s42, s51, 0x3000\n"
    "buffer_load_dwordx4 v[176:179], v0, s[24:27], s42 offen sc1\n"
    "ds_read_b128 v[60:63], v1 offset:34816\n"
    "v_mfma_f32_32x32x16_bf16 v[80:95], v[44:47], v[128:131], 0\n"
    "ds_read_b128 v[40:43], v0\n"
    "s_waitcnt vmcnt(10)\n"
    "v_max3_u32 v15, v136, v137, v138\n"
    "s_waitcnt lgkmcnt(3)\n"
    "v_mfma_f32_32x32x16_bf16 v[64:79], v[48:51], v[132:135], v[64:79]\n"
    "ds_read_b128 v[44:47], v0 offset:35840\n"
    "s_waitcnt vmcnt(9)\n"
    "v_max3_u32 v15, v15, v139, v140\n"
    "v_max3_u32 v15, v15, v141, v142\n"
    "v_max_u32 v15, v15, v143\n"
    "v_cmp_eq_u32 vcc, -1, v15\n"
    "v_mfma_f32_32x32x16_bf16 v[80:95], v[52:55], v[132:135], v[80:95]\n"
    "buffer_load_dwordx4 v[180:183], v0, s[24:27], s42 offen offset:1024 sc1\n"
    "ds_read_b128 v[48:51], v1 offset:35840\n"
    "s_cbranch_vccnz Lre295_%=\n"
    "Lok296_%=:\n"
    "s_waitcnt lgkmcnt(3)\n"
    "v_mfma_f32_32x32x16_bf16 v[64:79], v[56:59], v[136:139], v[64:79]\n"
    "buffer_load_dwordx4 v[184:187], v0, s[24:27], s42 offen offset:2048 sc1\n"
    "ds_read_b128 v[52:55], v0 offset:36864\n"
    "v_mov_b32 v36, -1\n"
    "v_mov_b32 v37, -1\n"
    "v_mov_b32 v38, -1\n"
    "v_mfma_f32_32x32x16_bf16 v[80:95], v[60:63], v[136:139], v[80:95]\n"
    "ds_read_b128 v[56:59], v1 offset:36864\n"
    "v_mov_b32 v39, -1\n"
    "s_waitcnt lgkmcnt(3)\n"
    "v_mfma_f32_32x32x16_bf16 v[96:111], v[40:43], v[128:131], 0\n"
    "ds_read_b128 v[60:63], v0 offset:37888\n"
    "s_waitcnt vmcnt(10)\n"
    "v_max3_u32 v15, v144, v145, v146\n"
    "v_mfma_f32_32x32x16_bf16 v[64:79], v[44:47], v[140:143], v[64:79]\n"
    "ds_read_b128 v[40:43], v1 offset:37888\n"
    "s_waitcnt vmcnt(9)\n"
    "v_max3_u32 v15, v15, v147, v148\n"
    "v_max3_u32 v15, v15, v149, v150\n"
    "v_max_u32 v15, v15, v151\n"
    "v_cmp_eq_u32 vcc, -1, v15\n"
    "s_waitcnt lgkmcnt(3)\n"
    "v_mfma_f32_32x32x16_bf16 v[80:95], v[48:51], v[140:143], v[80:95]\n"
    "buffer_load_dwordx4 v[188:191], v0, s[24:27], s42 offen offset:3072 sc1\n"
    "ds_read_b128 v[44:47], v0 offset:1024\n"
    "s_cbranch_vccnz Lre297_%=\n"
    "Lok298_%=:\n"
    "v_mfma_f32_32x32x16_bf16 v[64:79], v[52:55], v[144:147], v[64:79]\n"
    "s_add_u32 s42, s51, 0x4000\n"
    "buffer_load_dwordx4 v[192:195], v0, s[24:27], s42 offen sc1\n"
    "ds_read_b128 v[48:51], v0 offset:38912\n"
    "s_waitcnt lgkmcnt(3)\n"
    "v_mfma_f32_32x32x16_bf16 v[80:95], v[56:59], v[144:147], v[80:95]\n"
    "ds_read_b128 v[52:55], v1 offset:38912\n"
    "v_mfma_f32_32x32x16_bf16 v[64:79], v[60:63], v[148:151], v[64:79]\n"
    "buffer_load_dwordx4 v[196:199], v0, s[24:27], s42 offen offset:1024 sc1\n"
    "ds_read_b128 v[56:59], v0 offset:39936\n"
    "s_waitcnt vmcnt(11)\n"
    "v_max3_u32 v15, v152, v153, v154\n"
    "s_waitcnt lgkmcnt(3)\n"
    "v_mfma_f32_32x32x16_bf16 v[80:95], v[40:43], v[148:151], v[80:95]\n"
    "ds_read_b128 v[60:63], v1 offset:39936\n"
    "s_waitcnt vmcnt(10)\n"
    "v_max3_u32 v15, v15, v155, v156\n"
    "v_max3_u32 v15, v15, v157, v158\n"
    "v_max_u32 v15, v15, v159\n"
    "v_cmp_eq_u32 vcc, -1, v15\n"
    "v_mfma_f32_32x32x16_bf16 v[96:111], v[44:47], v[132:135], v[96:111]\n"
    "ds_read_b128 v[40:43], v0 offset:2048\n"
    "s_cbranch_vccnz Lre299_%=\n"
    "Lok300_%=:\n"
    "s_waitcnt lgkmcnt(3)\n"
    "v_mfma_f32_32x32x16_bf16 v[64:79], v[48:51], v[152:155], v[64:79]\n"
    "buffer_load_dwordx4 v[200:203], v0, s[24:27], s42 offen offset:2048 sc1\n"
    "ds_read_b128 v[44:47], v0 offset:40960\n"
    "v_mfma_f32_32x32x16_bf16 v[80:95], v[52:55], v[152:155], v[80:95]\n"
    "s_add_u32 s43, s51, 0x8000\n"
    "buffer_load_dwordx4 v[112:115], v0, s[24:27], s43 offen sc1\n"
    "ds_read_b128 v[48:51], v1 offset:40960\n"
    "s_waitcnt lgkmcnt(3)\n"
    "v_mfma_f32_32x32x16_bf16 v[64:79], v[56:59], v[156:159], v[64:79]\n"
    "buffer_load_dwordx4 v[204:207], v0, s[24:27], s42 offen offset:3072 sc1\n"
    "ds_read_b128 v[52:55], v0 offset:41984\n"
    "s_waitcnt vmcnt(12)\n"
    "v_max3_u32 v15, v160, v161, v162\n"
    "v_mfma_f32_32x32x16_bf16 v[80:95], v[60:63], v[156:159], v[80:95]\n"
    "ds_read_b128 v[56:59], v1 offset:41984\n"
    "s_waitcnt vmcnt(11)\n"
    "v_max3_u32 v15, v15, v163, v164\n"
    "v_max3_u32 v15, v15, v165, v166\n"
    "v_max_u32 v15, v15, v167\n"
    "v_cmp_eq_u32 vcc, -1, v15\n"
    "s_waitcnt lgkmcnt(3)\n"
    "v_mfma_f32_32x32x16_bf16 v[96:111], v[40:43], v[136:139], v[96:111]\n"
    "ds_read_b128 v[60:63], v0 offset:43008\n"
    "s_cbranch_vccnz Lre301_%=\n"
    "Lok302_%=:\n"
    "v_mfma_f32_32x32x16_bf16 v[64:79], v[44:47], v[160:163], v[64:79]\n"
    "s_add_u32 s42, s51, 0x5000\n"
    "buffer_load_dwordx4 v[208:211], v0, s[24:27], s42 offen sc1\n"
    "ds_read_b128 v[40:43], v1 offset:43008\n"
    "s_waitcnt lgkmcnt(3)\n"
    "v_mfma_f32_32x32x16_bf16 v[80:95], v[48:51], v[160:163], v[80:95]\n"
    "ds_read_b128 v[44:47], v0 offset:3072\n"
    "s_waitcnt vmcnt(11)\n"
    "v_max3_u32 v15, v168, v169, v170\n"
    "v_mfma_f32_32x32x16_bf16 v[64:79], v[52:55], v[164:167], v[64:79]\n"
    "ds_read_b128 v[48:51], v0 offset:44032\n"
    "s_waitcnt vmcnt(10)\n"
    "v_max3_u32 v15, v15, v171, v172\n"
    "v_max3_u32 v15, v15, v173, v174\n"
    "v_max_u32 v15, v15, v175\n"
    "v_cmp_eq_u32 vcc, -1, v15\n"
    "s_waitcnt lgkmcnt(3)\n"
    "v_mfma_f32_32x32x16_bf16 v[80:95], v[56:59], v[164:167], v[80:95]\n"
    "buffer_load_dwordx4 v[212:215], v0, s[24:27], s42 offen offset:1024 sc1\n"
    "ds_read_b128 v[52:55], v1 offset:44032\n"
    "s_cbranch_vccnz Lre303_%=\n"
    "Lok304_%=:\n"
    "v_mfma_f32_32x32x16_bf16 v[64:79], v[60:63], v[168:171], v[64:79]\n"
    "buffer_load_dwordx4 v[216:219], v0, s[24:27], s42 offen offset:2048 sc1\n"
    "ds_read_b128 v[56:59], v0 offset:45056\n"
    "s_waitcnt lgkmcnt(3)\n"
    "v_mfma_f32_32x32x16_bf16 v[80:95], v[40:43], v[168:171], v[80:95]\n"
    "buffer_load_dwordx4 v[116:119], v0, s[24:27], s43 offen offset:1024 sc1\n"
    "ds_read_b128 v[60:63], v1 offset:45056\n"
    "v_mfma_f32_32x32x16_bf16 v[96:111], v[44:47], v[140:143], v[96:111]\n"
    "ds_read_b128 v[40:43], v0 offset:46080\n"
    "s_waitcnt vmcnt(12)\n"
    "v_max3_u32 v15, v176, v177, v178\n"
    "s_waitcnt lgkmcnt(3)\n"
    "v_mfma_f32_32x32x16_bf16 v[64:79], v[48:51], v[172:175], v[64:79]\n"
    "ds_read_b128 v[44:47], v1 offset:46080\n"
    "s_waitcnt vmcnt(11)\n"
    "v_max3_u32 v15, v15, v179, v180\n"
    "v_max3_u32 v15, v15, v181, v182\n"
    "v_max_u32 v15, v15, v183\n"
    "v_cmp_eq_u32 vcc, -1, v15\n"
    "v_mfma_f32_32x32x16_bf16 v[80:95], v[52:55], v[172:175], v[80:95]\n"
    "buffer_load_dwordx4 v[220:223], v0, s[24:27], s42 offen offset:3072 sc1\n"
    "ds_read_b128 v[48:51], v0 offset:4096\n"
    "s_cbranch_vccnz Lre305_%=\n"
    "Lok306_%=:\n"
    "s_waitcnt lgkmcnt(3)\n"
    "v_mfma_f32_32x32x16_bf16 v[64:79], v[56:59], v[176:179], v[64:79]\n"
    "s_add_u32 s42, s51, 0x6000\n"
    "buffer_load_dwordx4 v[224:227], v0, s[24:27], s42 offen sc1\n"
    "ds_read_b128 v[52:55], v0 offset:47104\n"
    "v_mfma_f32_32x32x16_bf16 v[80:95], v[60:63], v[176:179], v[80:95]\n"
    "ds_read_b128 v[56:59], v1 offset:47104\n"
    "s_waitcnt lgkmcnt(3)\n"
    "v_mfma_f32_32x32x16_bf16 v[64:79], v[40:43], v[180:183], v[64:79]\n"
    "buffer_load_dwordx4 v[228:231], v0, s[24:27], s42 offen offset:1024 sc1\n"
    "ds_read_b128 v[60:63], v0 offset:48128\n"
    "s_waitcnt vmcnt(13)\n"
    "v_max3_u32 v15, v184, v185, v186\n"
    "v_mfma_f32_32x32x16_bf16 v[80:95], v[44:47], v[180:183], v[80:95]\n"
    "ds_read_b128 v[40:43], v1 offset:48128\n"
    "s_waitcnt vmcnt(12)\n"
    "v_max3_u32 v15, v15, v187, v188\n"
    "v_max3_u32 v15, v15, v189, v190\n"
    "v_max_u32 v15, v15, v191\n"
    "v_cmp_eq_u32 vcc, -1, v15\n"
    "s_waitcnt lgkmcnt(3)\n"
    "v_mfma_f32_32x32x16_bf16 v[96:111], v[48:51], v[144:147], v[96:111]\n"
    "ds_read_b128 v[44:47], v0 offset:5120\n"
    "s_cbranch_vccnz Lre307_%=\n"
    "Lok308_%=:\n"
    "v_mfma_f32_32x32x16_bf16 v[64:79], v[52:55], v[184:187], v[64:79]\n"
    "buffer_load_dwordx4 v[232:235], v0, s[24:27], s42 offen offset:2048 sc1\n"
    "ds_read_b128 v[48:51], v0 offset:49152\n"
    "s_waitcnt lgkmcnt(3)\n"
    "v_mfma_f32_32x32x16_bf16 v[80:95], v[56:59], v[184:187], v[80:95]\n"
    "ds_read_b128 v[52:55], v1 offset:49152\n"
    "v_mfma_f32_32x32x16_bf16 v[64:79], v[60:63], v[188:191], v[64:79]\n"
    "buffer_load_dwordx4 v[236:239], v0, s[24:27], s42 offen offset:3072 sc1\n"
    "ds_read_b128 v[56:59], v0 offset:50176\n"
    "s_waitcnt vmcnt(13)\n"
    "v_max3_u32 v15, v192, v193, v194\n"
    "s_waitcnt lgkmcnt(3)\n"
    "v_mfma_f32_32x32x16_bf16 v[80:95], v[40:43], v[188:191], v[80:95]\n"
    "ds_read_b128 v[60:63], v1 offset:50176\n"
    "s_waitcnt vmcnt(12)\n"
    "v_max3_u32 v15, v15, v195, v196\n"
    "v_max3_u32 v15, v15, v197, v198\n"
    "v_max_u32 v15, v15, v199\n"
    "v_cmp_eq_u32 vcc, -1, v15\n"
    "v_mfma_f32_32x32x16_bf16 v[96:111], v[44:47], v[148:151], v[96:111]\n"
    "ds_read_b128 v[40:43], v0 offset:51200\n"
    "s_cbranch_vccnz Lre309_%=\n"
    "Lok310_%=:\n"
    "s_waitcnt lgkmcnt(3)\n"
    "v_mfma_f32_32x32x16_bf16 v[64:79], v[48:51], v[192:195], v[64:79]\n"
    "s_add_u32 s42, s51, 0x7000\n"
    "buffer_load_dwordx4 v[240:243], v0, s[24:27], s42 offen sc1\n"
    "ds_read_b128 v[44:47], v1 offset:51200\n"
    "v_mfma_f32_32x32x16_bf16 v[80:95], v[52:55], v[192:195], v[80:95]\n"
    "ds_read_b128 v[48:51], v0 offset:6144\n"
    "s_waitcnt vmcnt(12)\n"
    "v_max3_u32 v15, v200, v201, v202\n"
    "s_waitcnt lgkmcnt(3)\n"
    "v_mfma_f32_32x32x16_bf16 v[64:79], v[56:59], v[196:199], v[64:79]\n"
    "ds_read_b128 v[52:55], v0 offset:52224\n"
    "s_waitcnt vmcnt(10)\n"
    "v_max3_u32 v15, v15, v203, v204\n"
    "v_max3_u32 v15, v15, v205, v206\n"
    "v_max_u32 v15, v15, v207\n"
    "v_cmp_eq_u32 vcc, -1, v15\n"
    "v_mfma_f32_32x32x16_bf16 v[80:95], v[60:63], v[196:199], v[80:95]\n"
    "buffer_load_dwordx4 v[244:247], v0, s[24:27], s42 offen offset:1024 sc1\n"
    "ds_read_b128 v[56:59], v1 offset:52224\n"
    "s_cbranch_vccnz Lre311_%=\n"
    "Lok312_%=:\n"
    "s_waitcnt lgkmcnt(3)\n"
    "v_mfma_f32_32x32x16_bf16 v[64:79], v[40:43], v[200:203], v[64:79]\n"
    "buffer_load_dwordx4 v[248:251], v0, s[24:27], s42 offen offset:2048 sc1\n"
    "ds_read_b128 v[60:63], v0 offset:53248\n"
    "v_mfma_f32_32x32x16_bf16 v[80:95], v[44:47], v[200:203], v[80:95]\n"
    "ds_read_b128 v[40:43], v1 offset:53248\n"
    "s_waitcnt lgkmcnt(3)\n"
    "v_mfma_f32_32x32x16_bf16 v[96:111], v[48:51], v[152:155], v[96:111]\n"
    "ds_read_b128 v[44:47], v0 offset:54272\n"
    "s_waitcnt vmcnt(11)\n"
    "v_max3_u32 v15, v208, v209, v210\n"
    "v_mfma_f32_32x32x16_bf16 v[64:79], v[52:55], v[204:207], v[64:79]\n"
    "ds_read_b128 v[48:51], v1 offset:54272\n"
    "s_waitcnt vmcnt(10)\n"
    "v_max3_u32 v15, v15, v211, v212\n"
    "v_max3_u32 v15, v15, v213, v214\n"
    "v_max_u32 v15, v15, v215\n"
    "v_cmp_eq_u32 vcc, -1, v15\n"
    "s_waitcnt lgkmcnt(3)\n"
    "v_mfma_f32_32x32x16_bf16 v[80:95], v[56:59], v[204:207], v[80:95]\n"
    "buffer_load_dwordx4 v[252:255], v0, s[24:27], s42 offen offset:3072 sc1\n"
    "ds_read_b128 v[52:55], v0 offset:7168\n"
    "s_cbranch_vccnz Lre313_%=\n"
    "Lok314_%=:\n"
    "v_mfma_f32_32x32x16_bf16 v[64:79], v[60:63], v[208:211], v[64:79]\n"
    "ds_read_b128 v[56:59], v0 offset:55296\n"
    "s_waitcnt lgkmcnt(3)\n"
    "v_mfma_f32_32x32x16_bf16 v[80:95], v[40:43], v[208:211], v[80:95]\n"
    "s_add_u32 m0, s78, 0x10000\n"
    "s_add_u32 s47, s68, 0x0\n"
    "buffer_load_dwordx4 v0, s[24:27], s47 offen sc1 lds\n"
    "ds_read_b128 v[60:63], v1 offset:55296\n"
    "s_waitcnt vmcnt(10)\n"
    "v_max3_u32 v32, v119, v113, v114\n"
    "v_mfma_f32_32x32x16_bf16 v[64:79], v[44:47], v[212:215], v[64:79]\n"
    "ds_read_b128 v[40:43], v0 offset:56320\n"
    "v_max3_u32 v15, v216, v217, v218\n"
    "v_max3_u32 v32, v32, v112, v115\n"
    "v_max3_u32 v32, v32, v116, v117\n"
    "s_waitcnt lgkmcnt(3)\n"
    "v_mfma_f32_32x32x16_bf16 v[80:95], v[48:51], v[212:215], v[80:95]\n"
    "ds_read_b128 v[44:47], v1 offset:56320\n"
    "s_waitcnt vmcnt(9)\n"
    "v_max3_u32 v15, v15, v219, v220\n"
    "v_max3_u32 v15, v15, v221, v222\n"
    "v_max_u32 v15, v15, v223\n"
    "v_cmp_eq_u32 vcc, -1, v15\n"
    "v_mfma_f32_32x32x16_bf16 v[96:111], v[52:55], v[156:159], v[96:111]\n"
    "buffer_load_dwordx4 v0, s[24:27], s47 offen offset:1024 sc1 lds\n"
    "ds_read_b128 v[48:51], v0 offset:8192\n"
    "s_cbranch_vccnz Lre315_%=\n"
    "Lok316_%=:\n"
    "v_max_u32 v32, v32, v118\n"
    "s_waitcnt lgkmcnt(3)\n"
    "v_cmp_eq_u32 vcc, -1, v32\n"
    "v_mfma_f32_32x32x16_bf16 v[64:79], v[56:59], v[216:219], v[64:79]\n"
    "ds_read_b128 v[52:55], v0 offset:57344\n"
    "s_cbranch_vccnz Lre325_%=\n"
    "Lok326_%=:\n"
    "v_add_f32 v24, v112, v116\n"
    "v_add_f32 v25, v113, v117\n"
    "v_add_f32 v26, v114, v118\n"
    "v_add_f32 v27, v115, v119\n"
    "v_mfma_f32_32x32x16_bf16 v[80:95], v[60:63], v[216:219], v[80:95]\n"
    "buffer_load_dwordx4 v0, s[24:27], s47 offen offset:2048 sc1 lds\n"
    "ds_read_b128 v[56:59], v1 offset:57344\n"
    "v_permlane32_swap_b32 v24, v25\n"
    "v_permlane32_swap_b32 v26, v27\n"
    "s_waitcnt lgkmcnt(3)\n"
    "v_mfma_f32_32x32x16_bf16 v[64:79], v[40:43], v[220:223], v[64:79]\n"
    "ds_read_b128 v[60:63], v0 offset:58368\n"
    "s_waitcnt vmcnt(10)\n"
    "v_max3_u32 v15, v224, v225, v226\n"
    "v_add_f32 v24, v24, v25\n"
    "v_add_f32 v26, v26, v27\n"
    "s_nop 1\n"
    "v_permlane16_swap_b32 v24, v26\n"
    "v_mfma_f32_32x32x16_bf16 v[80:95], v[44:47], v[220:223], v[80:95]\n"
    "ds_read_b128 v[40:43], v1 offset:58368\n"
    "s_waitcnt vmcnt(9)\n"
    "v_max3_u32 v15, v15, v227, v228\n"
    "v_max3_u32 v15, v15, v229, v230\n"
    "v_max_u32 v15, v15, v231\n"
    "v_cmp_eq_u32 vcc, -1, v15\n"
    "s_waitcnt lgkmcnt(3)\n"
    "v_mfma_f32_32x32x16_bf16 v[96:111], v[48:51], v[160:163], v[96:111]\n"
    "buffer_load_dwordx4 v0, s[24:27], s47 offen offset:3072 sc1 lds\n"
    "ds_read_b128 v[44:47], v0 offset:59392\n"
    "s_cbranch_vccnz Lre317_%=\n"
    "Lok318_%=:\n"
    "v_add_f32 v24, v24, v26\n"
    "ds_bpermute_b32 v28, v13, v24\n"
    "v_mfma_f32_32x32x16_bf16 v[64:79], v[52:55], v[224:227], v[64:79]\n"
    "ds_read_b128 v[48:51], v1 offset:59392\n"
    "ds_bpermute_b32 v29, v13, v24 offset:32\n"
    "s_waitcnt lgkmcnt(5)\n"
    "v_mfma_f32_32x32x16_bf16 v[80:95], v[56:59], v[224:227], v[80:95]\n"
    "s_add_u32 m0, s78, 0x11000\n"
    "s_add_u32 s47, s69, 0x0\n"
    "buffer_load_dwordx4 v0, s[24:27], s47 offen sc1 lds\n"
    "ds_read_b128 v[52:55], v0 offset:9216\n"
    "s_waitcnt vmcnt(10)\n"
    "v_max3_u32 v15, v232, v233, v234\n"
    "v_mfma_f32_32x32x16_bf16 v[64:79], v[60:63], v[228:231], v[64:79]\n"
    "ds_read_b128 v[56:59], v0 offset:60416\n"
    "s_waitcnt vmcnt(9)\n"
    "v_max3_u32 v15, v15, v235, v236\n"
    "v_max3_u32 v15, v15, v237, v238\n"
    "v_max_u32 v15, v15, v239\n"
    "v_cmp_eq_u32 vcc, -1, v15\n"
    "s_waitcnt lgkmcnt(5)\n"
    "v_mfma_f32_32x32x16_bf16 v[80:95], v[40:43], v[228:231], v[80:95]\n"
    "ds_read_b128 v[60:63], v1 offset:60416\n"
    "s_cbranch_vccnz Lre319_%=\n"
    "Lok320_%=:\n"
    "s_waitcnt lgkmcnt(3)\n"
    "v_mul_f32 v28, 0xbb000000, v28\n"
    "v_mul_f32 v29, 0x3b000000, v29\n"
    "v_cvt_pk_bf16_f32 v30, v28, v28\n"
    "v_mfma_f32_32x32x16_bf16 v[64:79], v[44:47], v[232:235], v[64:79]\n"
    "buffer_load_dwordx4 v0, s[24:27], s47 offen offset:1024 sc1 lds\n"
    "ds_read_b128 v[40:43], v0 offset:61440\n"
    "v_fma_f32 v29, -v28, v28, v29\n"
    "v_lshlrev_b32 v31, 16, v30\n"
    "s_waitcnt lgkmcnt(3)\n"
    "v_mfma_f32_32x32x16_bf16 v[80:95], v[48:51], v[232:235], v[80:95]\n"
    "ds_read_b128 v[44:47], v1 offset:61440\n"
    "v_max_f32 v29, 0, v29\n"
    "v_sub_f32 v31, v28, v31\n"
    "v_add_f32 v29, 0x3727c5ac, v29\n"
    "v_cvt_pk_bf16_f32 v31, v31, v31\n"
    "v_mfma_f32_32x32x16_bf16 v[96:111], v[52:55], v[164:167], v[96:111]\n"
    "buffer_load_dwordx4 v0, s[24:27], s47 offen offset:2048 sc1 lds\n"
    "ds_read_b128 v[48:51], v0 offset:62464\n"
    "s_waitcnt vmcnt(10)\n"
    "v_max3_u32 v15, v240, v241, v242\n"
    "s_waitcnt lgkmcnt(3)\n"
    "v_mfma_f32_32x32x16_bf16 v[64:79], v[56:59], v[236:239], v[64:79]\n"
    "ds_read_b128 v[52:55], v1 offset:62464\n"
    "s_waitcnt vmcnt(9)\n"
    "v_max3_u32 v15, v15, v243, v244\n"
    "v_max3_u32 v15, v15, v245, v246\n"
    "v_max_u32 v15, v15, v247\n"
    "v_cmp_eq_u32 vcc, -1, v15\n"
    "v_mfma_f32_32x32x16_bf16 v[80:95], v[60:63], v[236:239], v[80:95]\n"
    "ds_read_b128 v[56:59], v0 offset:10240\n"
    "s_cbranch_vccnz Lre321_%=\n"
    "Lok322_%=:\n"
    "v_rsq_f32 v16, v29\n"
    "v_permlane32_swap_b32 v30, v31\n"
    "s_waitcnt lgkmcnt(3)\n"
    "v_mfma_f32_32x32x16_bf16 v[64:79], v[40:43], v[240:243], v[64:79]\n"
    "buffer_load_dwordx4 v0, s[24:27], s47 offen offset:3072 sc1 lds\n"
    "ds_read_b128 v[60:63], v0 offset:63488\n"
    "v_sqrt_f32 v18, v29\n"
    "v_mul_f32 v17, 0x3eeff558, v16\n"
    "v_mfma_f32_32x32x16_bf16 v[80:95], v[44:47], v[240:243], v[80:95]\n"
    "ds_read_b128 v[40:43], v1 offset:63488\n"
    "v_mul_f32 v18, 0x3eeff558, v18\n"
    "v_mov_b32 v20, v30\n"
    "v_mov_b32 v21, v30\n"
    "v_mov_b32 v22, v30\n"
    "s_waitcnt lgkmcnt(3)\n"
    "v_mfma_f32_32x32x16_bf16 v[64:79], v[48:51], v[244:247], v[64:79]\n"
    "s_add_u32 m0, s86, 0x400\n"
    "s_nop 0\n"
    "buffer_load_dword v3, s[24:27], s47 offen sc1 lds\n"
    "ds_read_b128 v[44:47], v0 offset:64512\n"
    "s_waitcnt vmcnt(10)\n"
    "v_max3_u32 v15, v248, v249, v250\n"
    "v_mfma_f32_32x32x16_bf16 v[80:95], v[52:55], v[244:247], v[80:95]\n"
    "ds_read_b128 v[48:51], v1 offset:64512\n"
    "s_waitcnt vmcnt(9)\n"
    "v_max3_u32 v15, v15, v251, v252\n"
    "v_max3_u32 v15, v15, v253, v254\n"
    "v_max_u32 v15, v15, v255\n"
    "v_cmp_eq_u32 vcc, -1, v15\n"
    "s_waitcnt lgkmcnt(3)\n"
    "v_mfma_f32_32x32x16_bf16 v[96:111], v[56:59], v[168:171], v[96:111]\n"
    "ds_read_b128 v[52:55], v0 offset:11264\n"
    "s_cbranch_vccnz Lre323_%=\n"
    "Lok324_%=:\n"
    "v_mov_b32 v23, v30\n"
    "v_mfma_f32_32x32x16_bf16 v[64:79], v[60:63], v[248:251], v[64:79]\n"
    "buffer_store_dwordx4 v[36:39], v0, s[24:27], s60 offen sc1\n"
    "ds_read_b128 v[56:59], v4 offset:512\n"
    "s_waitcnt lgkmcnt(3)\n"
    "v_mfma_f32_32x32x16_bf16 v[80:95], v[40:43], v[248:251], v[80:95]\n"
    "ds_read_b128 v[60:63], v4 offset:1536\n"
    "v_mfma_f32_32x32x16_bf16 v[64:79], v[44:47], v[252:255], v[64:79]\n"
    "s_add_u32 s44, s60, 0x2000\n"
    "buffer_store_dwordx4 v[36:39], v0, s[24:27], s44 offen sc1\n"
    "ds_read_b128 v[40:43], v0 offset:12288\n"
    "s_waitcnt lgkmcnt(3)\n"
    "v_mfma_f32_32x32x16_bf16 v[80:95], v[48:51], v[252:255], v[80:95]\n"
    "ds_read_b128 v[44:47], v0 offset:13312\n"
    "v_mfma_f32_32x32x16_bf16 v[96:111], v[52:55], v[172:175], v[96:111]\n"
    "ds_read_b128 v[48:51], v0 offset:14336\n"
    "s_waitcnt lgkmcnt(3)\n"
    "v_mfma_f32_32x32x16_bf16 v[64:79], v[56:59], v[20:23], v[64:79]\n"
    "ds_read_b128 v[52:55], v0 offset:15360\n"
    "v_mfma_f32_32x32x16_bf16 v[80:95], v[60:63], v[20:23], v[80:95]\n"
    "s_barrier\n"
    "ds_read_b128 v[56:59], v0 offset:16384\n"
    "s_add_u32 s51, s51, 0x240000\n"
    "s_waitcnt lgkmcnt(3)\n"
    "v_mfma_f32_32x32x16_bf16 v[96:111], v[40:43], v[176:179], v[96:111]\n"
    "s_add_u32 m0, s78, 0x8000\n"
    "s_add_u32 s47, s68, 0x41000\n"
    "buffer_load_dwordx4 v0, s[24:27], s47 offen sc1 lds\n"
    "ds_read_b128 v[60:63], v0 offset:17408\n"
    "v_mfma_f32_32x32x16_bf16 v[96:111], v[44:47], v[180:183], v[96:111]\n"
    "ds_read_b128 v[40:43], v0 offset:18432\n"
    "s_waitcnt lgkmcnt(3)\n"
    "v_mfma_f32_32x32x16_bf16 v[96:111], v[48:51], v[184:187], v[96:111]\n"
    "buffer_load_dwordx4 v0, s[24:27], s47 offen offset:1024 sc1 lds\n"
    "ds_read_b128 v[44:47], v0 offset:19456\n"
    "v_mul_f32 v64, v17, v64\n"
    "v_mul_f32 v65, v17, v65\n"
    "v_mul_f32 v66, v17, v66\n"
    "v_mul_f32 v67, v17, v67\n"
    "v_mfma_f32_32x32x16_bf16 v[96:111], v[52:55], v[188:191], v[96:111]\n"
    "ds_read_b128 v[48:51], v0 offset:20480\n"
    "v_fmaak_f32 v24, v64, v64, 0x409d30fc\n"
    "v_fmaak_f32 v25, v65, v65, 0x409d30fc\n"
    "v_fmaak_f32 v26, v66, v66, 0x409d30fc\n"
    "v_fmaak_f32 v27, v67, v67, 0x409d30fc\n"
    "v_mul_f32 v24, v24, v64\n"
    "s_waitcnt lgkmcnt(3)\n"
    "v_mfma_f32_32x32x16_bf16 v[96:111], v[56:59], v[192:195], v[96:111]\n"
    "buffer_load_dwordx4 v0, s[24:27], s47 offen offset:2048 sc1 lds\n"
    "ds_read_b128 v[52:55], v0 offset:21504\n"
    "v_mul_f32 v25, v25, v65\n"
    "v_mul_f32 v26, v26, v66\n"
    "v_mul_f32 v27, v27, v67\n"
    "v_mfma_f32_32x32x16_bf16 v[96:111], v[60:63], v[196:199], v[96:111]\n"
    "ds_read_b128 v[56:59], v0 offset:22528\n"
    "v_exp_f32 v24, -v24\n"
    "v_mul_f32 v64, v64, v72\n"
    "v_exp_f32 v25, -v25\n"
    "s_waitcnt lgkmcnt(3)\n"
    "v_mfma_f32_32x32x16_bf16 v[96:111], v[40:43], v[200:203], v[96:111]\n"
    "buffer_load_dwordx4 v0, s[24:27], s47 offen offset:3072 sc1 lds\n"
    "ds_read_b128 v[60:63], v0 offset:23552\n"
    "v_mul_f32 v65, v65, v73\n"
    "v_exp_f32 v26, -v26\n"
    "v_mul_f32 v66, v66, v74\n"
    "v_mfma_f32_32x32x16_bf16 v[96:111], v[44:47], v[204:207], v[96:111]\n"
    "ds_read_b128 v[40:43], v0 offset:24576\n"
    "v_exp_f32 v27, -v27\n"
    "v_mul_f32 v67, v67, v75\n"
    "v_fma_f32 v24, v24, v18, v18\n"
    "v_fma_f32 v25, v25, v18, v18\n"
    "s_waitcnt lgkmcnt(3)\n"
    "v_mfma_f32_32x32x16_bf16 v[96:111], v[48:51], v[208:211], v[96:111]\n"
    "s_add_u32 m0, s78, 0x9000\n"
    "s_add_u32 s47, s69, 0x41000\n"
    "buffer_load_dwordx4 v0, s[24:27], s47 offen sc1 lds\n"
    "ds_read_b128 v[44:47], v0 offset:25600\n"
    "v_fma_f32 v26, v26, v18, v18\n"
    "v_fma_f32 v27, v27, v18, v18\n"
    "v_mfma_f32_32x32x16_bf16 v[96:111], v[52:55], v[212:215], v[96:111]\n"
    "ds_read_b128 v[48:51], v0 offset:26624\n"
    "v_rcp_f32 v24, v24\n"
    "v_rcp_f32 v25, v25\n"
    "s_waitcnt lgkmcnt(3)\n"
    "v_mfma_f32_32x32x16_bf16 v[96:111], v[56:59], v[216:219], v[96:111]\n"
    "buffer_load_dwordx4 v0, s[24:27], s47 offen offset:1024 sc1 lds\n"
    "ds_read_b128 v[52:55], v0 offset:27648\n"
    "v_rcp_f32 v26, v26\n"
    "v_rcp_f32 v27, v27\n"
    "v_mfma_f32_32x32x16_bf16 v[96:111], v[60:63], v[220:223], v[96:111]\n"
    "ds_read_b128 v[56:59], v0 offset:28672\n"
    "v_mul_f32 v64, v64, v24\n"
    "v_mul_f32 v65, v65, v25\n"
    "v_mul_f32 v66, v66, v26\n"
    "v_mul_f32 v67, v67, v27\n"
    "v_cvt_pk_bf16_f32 v28, v64, v65\n"
    "s_waitcnt lgkmcnt(3)\n"
    "v_mfma_f32_32x32x16_bf16 v[96:111], v[40:43], v[224:227], v[96:111]\n"
    "buffer_load_dwordx4 v0, s[24:27], s47 offen offset:2048 sc1 lds\n"
    "ds_read_b128 v[60:63], v0 offset:29696\n"
    "v_cvt_pk_bf16_f32 v29, v66, v67\n"
    "v_mul_f32 v68, v17, v68\n"
    "v_mul_f32 v69, v17, v69\n"
    "v_mul_f32 v70, v17, v70\n"
    "v_mfma_f32_32x32x16_bf16 v[96:111], v[44:47], v[228:231], v[96:111]\n"
    "ds_read_b128 v[40:43], v0 offset:30720\n"
    "v_mul_f32 v71, v17, v71\n"
    "v_fmaak_f32 v24, v68, v68, 0x409d30fc\n"
    "v_fmaak_f32 v25, v69, v69, 0x409d30fc\n"
    "v_fmaak_f32 v26, v70, v70, 0x409d30fc\n"
    "v_fmaak_f32 v27, v71, v71, 0x409d30fc\n"
    "s_waitcnt lgkmcnt(3)\n"
    "v_mfma_f32_32x32x16_bf16 v[96:111], v[48:51], v[232:235], v[96:111]\n"
    "buffer_load_dwordx4 v0, s[24:27], s47 offen offset:3072 sc1 lds\n"
    "ds_read_b128 v[44:47], v0 offset:31744\n"
    "v_mul_f32 v24, v24, v68\n"
    "v_mul_f32 v25, v25, v69\n"
    "v_mul_f32 v26, v26, v70\n"
    "v_mul_f32 v27, v27, v71\n"
    "v_mfma_f32_32x32x16_bf16 v[96:111], v[52:55], v[236:239], v[96:111]\n"
    "s_waitcnt vmcnt(10)\n"
    "s_barrier\n"
    "ds_read_b128 v[48:51], v4 offset:0\n"
    "v_exp_f32 v24, -v24\n"
    "v_mul_f32 v68, v68, v76\n"
    "s_waitcnt lgkmcnt(3)\n"
    "v_mfma_f32_32x32x16_bf16 v[96:111], v[56:59], v[240:243], v[96:111]\n"
    "s_add_u32 m0, s86, 0x200\n"
    "s_nop 0\n"
    "buffer_load_dword v3, s[24:27], s47 offen sc1 lds\n"
    "ds_read_b128 v[52:55], v1\n"
    "v_exp_f32 v25, -v25\n"
    "v_mul_f32 v69, v69, v77\n"
    "v_mfma_f32_32x32x16_bf16 v[96:111], v[60:63], v[244:247], v[96:111]\n"
    "ds_read_b128 v[56:59], v1 offset:1024\n"
    "v_exp_f32 v26, -v26\n"
    "v_mul_f32 v70, v70, v78\n"
    "v_exp_f32 v27, -v27\n"
    "s_waitcnt lgkmcnt(3)\n"
    "v_mfma_f32_32x32x16_bf16 v[96:111], v[40:43], v[248:251], v[96:111]\n"
    "s_add_u32 s44, s61, 0x5000\n"
    "buffer_store_dwordx4 v[36:39], v0, s[24:27], s44 offen sc1\n"
    "ds_read_b128 v[60:63], v1 offset:2048\n"
    "v_mul_f32 v71, v71, v79\n"
    "v_fma_f32 v24, v24, v18, v18\n"
    "v_fma_f32 v25, v25, v18, v18\n"
    "v_mfma_f32_32x32x16_bf16 v[96:111], v[44:47], v[252:255], v[96:111]\n"
    "ds_read_b128 v[40:43], v1 offset:3072\n"
    "v_fma_f32 v26, v26, v18, v18\n"
    "v_fma_f32 v27, v27, v18, v18\n"
    "v_rcp_f32 v24, v24\n"
    "s_waitcnt lgkmcnt(3)\n"
    "v_mfma_f32_32x32x16_bf16 v[96:111], v[48:51], v[20:23], v[96:111]\n"
    "s_add_u32 m0, s78, 0x18000\n"
    "s_add_u32 s47, s70, 0xa2800\n"
    "buffer_load_dwordx4 v0, s[24:27], s47 offen sc1 lds\n"
    "ds_read_b128 v[44:47], v1 offset:4096\n"
    "v_rcp_f32 v25, v25\n"
    "v_mfma_f32_32x32x16_bf16 v[112:127], v[52:55], v[128:131], 0\n"
    "ds_read_b128 v[48:51], v1 offset:5120\n"
    "v_rcp_f32 v26, v26\n"
    "v_rcp_f32 v27, v27\n"
    "v_mul_f32 v68, v68, v24\n"
    "s_waitcnt lgkmcnt(3)\n"
    "v_mfma_f32_32x32x16_bf16 v[112:127], v[56:59], v[132:135], v[112:127]\n"
    "buffer_load_dwordx4 v0, s[24:27], s47 offen offset:1024 sc1 lds\n"
    "ds_read_b128 v[52:55], v1 offset:6144\n"
    "v_mul_f32 v69, v69, v25\n"
    "v_mul_f32 v70, v70, v26\n"
    "v_mul_f32 v71, v71, v27\n"
    "v_cvt_pk_bf16_f32 v30, v68, v69\n"
    "v_mfma_f32_32x32x16_bf16 v[112:127], v[60:63], v[136:139], v[112:127]\n"
    "ds_read_b128 v[56:59], v1 offset:7168\n"
    "v_cvt_pk_bf16_f32 v31, v70, v71\n"
    "s_add_u32 s44, s60, 0x3000\n"
    "buffer_store_dwordx4 v[28:31], v0, s[24:27], s44 offen sc1\n"
    "v_mul_f32 v80, v17, v80\n"
    "s_waitcnt lgkmcnt(3)\n"
    "v_mfma_f32_32x32x16_bf16 v[112:127], v[40:43], v[140:143], v[112:127]\n"
    "buffer_load_dwordx4 v0, s[24:27], s47 offen offset:2048 sc1 lds\n"
    "ds_read_b128 v[60:63], v1 offset:8192\n"
    "v_mul_f32 v81, v17, v81\n"
    "v_mul_f32 v82, v17, v82\n"
    "v_mul_f32 v83, v17, v83\n"
    "v_fmaak_f32 v24, v80, v80, 0x409d30fc\n"
    "v_mfma_f32_32x32x16_bf16 v[112:127], v[44:47], v[144:147], v[112:127]\n"
    "ds_read_b128 v[40:43], v1 offset:9216\n"
    "v_fmaak_f32 v25, v81, v81, 0x409d30fc\n"
    "v_fmaak_f32 v26, v82, v82, 0x409d30fc\n"
    "v_fmaak_f32 v27, v83, v83, 0x409d30fc\n"
    "v_mul_f32 v24, v24, v80\n"
    "v_mul_f32 v25, v25, v81\n"
    "s_waitcnt lgkmcnt(3)\n"
    "v_mfma_f32_32x32x16_bf16 v[112:127], v[48:51], v[148:151], v[112:127]\n"
    "buffer_load_dwordx4 v0, s[24:27], s47 offen offset:3072 sc1 lds\n"
    "ds_read_b128 v[44:47], v1 offset:10240\n"
    "v_mul_f32 v26, v26, v82\n"
    "v_mul_f32 v27, v27, v83\n"
    "v_exp_f32 v24, -v24\n"
    "v_mfma_f32_32x32x16_bf16 v[112:127], v[52:55], v[152:155], v[112:127]\n"
    "ds_read_b128 v[48:51], v1 offset:11264\n"
    "v_mul_f32 v80, v80, v88\n"
    "v_exp_f32 v25, -v25\n"
    "v_mul_f32 v81, v81, v89\n"
    "s_waitcnt lgkmcnt(3)\n"
    "v_mfma_f32_32x32x16_bf16 v[112:127], v[56:59], v[156:159], v[112:127]\n"
    "s_add_u32 m0, s78, 0x19000\n"
    "s_add_u32 s47, s71, 0xa2800\n"
    "buffer_load_dwordx4 v0, s[24:27], s47 offen sc1 lds\n"
    "ds_read_b128 v[52:55], v1 offset:12288\n"
    "v_exp_f32 v26, -v26\n"
    "v_mfma_f32_32x32x16_bf16 v[112:127], v[60:63], v[160:163], v[112:127]\n"
    "ds_read_b128 v[56:59], v1 offset:13312\n"
    "v_mul_f32 v82, v82, v90\n"
    "v_exp_f32 v27, -v27\n"
    "v_mul_f32 v83, v83, v91\n"
    "v_fma_f32 v24, v24, v18, v18\n"
    "s_waitcnt lgkmcnt(3)\n"
    "v_mfma_f32_32x32x16_bf16 v[112:127], v[40:43], v[164:167], v[112:127]\n"
    "buffer_load_dwordx4 v0, s[24:27], s47 offen offset:1024 sc1 lds\n"
    "ds_read_b128 v[60:63], v1 offset:14336\n"
    "v_fma_f32 v25, v25, v18, v18\n"
    "v_fma_f32 v26, v26, v18, v18\n"
    "v_fma_f32 v27, v27, v18, v18\n"
    "v_mfma_f32_32x32x16_bf16 v[112:127], v[44:47], v[168:171], v[112:127]\n"
    "ds_read_b128 v[40:43], v1 offset:15360\n"
    "v_rcp_f32 v24, v24\n"
    "v_rcp_f32 v25, v25\n"
    "s_waitcnt lgkmcnt(3)\n"
    "v_mfma_f32_32x32x16_bf16 v[112:127], v[48:51], v[172:175], v[112:127]\n"
    "buffer_load_dwordx4 v0, s[24:27], s47 offen offset:2048 sc1 lds\n"
    "ds_read_b128 v[44:47], v1 offset:16384\n"
    "v_rcp_f32 v26, v26\n"
    "v_rcp_f32 v27, v27\n"
    "v_mfma_f32_32x32x16_bf16 v[112:127], v[52:55], v[176:179], v[112:127]\n"
    "ds_read_b128 v[48:51], v1 offset:17408\n"
    "v_mul_f32 v80, v80, v24\n"
    "v_mul_f32 v81, v81, v25\n"
    "v_mul_f32 v82, v82, v26\n"
    "v_mul_f32 v83, v83, v27\n"
    "v_cvt_pk_bf16_f32 v32, v80, v81\n"
    "s_waitcnt lgkmcnt(3)\n"
    "v_mfma_f32_32x32x16_bf16 v[112:127], v[56:59], v[180:183], v[112:127]\n"
    "buffer_load_dwordx4 v0, s[24:27], s47 offen offset:3072 sc1 lds\n"
    "ds_read_b128 v[52:55], v1 offset:18432\n"
    "v_cvt_pk_bf16_f32 v33, v82, v83\n"
    "v_mul_f32 v84, v17, v84\n"
    "v_mul_f32 v85, v17, v85\n"
    "v_mul_f32 v86, v17, v86\n"
    "v_mfma_f32_32x32x16_bf16 v[112:127], v[60:63], v[184:187], v[112:127]\n"
    "ds_read_b128 v[56:59], v1 offset:19456\n"
    "v_mul_f32 v87, v17, v87\n"
    "v_fmaak_f32 v24, v84, v84, 0x409d30fc\n"
    "v_fmaak_f32 v25, v85, v85, 0x409d30fc\n"
    "v_fmaak_f32 v26, v86, v86, 0x409d30fc\n"
    "v_fmaak_f32 v27, v87, v87, 0x409d30fc\n"
    "s_waitcnt lgkmcnt(3)\n"
    "v_mfma_f32_32x32x16_bf16 v[112:127], v[40:43], v[188:191], v[112:127]\n"
    "s_add_u32 m0, s86, 0x600\n"
    "s_nop 0\n"
    "buffer_load_dword v3, s[24:27], s47 offen sc1 lds\n"
    "ds_read_b128 v[60:63], v1 offset:20480\n"
    "v_mul_f32 v24, v24, v84\n"
    "v_mul_f32 v25, v25, v85\n"
    "v_mul_f32 v26, v26, v86\n"
    "v_mfma_f32_32x32x16_bf16 v[112:127], v[44:47], v[192:195], v[112:127]\n"
    "ds_read_b128 v[40:43], v1 offset:21504\n"
    "v_mul_f32 v27, v27, v87\n"
    "v_exp_f32 v24, -v24\n"
    "v_mul_f32 v84, v84, v92\n"
    "s_waitcnt lgkmcnt(3)\n"
    "v_mfma_f32_32x32x16_bf16 v[112:127], v[48:51], v[196:199], v[112:127]\n"
    "ds_read_b128 v[44:47], v1 offset:22528\n"
    "v_exp_f32 v25, -v25\n"
    "v_mul_f32 v85, v85, v93\n"
    "v_exp_f32 v26, -v26\n"
    "v_mul_f32 v86, v86, v94\n"
    "v_mfma_f32_32x32x16_bf16 v[112:127], v[52:55], v[200:203], v[112:127]\n"
    "ds_read_b128 v[48:51], v1 offset:23552\n"
    "v_exp_f32 v27, -v27\n"
    "v_mul_f32 v87, v87, v95\n"
    "v_fma_f32 v24, v24, v18, v18\n"
    "v_fma_f32 v25, v25, v18, v18\n"
    "s_waitcnt lgkmcnt(3)\n"
    "v_mfma_f32_32x32x16_bf16 v[112:127], v[56:59], v[204:207], v[112:127]\n"
    "ds_read_b128 v[52:55], v1 offset:24576\n"
    "v_fma_f32 v26, v26, v18, v18\n"
    "v_fma_f32 v27, v27, v18, v18\n"
    "v_rcp_f32 v24, v24\n"
    "v_rcp_f32 v25, v25\n"
    "v_mfma_f32_32x32x16_bf16 v[112:127], v[60:63], v[208:211], v[112:127]\n"
    "ds_read_b128 v[56:59], v1 offset:25600\n"
    "v_rcp_f32 v26, v26\n"
    "v_rcp_f32 v27, v27\n"
    "v_mul_f32 v84, v84, v24\n"
    "s_waitcnt lgkmcnt(3)\n"
    "v_mfma_f32_32x32x16_bf16 v[112:127], v[40:43], v[212:215], v[112:127]\n"
    "ds_read_b128 v[60:63], v1 offset:26624\n"
    "v_mul_f32 v85, v85, v25\n"
    "v_mul_f32 v86, v86, v26\n"
    "v_mul_f32 v87, v87, v27\n"
    "v_cvt_pk_bf16_f32 v34, v84, v85\n"
    "v_cvt_pk_bf16_f32 v35, v86, v87\n"
    "v_mfma_f32_32x32x16_bf16 v[112:127], v[44:47], v[216:219], v[112:127]\n"
    "ds_read_b128 v[40:43], v1 offset:27648\n"
    "s_add_u32 s44, s60, 0x1000\n"
    "buffer_store_dwordx4 v[32:35], v0, s[24:27], s44 offen sc1\n"
    "v_mul_f32 v96, v17, v96\n"
    "v_mul_f32 v97, v17, v97\n"
    "s_waitcnt lgkmcnt(3)\n"
    "v_mfma_f32_32x32x16_bf16 v[112:127], v[48:51], v[220:223], v[112:127]\n"
    "ds_read_b128 v[44:47], v1 offset:28672\n"
    "v_mul_f32 v98, v17, v98\n"
    "v_mul_f32 v99, v17, v99\n"
    "v_fmaak_f32 v24, v96, v96, 0x409d30fc\n"
    "v_fmaak_f32 v25, v97, v97, 0x409d30fc\n"
    "v_fmaak_f32 v26, v98, v98, 0x409d30fc\n"
    "v_fmaak_f32 v27, v99, v99, 0x409d30fc\n"
    "v_mfma_f32_32x32x16_bf16 v[112:127], v[52:55], v[224:227], v[112:127]\n"
    "ds_read_b128 v[48:51], v1 offset:29696\n"
    "v_mul_f32 v24, v24, v96\n"
    "v_mul_f32 v25, v25, v97\n"
    "v_mul_f32 v26, v26, v98\n"
    "v_mul_f32 v27, v27, v99\n"
    "s_waitcnt lgkmcnt(3)\n"
    "v_mfma_f32_32x32x16_bf16 v[112:127], v[56:59], v[228:231], v[112:127]\n"
    "ds_read_b128 v[52:55], v1 offset:30720\n"
    "v_exp_f32 v24, -v24\n"
    "v_mul_f32 v96, v96, v104\n"
    "v_exp_f32 v25, -v25\n"
    "v_mul_f32 v97, v97, v105\n"
    "v_mfma_f32_32x32x16_bf16 v[112:127], v[60:63], v[232:235], v[112:127]\n"
    "ds_read_b128 v[56:59], v1 offset:31744\n"
    "v_exp_f32 v26, -v26\n"
    "v_mul_f32 v98, v98, v106\n"
    "v_exp_f32 v27, -v27\n"
    "s_waitcnt lgkmcnt(3)\n"
    "v_mfma_f32_32x32x16_bf16 v[112:127], v[40:43], v[236:239], v[112:127]\n"
    "s_waitcnt vmcnt(12)\n"
    "s_barrier\n"
    "ds_read_b128 v[60:63], v4 offset:1024\n"
    "v_mul_f32 v99, v99, v107\n"
    "v_fma_f32 v24, v24, v18, v18\n"
    "v_fma_f32 v25, v25, v18, v18\n"
    "v_fma_f32 v26, v26, v18, v18\n"
    "v_mfma_f32_32x32x16_bf16 v[112:127], v[44:47], v[240:243], v[112:127]\n"
    "ds_read_b128 v[40:43], v0 offset:32768\n"
    "v_fma_f32 v27, v27, v18, v18\n"
    "v_rcp_f32 v24, v24\n"
    "v_rcp_f32 v25, v25\n"
    "s_waitcnt lgkmcnt(3)\n"
    "v_mfma_f32_32x32x16_bf16 v[112:127], v[48:51], v[244:247], v[112:127]\n"
    "ds_read_b128 v[44:47], v0 offset:33792\n"
    "v_rcp_f32 v26, v26\n"
    "v_rcp_f32 v27, v27\n"
    "v_mul_f32 v96, v96, v24\n"
    "v_mul_f32 v97, v97, v25\n"
    "v_mfma_f32_32x32x16_bf16 v[112:127], v[52:55], v[248:251], v[112:127]\n"
    "ds_read_b128 v[48:51], v0 offset:34816\n"
    "v_mul_f32 v98, v98, v26\n"
    "v_mul_f32 v99, v99, v27\n"
    "v_cvt_pk_bf16_f32 v28, v96, v97\n"
    "v_cvt_pk_bf16_f32 v29, v98, v99\n"
    "v_mul_f32 v100, v17, v100\n"
    "s_waitcnt lgkmcnt(3)\n"
    "v_mfma_f32_32x32x16_bf16 v[112:127], v[56:59], v[252:255], v[112:127]\n"
    "ds_read_b128 v[52:55], v0 offset:35840\n"
    "v_mul_f32 v101, v17, v101\n"
    "v_mul_f32 v102, v17, v102\n"
    "v_mul_f32 v103, v17, v103\n"
    "v_fmaak_f32 v24, v100, v100, 0x409d30fc\n"
    "v_fmaak_f32 v25, v101, v101, 0x409d30fc\n"
    "v_fmaak_f32 v26, v102, v102, 0x409d30fc\n"
    "v_mfma_f32_32x32x16_bf16 v[112:127], v[60:63], v[20:23], v[112:127]\n"
    "ds_read_b128 v[56:59], v0 offset:36864\n"
    "v_fmaak_f32 v27, v103, v103, 0x409d30fc\n"
    "v_mul_f32 v24, v24, v100\n"
    "v_mul_f32 v25, v25, v101\n"
    "v_mul_f32 v26, v26, v102\n"
    "v_mul_f32 v27, v27, v103\n"
    "s_waitcnt lgkmcnt(3)\n"
    "v_mfma_f32_32x32x16_bf16 v[64:79], v[40:43], v[128:131], 0\n"
    "ds_read_b128 v[60:63], v0 offset:37888\n"
    "v_exp_f32 v24, -v24\n"
    "v_mul_f32 v100, v100, v108\n"
    "v_exp_f32 v25, -v25\n"
    "v_mul_f32 v101, v101, v109\n"
    "v_mfma_f32_32x32x16_bf16 v[64:79], v[44:47], v[132:135], v[64:79]\n"
    "ds_read_b128 v[40:43], v0 offset:38912\n"
    "v_exp_f32 v26, -v26\n"
    "v_mul_f32 v102, v102, v110\n"
    "v_exp_f32 v27, -v27\n"
    "s_waitcnt lgkmcnt(3)\n"
    "v_mfma_f32_32x32x16_bf16 v[64:79], v[48:51], v[136:139], v[64:79]\n"
    "ds_read_b128 v[44:47], v0 offset:39936\n"
    "v_mul_f32 v103, v103, v111\n"
    "v_fma_f32 v24, v24, v18, v18\n"
    "v_fma_f32 v25, v25, v18, v18\n"
    "v_fma_f32 v26, v26, v18, v18\n"
    "v_fma_f32 v27, v27, v18, v18\n"
    "v_mfma_f32_32x32x16_bf16 v[64:79], v[52:55], v[140:143], v[64:79]\n"
    "ds_read_b128 v[48:51], v0 offset:40960\n"
    "v_rcp_f32 v24, v24\n"
    "v_rcp_f32 v25, v25\n"
    "s_waitcnt lgkmcnt(3)\n"
    "v_mfma_f32_32x32x16_bf16 v[64:79], v[56:59], v[144:147], v[64:79]\n"
    "ds_read_b128 v[52:55], v0 offset:41984\n"
    "v_rcp_f32 v26, v26\n"
    "v_rcp_f32 v27, v27\n"
    "v_mul_f32 v100, v100, v24\n"
    "v_mfma_f32_32x32x16_bf16 v[64:79], v[60:63], v[148:151], v[64:79]\n"
    "ds_read_b128 v[56:59], v0 offset:43008\n"
    "v_mul_f32 v101, v101, v25\n"
    "v_mul_f32 v102, v102, v26\n"
    "v_mul_f32 v103, v103, v27\n"
    "v_cvt_pk_bf16_f32 v30, v100, v101\n"
    "s_waitcnt lgkmcnt(3)\n"
    "v_mfma_f32_32x32x16_bf16 v[64:79], v[40:43], v[152:155], v[64:79]\n"
    "ds_read_b128 v[60:63], v0 offset:44032\n"
    "v_cvt_pk_bf16_f32 v31, v102, v103\n"
    "s_waitcnt vmcnt(11)\n"
    "s_add_u32 s44, s60, 0x4000\n"
    "buffer_store_dwordx4 v[28:31], v0, s[24:27], s44 offen sc1\n"
    "v_mfma_f32_32x32x16_bf16 v[64:79], v[44:47], v[156:159], v[64:79]\n"
    "ds_read_b128 v[40:43], v0 offset:45056\n"
    "v_mul_f32 v112, v17, v112\n"
    "v_mul_f32 v113, v17, v113\n"
    "v_mul_f32 v114, v17, v114\n"
    "v_mul_f32 v115, v17, v115\n"
    "s_waitcnt lgkmcnt(3)\n"
    "v_mfma_f32_32x32x16_bf16 v[64:79], v[48:51], v[160:163], v[64:79]\n"
    "s_waitcnt vmcnt(1)\n"
    "s_barrier\n"
    "v_lshlrev_b32 v36, 3, v0\n"
    "v_mov_b32 v37, 1\n"
    "s_mov_b64 exec, s[18:19]\n"
    "buffer_atomic_add v37, v36, s[24:27], s88 offen offset:32 sc1\n"
    "s_mov_b64 exec, -1\n"
    "ds_read_b128 v[44:47], v0 offset:46080\n"
    "v_mfma_f32_32x32x16_bf16 v[64:79], v[52:55], v[164:167], v[64:79]\n"
    "ds_read_b128 v[48:51], v0 offset:47104\n"
    "v_fmaak_f32 v24, v112, v112, 0x409d30fc\n"
    "v_fmaak_f32 v25, v113, v113, 0x409d30fc\n"
    "v_fmaak_f32 v26, v114, v114, 0x409d30fc\n"
    "v_fmaak_f32 v27, v115, v115, 0x409d30fc\n"
    "s_waitcnt lgkmcnt(3)\n"
    "v_mfma_f32_32x32x16_bf16 v[64:79], v[56:59], v[168:171], v[64:79]\n"
    "ds_read_b128 v[52:55], v0 offset:48128\n"
    "v_mul_f32 v24, v24, v112\n"
    "v_mul_f32 v25, v25, v113\n"
    "v_mul_f32 v26, v26, v114\n"
    "v_mul_f32 v27, v27, v115\n"
    "v_mfma_f32_32x32x16_bf16 v[64:79], v[60:63], v[172:175], v[64:79]\n"
    "ds_read_b128 v[56:59], v0 offset:49152\n"
    "v_exp_f32 v24, -v24\n"
    "v_mul_f32 v112, v112, v120\n"
    "s_waitcnt lgkmcnt(3)\n"
    "v_mfma_f32_32x32x16_bf16 v[64:79], v[40:43], v[176:179], v[64:79]\n"
    "ds_read_b128 v[60:63], v0 offset:50176\n"
    "v_exp_f32 v25, -v25\n"
    "v_mul_f32 v113, v113, v121\n"
    "v_exp_f32 v26, -v26\n"
    "v_mfma_f32_32x32x16_bf16 v[64:79], v[44:47], v[180:183], v[64:79]\n"
    "ds_read_b128 v[40:43], v0 offset:51200\n"
    "v_mul_f32 v114, v114, v122\n"
    "v_exp_f32 v27, -v27\n"
    "v_mul_f32 v115, v115, v123\n"
    "s_waitcnt lgkmcnt(3)\n"
    "v_mfma_f32_32x32x16_bf16 v[64:79], v[48:51], v[184:187], v[64:79]\n"
    "ds_read_b128 v[44:47], v0 offset:52224\n"
    "v_fma_f32 v24, v24, v18, v18\n"
    "v_fma_f32 v25, v25, v18, v18\n"
    "v_fma_f32 v26, v26, v18, v18\n"
    "v_fma_f32 v27, v27, v18, v18\n"
    "v_mfma_f32_32x32x16_bf16 v[64:79], v[52:55], v[188:191], v[64:79]\n"
    "ds_read_b128 v[48:51], v0 offset:53248\n"
    "v_rcp_f32 v24, v24\n"
    "v_rcp_f32 v25, v25\n"
    "s_waitcnt lgkmcnt(3)\n"
    "v_mfma_f32_32x32x16_bf16 v[64:79], v[56:59], v[192:195], v[64:79]\n"
    "ds_read_b128 v[52:55], v0 offset:54272\n"
    "v_rcp_f32 v26, v26\n"
    "v_rcp_f32 v27, v27\n"
    "v_mul_f32 v112, v112, v24\n"
    "v_mfma_f32_32x32x16_bf16 v[64:79], v[60:63], v[196:199], v[64:79]\n"
    "ds_read_b128 v[56:59], v0 offset:55296\n"
    "v_mul_f32 v113, v113, v25\n"
    "v_mul_f32 v114, v114, v26\n"
    "v_mul_f32 v115, v115, v27\n"
    "v_cvt_pk_bf16_f32 v32, v112, v113\n"
    "s_waitcnt lgkmcnt(3)\n"
    "v_mfma_f32_32x32x16_bf16 v[64:79], v[40:43], v[200:203], v[64:79]\n"
    "ds_read_b128 v[60:63], v0 offset:56320\n"
    "v_cvt_pk_bf16_f32 v33, v114, v115\n"
    "v_mul_f32 v116, v17, v116\n"
    "v_mul_f32 v117, v17, v117\n"
    "v_mul_f32 v118, v17, v118\n"
    "v_mul_f32 v119, v17, v119\n"
    "v_mfma_f32_32x32x16_bf16 v[64:79], v[44:47], v[204:207], v[64:79]\n"
    "ds_read_b128 v[40:43], v0 offset:57344\n"
    "v_fmaak_f32 v24, v116, v116, 0x409d30fc\n"
    "v_fmaak_f32 v25, v117, v117, 0x409d30fc\n"
    "v_fmaak_f32 v26, v118, v118, 0x409d30fc\n"
    "v_fmaak_f32 v27, v119, v119, 0x409d30fc\n"
    "s_waitcnt lgkmcnt(3)\n"
    "v_mfma_f32_32x32x16_bf16 v[64:79], v[48:51], v[208:211], v[64:79]\n"
    "v_mov_b32 v36, 0\n"
    "s_mov_b64 exec, s[16:17]\n"
    "buffer_atomic_add v36, off, s[24:27], s92 offset:32 sc0 sc1\n"
    "s_mov_b64 exec, -1\n"
    "ds_read_b128 v[44:47], v0 offset:58368\n"
    "v_mfma_f32_32x32x16_bf16 v[64:79], v[52:55], v[212:215], v[64:79]\n"
    "ds_read_b128 v[48:51], v0 offset:59392\n"
    "v_mul_f32 v24, v24, v116\n"
    "v_mul_f32 v25, v25, v117\n"
    "v_mul_f32 v26, v26, v118\n"
    "v_mul_f32 v27, v27, v119\n"
    "s_waitcnt lgkmcnt(3)\n"
    "v_mfma_f32_32x32x16_bf16 v[64:79], v[56:59], v[216:219], v[64:79]\n"
    "ds_read_b128 v[52:55], v0 offset:60416\n"
    "v_exp_f32 v24, -v24\n"
    "v_mul_f32 v116, v116, v124\n"
    "v_exp_f32 v25, -v25\n"
    "v_mfma_f32_32x32x16_bf16 v[64:79], v[60:63], v[220:223], v[64:79]\n"
    "ds_read_b128 v[56:59], v0 offset:61440\n"
    "v_mul_f32 v117, v117, v125\n"
    "v_exp_f32 v26, -v26\n"
    "v_mul_f32 v118, v118, v126\n"
    "s_waitcnt lgkmcnt(3)\n"
    "v_mfma_f32_32x32x16_bf16 v[64:79], v[40:43], v[224:227], v[64:79]\n"
    "ds_read_b128 v[60:63], v0 offset:62464\n"
    "v_exp_f32 v27, -v27\n"
    "v_mul_f32 v119, v119, v127\n"
    "v_fma_f32 v24, v24, v18, v18\n"
    "v_fma_f32 v25, v25, v18, v18\n"
    "v_mfma_f32_32x32x16_bf16 v[64:79], v[44:47], v[228:231], v[64:79]\n"
    "ds_read_b128 v[40:43], v0 offset:63488\n"
    "v_fma_f32 v26, v26, v18, v18\n"
    "v_fma_f32 v27, v27, v18, v18\n"
    "v_rcp_f32 v24, v24\n"
    "s_waitcnt lgkmcnt(3)\n"
    "v_mfma_f32_32x32x16_bf16 v[64:79], v[48:51], v[232:235], v[64:79]\n"
    "ds_read_b128 v[44:47], v0 offset:64512\n"
    "v_rcp_f32 v25, v25\n"
    "v_rcp_f32 v26, v26\n"
    "v_mfma_f32_32x32x16_bf16 v[64:79], v[52:55], v[236:239], v[64:79]\n"
    "s_barrier\n"
    "ds_read_b128 v[48:51], v4 offset:512\n"
    "v_rcp_f32 v27, v27\n"
    "s_waitcnt lgkmcnt(3)\n"
    "v_mfma_f32_32x32x16_bf16 v[64:79], v[56:59], v[240:243], v[64:79]\n"
    "ds_read_b128 v[52:55], v1 offset:32768\n"
    "v_mul_f32 v116, v116, v24\n"
    "v_mul_f32 v117, v117, v25\n"
    "v_mul_f32 v118, v118, v26\n"
    "v_mul_f32 v119, v119, v27\n"
    "v_cvt_pk_bf16_f32 v34, v116, v117\n"
    "v_mfma_f32_32x32x16_bf16 v[64:79], v[60:63], v[244:247], v[64:79]\n"
    "ds_read_b128 v[56:59], v1 offset:33792\n"
    "v_cvt_pk_bf16_f32 v35, v118, v119\n"
    "buffer_store_dwordx4 v[32:35], v0, s[24:27], s60 offen sc1\n"
    "s_waitcnt lgkmcnt(3)\n"
    "v_mfma_f32_32x32x16_bf16 v[64:79], v[40:43], v[248:251], v[64:79]\n"
    "ds_read_b128 v[60:63], v1 offset:34816\n"
    "v_mfma_f32_32x32x16_bf16 v[64:79], v[44:47], v[252:255], v[64:79]\n"
    "ds_read_b128 v[40:43], v1 offset:35840\n"
    "s_waitcnt lgkmcnt(3)\n"
    "v_mfma_f32_32x32x16_bf16 v[64:79], v[48:51], v[20:23], v[64:79]\n"
    "ds_read_b128 v[44:47], v1 offset:36864\n"
    "v_mfma_f32_32x32x16_bf16 v[80:95], v[52:55], v[128:131], 0\n"
    "ds_read_b128 v[48:51], v1 offset:37888\n"
    "s_waitcnt lgkmcnt(3)\n"
    "v_mfma_f32_32x32x16_bf16 v[80:95], v[56:59], v[132:135], v[80:95]\n"
    "ds_read_b128 v[52:55], v1 offset:38912\n"
    "v_mfma_f32_32x32x16_bf16 v[80:95], v[60:63], v[136:139], v[80:95]\n"
    "ds_read_b128 v[56:59], v1 offset:39936\n"
    "s_nop 3\n"
    "v_mul_f32 v64, v17, v64\n"
    "v_mul_f32 v65, v17, v65\n"
    "v_mul_f32 v66, v17, v66\n"
    "v_mul_f32 v67, v17, v67\n"
    "s_waitcnt lgkmcnt(3)\n"
    "v_mfma_f32_32x32x16_bf16 v[80:95], v[40:43], v[140:143], v[80:95]\n"
    "ds_read_b128 v[60:63], v1 offset:40960\n"
    "s_cmp_lg_u32 s37, 0\n"
    "s_cbranch_scc1 Lhs329_%=\n"
    "s_mov_b64 exec, 1\n"
    "v_mov_b32 v37, 0\n"
    "buffer_atomic_add v37, off, s[24:27], s92 offset:32 sc0 sc1\n"
    "s_waitcnt vmcnt(2)\n"
    "v_cmp_eq_u32 vcc, 16, v36\n"
    "s_cbranch_vccnz Lhd328_%=\n"
    "v_mov_b32 v36, 0\n"
    "buffer_atomic_add v36, off, s[24:27], s92 offset:32 sc0 sc1\n"
    "Lhp327_%=:\n"
    "s_waitcnt vmcnt(1)\n"
    "v_cmp_eq_u32 vcc, 16, v37\n"
    "s_cbranch_vccnz Lhd328_%=\n"
    "v_mov_b32 v37, 0\n"
    "buffer_atomic_add v37, off, s[24:27], s92 offset:32 sc0 sc1\n"
    "s_waitcnt vmcnt(1)\n"
    "v_cmp_eq_u32 vcc, 16, v36\n"
    "s_cbranch_vccnz Lhd328_%=\n"
    "v_mov_b32 v36, 0\n"
    "buffer_atomic_add v36, off, s[24:27], s92 offset:32 sc0 sc1\n"
    "s_sub_u32 s50, s50, 1\n"
    "s_cbranch_scc0 Lhp327_%=\n"
    "s_branch Lfail_%=\n"
    "Lhd328_%=:\n"
    "s_mov_b64 exec, -1\n"
    "Lhs329_%=:\n"
    "s_barrier\n"
    "v_mfma_f32_32x32x16_bf16 v[80:95], v[44:47], v[144:147], v[80:95]\n"
    "s_add_u32 s42, s63, 0x3000\n"
    "buffer_load_dwordx4 v[128:131], v0, s[24:27], s42 offen sc1\n"
    "ds_read_b128 v[40:43], v1 offset:41984\n"
    "v_fmaak_f32 v24, v64, v64, 0x409d30fc\n"
    "s_waitcnt lgkmcnt(3)\n"
    "v_mfma_f32_32x32x16_bf16 v[80:95], v[48:51], v[148:151], v[80:95]\n"
    "ds_read_b128 v[44:47], v1 offset:43008\n"
    "v_fmaak_f32 v25, v65, v65, 0x409d30fc\n"
    "v_fmaak_f32 v26, v66, v66, 0x409d30fc\n"
    "v_fmaak_f32 v27, v67, v67, 0x409d30fc\n"
    "v_mul_f32 v24, v24, v64\n"
    "v_mul_f32 v25, v25, v65\n"
    "v_mfma_f32_32x32x16_bf16 v[80:95], v[52:55], v[152:155], v[80:95]\n"
    "buffer_load_dwordx4 v[132:135], v0, s[24:27], s42 offen offset:1024 sc1\n"
    "ds_read_b128 v[48:51], v1 offset:44032\n"
    "v_mul_f32 v26, v26, v66\n"
    "v_mul_f32 v27, v27, v67\n"
    "s_waitcnt lgkmcnt(3)\n"
    "v_mfma_f32_32x32x16_bf16 v[80:95], v[56:59], v[156:159], v[80:95]\n"
    "ds_read_b128 v[52:55], v1 offset:45056\n"
    "v_exp_f32 v24, -v24\n"
    "v_mul_f32 v64, v64, v72\n"
    "v_exp_f32 v25, -v25\n"
    "v_mfma_f32_32x32x16_bf16 v[80:95], v[60:63], v[160:163], v[80:95]\n"
    "buffer_load_dwordx4 v[136:139], v0, s[24:27], s42 offen offset:2048 sc1\n"
    "ds_read_b128 v[56:59], v1 offset:46080\n"
    "v_mul_f32 v65, v65, v73\n"
    "s_waitcnt lgkmcnt(3)\n"
    "v_mfma_f32_32x32x16_bf16 v[80:95], v[40:43], v[164:167], v[80:95]\n"
    "ds_read_b128 v[60:63], v1 offset:47104\n"
    "v_exp_f32 v26, -v26\n"
    "v_mul_f32 v66, v66, v74\n"
    "v_exp_f32 v27, -v27\n"
    "v_mfma_f32_32x32x16_bf16 v[80:95], v[44:47], v[168:171], v[80:95]\n"
    "buffer_load_dwordx4 v[140:143], v0, s[24:27], s42 offen offset:3072 sc1\n"
    "ds_read_b128 v[40:43], v1 offset:48128\n"
    "v_mul_f32 v67, v67, v75\n"
    "v_fma_f32 v24, v24, v18, v18\n"
    "s_waitcnt lgkmcnt(3)\n"
    "v_mfma_f32_32x32x16_bf16 v[80:95], v[48:51], v[172:175], v[80:95]\n"
    "ds_read_b128 v[44:47], v1 offset:49152\n"
    "v_fma_f32 v25, v25, v18, v18\n"
    "v_fma_f32 v26, v26, v18, v18\n"
    "v_fma_f32 v27, v27, v18, v18\n"
    "v_rcp_f32 v24, v24\n"
    "v_mfma_f32_32x32x16_bf16 v[80:95], v[52:55], v[176:179], v[80:95]\n"
    "s_add_u32 s42, s63, 0x1000\n"
    "buffer_load_dwordx4 v[144:147], v0, s[24:27], s42 offen sc1\n"
    "ds_read_b128 v[48:51], v1 offset:50176\n"
    "s_waitcnt lgkmcnt(3)\n"
    "v_mfma_f32_32x32x16_bf16 v[80:95], v[56:59], v[180:183], v[80:95]\n"
    "ds_read_b128 v[52:55], v1 offset:51200\n"
    "v_rcp_f32 v25, v25\n"
    "v_rcp_f32 v26, v26\n"
    "v_mfma_f32_32x32x16_bf16 v[80:95], v[60:63], v[184:187], v[80:95]\n"
    "buffer_load_dwordx4 v[148:151], v0, s[24:27], s42 offen offset:1024 sc1\n"
    "ds_read_b128 v[56:59], v1 offset:52224\n"
    "v_rcp_f32 v27, v27\n"
    "s_waitcnt lgkmcnt(3)\n"
    "v_mfma_f32_32x32x16_bf16 v[80:95], v[40:43], v[188:191], v[80:95]\n"
    "ds_read_b128 v[60:63], v1 offset:53248\n"
    "v_mul_f32 v64, v64, v24\n"
    "v_mul_f32 v65, v65, v25\n"
    "v_mul_f32 v66, v66, v26\n"
    "v_mul_f32 v67, v67, v27\n"
    "v_cvt_pk_bf16_f32 v28, v64, v65\n"
    "v_mfma_f32_32x32x16_bf16 v[80:95], v[44:47], v[192:195], v[80:95]\n"
    "buffer_load_dwordx4 v[152:155], v0, s[24:27], s42 offen offset:2048 sc1\n"
    "ds_read_b128 v[40:43], v1 offset:54272\n"
    "v_cvt_pk_bf16_f32 v29, v66, v67\n"
    "v_mul_f32 v68, v17, v68\n"
    "s_waitcnt lgkmcnt(3)\n"
    "v_mfma_f32_32x32x16_bf16 v[80:95], v[48:51], v[196:199], v[80:95]\n"
    "ds_read_b128 v[44:47], v1 offset:55296\n"
    "v_mul_f32 v69, v17, v69\n"
    "v_mul_f32 v70, v17, v70\n"
    "v_mul_f32 v71, v17, v71\n"
    "v_fmaak_f32 v24, v68, v68, 0x409d30fc\n"
    "v_fmaak_f32 v25, v69, v69, 0x409d30fc\n"
    "v_mfma_f32_32x32x16_bf16 v[80:95], v[52:55], v[200:203], v[80:95]\n"
    "buffer_load_dwordx4 v[156:159], v0, s[24:27], s42 offen offset:3072 sc1\n"
    "ds_read_b128 v[48:51], v1 offset:56320\n"
    "v_fmaak_f32 v26, v70, v70, 0x409d30fc\n"
    "v_fmaak_f32 v27, v71, v71, 0x409d30fc\n"
    "s_waitcnt lgkmcnt(3)\n"
    "v_mfma_f32_32x32x16_bf16 v[80:95], v[56:59], v[204:207], v[80:95]\n"
    "ds_read_b128 v[52:55], v1 offset:57344\n"
    "v_mul_f32 v24, v24, v68\n"
    "v_mul_f32 v25, v25, v69\n"
    "v_mul_f32 v26, v26, v70\n"
    "v_mul_f32 v27, v27, v71\n"
    "v_mfma_f32_32x32x16_bf16 v[80:95], v[60:63], v[208:211], v[80:95]\n"
    "s_add_u32 s42, s62, 0x3000\n"
    "buffer_load_dwordx4 v[160:163], v0, s[24:27], s42 offen sc1\n"
    "ds_read_b128 v[56:59], v1 offset:58368\n"
    "s_waitcnt lgkmcnt(3)\n"
    "v_mfma_f32_32x32x16_bf16 v[80:95], v[40:43], v[212:215], v[80:95]\n"
    "ds_read_b128 v[60:63], v1 offset:59392\n"
    "v_exp_f32 v24, -v24\n"
    "v_mul_f32 v68, v68, v76\n"
    "v_exp_f32 v25, -v25\n"
    "v_mfma_f32_32x32x16_bf16 v[80:95], v[44:47], v[216:219], v[80:95]\n"
    "buffer_load_dwordx4 v[164:167], v0, s[24:27], s42 offen offset:1024 sc1\n"
    "ds_read_b128 v[40:43], v1 offset:60416\n"
    "v_mul_f32 v69, v69, v77\n"
    "s_waitcnt lgkmcnt(3)\n"
    "v_mfma_f32_32x32x16_bf16 v[80:95], v[48:51], v[220:223], v[80:95]\n"
    "ds_read_b128 v[44:47], v1 offset:61440\n"
    "v_exp_f32 v26, -v26\n"
    "v_mul_f32 v70, v70, v78\n"
    "v_exp_f32 v27, -v27\n"
    "v_mfma_f32_32x32x16_bf16 v[80:95], v[52:55], v[224:227], v[80:95]\n"
    "buffer_load_dwordx4 v[168:171], v0, s[24:27], s42 offen offset:2048 sc1\n"
    "ds_read_b128 v[48:51], v1 offset:62464\n"
    "v_mul_f32 v71, v71, v79\n"
    "v_fma_f32 v24, v24, v18, v18\n"
    "s_waitcnt lgkmcnt(3)\n"
    "v_mfma_f32_32x32x16_bf16 v[80:95], v[56:59], v[228:231], v[80:95]\n"
    "ds_read_b128 v[52:55], v1 offset:63488\n"
    "v_fma_f32 v25, v25, v18, v18\n"
    "v_fma_f32 v26, v26, v18, v18\n"
    "v_fma_f32 v27, v27, v18, v18\n"
    "v_rcp_f32 v24, v24\n"
    "v_mfma_f32_32x32x16_bf16 v[80:95], v[60:63], v[232:235], v[80:95]\n"
    "buffer_load_dwordx4 v[172:175], v0, s[24:27], s42 offen offset:3072 sc1\n"
    "ds_read_b128 v[56:59], v1 offset:64512\n"
    "v_rcp_f32 v25, v25\n"
    "s_waitcnt lgkmcnt(3)\n"
    "v_mfma_f32_32x32x16_bf16 v[80:95], v[40:43], v[236:239], v[80:95]\n"
    "ds_read_b128 v[60:63], v4 offset:1536\n"
    "v_rcp_f32 v26, v26\n"
    "v_rcp_f32 v27, v27\n"
    "v_mul_f32 v68, v68, v24\n"
    "v_mfma_f32_32x32x16_bf16 v[80:95], v[44:47], v[240:243], v[80:95]\n"
    "s_add_u32 s42, s62, 0x1000\n"
    "buffer_load_dwordx4 v[176:179], v0, s[24:27], s42 offen sc1\n"
    "v_mul_f32 v69, v69, v25\n"
    "v_mul_f32 v70, v70, v26\n"
    "s_waitcnt lgkmcnt(2)\n"
    "v_mfma_f32_32x32x16_bf16 v[80:95], v[48:51], v[244:247], v[80:95]\n"
    "v_mul_f32 v71, v71, v27\n"
    "v_cvt_pk_bf16_f32 v30, v68, v69\n"
    "v_cvt_pk_bf16_f32 v31, v70, v71\n"
    "s_add_u32 s44, s60, 0x2000\n"
    "buffer_store_dwordx4 v[28:31], v0, s[24:27], s44 offen sc1\n"
    "v_mfma_f32_32x32x16_bf16 v[80:95], v[52:55], v[248:251], v[80:95]\n"
    "buffer_load_dwordx4 v[180:183], v0, s[24:27], s42 offen offset:1024 sc1\n"
    "s_waitcnt lgkmcnt(0)\n"
    "v_mfma_f32_32x32x16_bf16 v[80:95], v[56:59], v[252:255], v[80:95]\n"
    "v_mfma_f32_32x32x16_bf16 v[80:95], v[60:63], v[20:23], v[80:95]\n"
    "buffer_load_dwordx4 v[184:187], v0, s[24:27], s42 offen offset:2048 sc1\n"
    "s_barrier\n"
    "s_waitcnt vmcnt(14)\n"
    "v_mfma_f32_32x32x16_bf16 v[112:127], a[136:139], v[128:131], 0\n"
    "v_mfma_f32_32x32x16_bf16 v[96:111], a[48:51], v[128:131], 0\n"
    "buffer_load_dwordx4 v[188:191], v0, s[24:27], s42 offen offset:3072 sc1\n"
    "s_nop 5\n"
    "v_mul_f32 v80, v17, v80\n"
    "v_mul_f32 v81, v17, v81\n"
    "v_mul_f32 v82, v17, v82\n"
    "v_mul_f32 v83, v17, v83\n"
    "v_fmaak_f32 v24, v80, v80, 0x409d30fc\n"
    "v_mfma_f32_32x32x16_bf16 v[112:127], a[140:143], v[132:135], v[112:127]\n"
    "v_fmaak_f32 v25, v81, v81, 0x409d30fc\n"
    "v_fmaak_f32 v26, v82, v82, 0x409d30fc\n"
    "v_fmaak_f32 v27, v83, v83, 0x409d30fc\n"
    "v_mul_f32 v24, v24, v80\n"
    "v_mul_f32 v25, v25, v81\n"
    "v_mul_f32 v26, v26, v82\n"
    "v_mul_f32 v27, v27, v83\n"
    "v_mfma_f32_32x32x16_bf16 v[96:111], a[52:55], v[132:135], v[96:111]\n"
    "s_add_u32 s42, s63, 0x4000\n"
    "buffer_load_dwordx4 v[192:195], v0, s[24:27], s42 offen sc1\n"
    "v_exp_f32 v24, -v24\n"
    "v_mul_f32 v80, v80, v88\n"
    "s_waitcnt vmcnt(14)\n"
    "v_mfma_f32_32x32x16_bf16 v[112:127], a[144:147], v[136:139], v[112:127]\n"
    "v_exp_f32 v25, -v25\n"
    "v_mul_f32 v81, v81, v89\n"
    "v_exp_f32 v26, -v26\n"
    "v_mul_f32 v82, v82, v90\n"
    "v_mfma_f32_32x32x16_bf16 v[96:111], a[56:59], v[136:139], v[96:111]\n"
    "buffer_load_dwordx4 v[196:199], v0, s[24:27], s42 offen offset:1024 sc1\n"
    "v_exp_f32 v27, -v27\n"
    "v_mul_f32 v83, v83, v91\n"
    "v_fma_f32 v24, v24, v18, v18\n"
    "v_fma_f32 v25, v25, v18, v18\n"
    "v_mfma_f32_32x32x16_bf16 v[112:127], a[148:151], v[140:143], v[112:127]\n"
    "v_fma_f32 v26, v26, v18, v18\n"
    "v_fma_f32 v27, v27, v18, v18\n"
    "v_rcp_f32 v24, v24\n"
    "v_rcp_f32 v25, v25\n"
    "v_mfma_f32_32x32x16_bf16 v[96:111], a[60:63], v[140:143], v[96:111]\n"
    "buffer_load_dwordx4 v[200:203], v0, s[24:27], s42 offen offset:2048 sc1\n"
    "v_rcp_f32 v26, v26\n"
    "v_rcp_f32 v27, v27\n"
    "s_waitcnt vmcnt(14)\n"
    "v_mfma_f32_32x32x16_bf16 v[112:127], a[104:107], v[144:147], v[112:127]\n"
    "v_mul_f32 v80, v80, v24\n"
    "v_mul_f32 v81, v81, v25\n"
    "v_mul_f32 v82, v82, v26\n"
    "v_mul_f32 v83, v83, v27\n"
    "v_cvt_pk_bf16_f32 v32, v80, v81\n"
    "v_cvt_pk_bf16_f32 v33, v82, v83\n"
    "v_mul_f32 v84, v17, v84\n"
    "v_mfma_f32_32x32x16_bf16 v[96:111], a[16:19], v[144:147], v[96:111]\n"
    "buffer_load_dwordx4 v[204:207], v0, s[24:27], s42 offen offset:3072 sc1\n"
    "v_mul_f32 v85, v17, v85\n"
    "v_mul_f32 v86, v17, v86\n"
    "v_mul_f32 v87, v17, v87\n"
    "v_fmaak_f32 v24, v84, v84, 0x409d30fc\n"
    "v_fmaak_f32 v25, v85, v85, 0x409d30fc\n"
    "v_mfma_f32_32x32x16_bf16 v[112:127], a[108:111], v[148:151], v[112:127]\n"
    "v_fmaak_f32 v26, v86, v86, 0x409d30fc\n"
    "v_fmaak_f32 v27, v87, v87, 0x409d30fc\n"
    "v_mul_f32 v24, v24, v84\n"
    "v_mul_f32 v25, v25, v85\n"
    "v_mul_f32 v26, v26, v86\n"
    "v_mul_f32 v27, v27, v87\n"
    "v_mfma_f32_32x32x16_bf16 v[96:111], a[20:23], v[148:151], v[96:111]\n"
    "s_add_u32 s42, s62, 0x4000\n"
    "buffer_load_dwordx4 v[208:211], v0, s[24:27], s42 offen sc1\n"
    "v_exp_f32 v24, -v24\n"
    "v_mul_f32 v84, v84, v92\n"
    "s_waitcnt vmcnt(14)\n"
    "v_mfma_f32_32x32x16_bf16 v[112:127], a[112:115], v[152:155], v[112:127]\n"
    "v_exp_f32 v25, -v25\n"
    "v_mul_f32 v85, v85, v93\n"
    "v_exp_f32 v26, -v26\n"
    "v_mul_f32 v86, v86, v94\n"
    "v_mfma_f32_32x32x16_bf16 v[96:111], a[24:27], v[152:155], v[96:111]\n"
    "buffer_load_dwordx4 v[212:215], v0, s[24:27], s42 offen offset:1024 sc1\n"
    "v_exp_f32 v27, -v27\n"
    "v_mul_f32 v87, v87, v95\n"
    "v_fma_f32 v24, v24, v18, v18\n"
    "v_fma_f32 v25, v25, v18, v18\n"
    "v_mfma_f32_32x32x16_bf16 v[112:127], a[116:119], v[156:159], v[112:127]\n"
    "v_fma_f32 v26, v26, v18, v18\n"
    "v_fma_f32 v27, v27, v18, v18\n"
    "v_rcp_f32 v24, v24\n"
    "v_rcp_f32 v25, v25\n"
    "v_mfma_f32_32x32x16_bf16 v[96:111], a[28:31], v[156:159], v[96:111]\n"
    "buffer_load_dwordx4 v[216:219], v0, s[24:27], s42 offen offset:2048 sc1\n"
    "v_rcp_f32 v26, v26\n"
    "v_rcp_f32 v27, v27\n"
    "s_waitcnt vmcnt(14)\n"
    "v_mfma_f32_32x32x16_bf16 v[64:79], a[136:139], v[160:163], 0\n"
    "v_mul_f32 v84, v84, v24\n"
    "v_mul_f32 v85, v85, v25\n"
    "v_mul_f32 v86, v86, v26\n"
    "v_mul_f32 v87, v87, v27\n"
    "v_cvt_pk_bf16_f32 v34, v84, v85\n"
    "v_cvt_pk_bf16_f32 v35, v86, v87\n"
    "v_mfma_f32_32x32x16_bf16 v[40:55], a[48:51], v[160:163], 0\n"
    "buffer_load_dwordx4 v[220:223], v0, s[24:27], s42 offen offset:3072 sc1\n"
    "s_add_u32 s44, s61, 0x5000\n"
    "buffer_store_dwordx4 v[32:35], v0, s[24:27], s44 offen sc1\n"
    "v_accvgpr_read_b32 v82, a248\n"
    "v_accvgpr_read_b32 v86, a249\n"
    "v_mfma_f32_32x32x16_bf16 v[64:79], a[140:143], v[164:167], v[64:79]\n"
    "v_accvgpr_read_b32 v90, a250\n"
    "v_accvgpr_read_b32 v94, a251\n"
    "v_accvgpr_read_b32 v83, a252\n"
    "v_accvgpr_read_b32 v87, a253\n"
    "v_accvgpr_read_b32 v91, a254\n"
    "v_accvgpr_read_b32 v95, a255\n"
    "v_lshlrev_b32 v80, 16, v82\n"
    "v_mfma_f32_32x32x16_bf16 v[40:55], a[52:55], v[164:167], v[40:55]\n"
    "buffer_load_dwordx4 v[224:227], v0, s[24:27], s63 offen sc1\n"
    "v_and_b32 v82, 0xffff0000, v82\n"
    "v_lshlrev_b32 v84, 16, v86\n"
    "v_and_b32 v86, 0xffff0000, v86\n"
    "v_lshlrev_b32 v88, 16, v90\n"
    "s_waitcnt vmcnt(15)\n"
    "v_mfma_f32_32x32x16_bf16 v[64:79], a[144:147], v[168:171], v[64:79]\n"
    "v_and_b32 v90, 0xffff0000, v90\n"
    "v_lshlrev_b32 v92, 16, v94\n"
    "v_and_b32 v94, 0xffff0000, v94\n"
    "v_lshlrev_b32 v81, 16, v83\n"
    "v_and_b32 v83, 0xffff0000, v83\n"
    "v_lshlrev_b32 v85, 16, v87\n"
    "v_and_b32 v87, 0xffff0000, v87\n"
    "v_mfma_f32_32x32x16_bf16 v[40:55], a[56:59], v[168:171], v[40:55]\n"
    "buffer_load_dwordx4 v[228:231], v0, s[24:27], s63 offen offset:1024 sc1\n"
    "v_lshlrev_b32 v89, 16, v91\n"
    "v_and_b32 v91, 0xffff0000, v91\n"
    "v_lshlrev_b32 v93, 16, v95\n"
    "v_and_b32 v95, 0xffff0000, v95\n"
    "v_mfma_f32_32x32x16_bf16 v[64:79], a[148:151], v[172:175], v[64:79]\n"
    "v_mfma_f32_32x32x16_bf16 v[40:55], a[60:63], v[172:175], v[40:55]\n"
    "buffer_load_dwordx4 v[232:235], v0, s[24:27], s63 offen offset:2048 sc1\n"
    "s_waitcnt vmcnt(14)\n"
    "v_mfma_f32_32x32x16_bf16 v[64:79], a[104:107], v[176:179], v[64:79]\n"
    "v_mfma_f32_32x32x16_bf16 v[40:55], a[16:19], v[176:179], v[40:55]\n"
    "buffer_load_dwordx4 v[236:239], v0, s[24:27], s63 offen offset:3072 sc1\n"
    "v_mfma_f32_32x32x16_bf16 v[64:79], a[108:111], v[180:183], v[64:79]\n"
    "v_mfma_f32_32x32x16_bf16 v[40:55], a[20:23], v[180:183], v[40:55]\n"
    "buffer_load_dwordx4 v[240:243], v0, s[24:27], s62 offen sc1\n"
    "s_waitcnt vmcnt(14)\n"
    "v_mfma_f32_32x32x16_bf16 v[64:79], a[112:115], v[184:187], v[64:79]\n"
    "v_mfma_f32_32x32x16_bf16 v[40:55], a[24:27], v[184:187], v[40:55]\n"
    "buffer_load_dwordx4 v[244:247], v0, s[24:27], s62 offen offset:1024 sc1\n"
    "s_waitcnt vmcnt(14)\n"
    "v_max3_u32 v15, v192, v193, v194\n"
    "v_mfma_f32_32x32x16_bf16 v[64:79], a[116:119], v[188:191], v[64:79]\n"
    "s_waitcnt vmcnt(13)\n"
    "v_max3_u32 v15, v15, v195, v196\n"
    "v_max3_u32 v15, v15, v197, v198\n"
    "v_max_u32 v15, v15, v199\n"
    "v_cmp_eq_u32 vcc, -1, v15\n"
    "v_mfma_f32_32x32x16_bf16 v[40:55], a[28:31], v[188:191], v[40:55]\n"
    "buffer_load_dwordx4 v[248:251], v0, s[24:27], s62 offen offset:2048 sc1\n"
    "s_cbranch_vccnz Lre359_%=\n"
    "Lok360_%=:\n"
    "v_mfma_f32_32x32x16_bf16 v[112:127], a[152:155], v[192:195], v[112:127]\n"
    "v_mfma_f32_32x32x16_bf16 v[96:111], a[64:67], v[192:195], v[96:111]\n"
    "buffer_load_dwordx4 v[252:255], v0, s[24:27], s62 offen offset:3072 sc1\n"
    "s_waitcnt vmcnt(14)\n"
    "v_max3_u32 v15, v200, v201, v202\n"
    "v_mfma_f32_32x32x16_bf16 v[112:127], a[156:159], v[196:199], v[112:127]\n"
    "s_waitcnt vmcnt(13)\n"
    "v_max3_u32 v15, v15, v203, v204\n"
    "v_max3_u32 v15, v15, v205, v206\n"
    "v_max_u32 v15, v15, v207\n"
    "v_cmp_eq_u32 vcc, -1, v15\n"
    "v_mfma_f32_32x32x16_bf16 v[96:111], a[68:71], v[196:199], v[96:111]\n"
    "s_add_u32 s42, s63, 0x2000\n"
    "buffer_load_dwordx4 v[128:131], v0, s[24:27], s42 offen sc1\n"
    "s_cbranch_vccnz Lre361_%=\n"
    "Lok362_%=:\n"
    "v_mfma_f32_32x32x16_bf16 v[112:127], a[160:163], v[200:203], v[112:127]\n"
    "v_mfma_f32_32x32x16_bf16 v[96:111], a[72:75], v[200:203], v[96:111]\n"
    "buffer_load_dwordx4 v[132:135], v0, s[24:27], s42 offen offset:1024 sc1\n"
    "s_waitcnt vmcnt(14)\n"
    "v_max3_u32 v15, v208, v209, v210\n"
    "v_mfma_f32_32x32x16_bf16 v[112:127], a[164:167], v[204:207], v[112:127]\n"
    "s_waitcnt vmcnt(13)\n"
    "v_max3_u32 v15, v15, v211, v212\n"
    "v_max3_u32 v15, v15, v213, v214\n"
    "v_max_u32 v15, v15, v215\n"
    "v_cmp_eq_u32 vcc, -1, v15\n"
    "v_mfma_f32_32x32x16_bf16 v[96:111], a[76:79], v[204:207], v[96:111]\n"
    "buffer_load_dwordx4 v[136:139], v0, s[24:27], s42 offen offset:2048 sc1\n"
    "s_cbranch_vccnz Lre363_%=\n"
    "Lok364_%=:\n"
    "v_mfma_f32_32x32x16_bf16 v[64:79], a[152:155], v[208:211], v[64:79]\n"
    "v_mfma_f32_32x32x16_bf16 v[40:55], a[64:67], v[208:211], v[40:55]\n"
    "buffer_load_dwordx4 v[140:143], v0, s[24:27], s42 offen offset:3072 sc1\n"
    "s_waitcnt vmcnt(14)\n"
    "v_max3_u32 v15, v216, v217, v218\n"
    "v_mfma_f32_32x32x16_bf16 v[64:79], a[156:159], v[212:215], v[64:79]\n"
    "s_waitcnt vmcnt(13)\n"
    "v_max3_u32 v15, v15, v219, v220\n"
    "v_max3_u32 v15, v15, v221, v222\n"
    "v_max_u32 v15, v15, v223\n"
    "v_cmp_eq_u32 vcc, -1, v15\n"
    "v_mfma_f32_32x32x16_bf16 v[40:55], a[68:71], v[212:215], v[40:55]\n"
    "s_cbranch_vccnz Lre365_%=\n"
    "Lok366_%=:\n"
    "v_mfma_f32_32x32x16_bf16 v[64:79], a[160:163], v[216:219], v[64:79]\n"
    "v_mfma_f32_32x32x16_bf16 v[40:55], a[72:75], v[216:219], v[40:55]\n"
    "s_waitcnt vmcnt(11)\n"
    "v_max3_u32 v15, v224, v225, v226\n"
    "v_mfma_f32_32x32x16_bf16 v[64:79], a[164:167], v[220:223], v[64:79]\n"
    "s_waitcnt vmcnt(10)\n"
    "v_max3_u32 v15, v15, v227, v228\n"
    "v_max3_u32 v15, v15, v229, v230\n"
    "v_max_u32 v15, v15, v231\n"
    "v_cmp_eq_u32 vcc, -1, v15\n"
    "v_mfma_f32_32x32x16_bf16 v[40:55], a[76:79], v[220:223], v[40:55]\n"
    "s_cbranch_vccnz Lre367_%=\n"
    "Lok368_%=:\n"
    "v_mfma_f32_32x32x16_bf16 v[112:127], a[88:91], v[224:227], v[112:127]\n"
    "v_mfma_f32_32x32x16_bf16 v[96:111], a[0:3], v[224:227], v[96:111]\n"
    "s_add_u32 s42, s63, 0x5000\n"
    "buffer_load_dwordx4 v[144:147], v0, s[24:27], s42 offen sc1\n"
    "s_waitcnt vmcnt(10)\n"
    "v_max3_u32 v15, v232, v233, v234\n"
    "v_mfma_f32_32x32x16_bf16 v[112:127], a[92:95], v[228:231], v[112:127]\n"
    "s_waitcnt vmcnt(9)\n"
    "v_max3_u32 v15, v15, v235, v236\n"
    "v_max3_u32 v15, v15, v237, v238\n"
    "v_max_u32 v15, v15, v239\n"
    "v_cmp_eq_u32 vcc, -1, v15\n"
    "v_mfma_f32_32x32x16_bf16 v[96:111], a[4:7], v[228:231], v[96:111]\n"
    "buffer_load_dwordx4 v[148:151], v0, s[24:27], s65 offen sc1\n"
    "s_cbranch_vccnz Lre369_%=\n"
    "Lok370_%=:\n"
    "v_mfma_f32_32x32x16_bf16 v[112:127], a[96:99], v[232:235], v[112:127]\n"
    "v_mfma_f32_32x32x16_bf16 v[96:111], a[8:11], v[232:235], v[96:111]\n"
    "s_add_u32 s42, s62, 0x2000\n"
    "buffer_load_dwordx4 v[152:155], v0, s[24:27], s42 offen sc1\n"
    "s_waitcnt vmcnt(10)\n"
    "v_max3_u32 v15, v240, v241, v242\n"
    "v_mfma_f32_32x32x16_bf16 v[112:127], a[100:103], v[236:239], v[112:127]\n"
    "s_waitcnt vmcnt(9)\n"
    "v_max3_u32 v15, v15, v243, v244\n"
    "v_max3_u32 v15, v15, v245, v246\n"
    "v_max_u32 v15, v15, v247\n"
    "v_cmp_eq_u32 vcc, -1, v15\n"
    "v_mfma_f32_32x32x16_bf16 v[96:111], a[12:15], v[236:239], v[96:111]\n"
    "buffer_load_dwordx4 v[156:159], v0, s[24:27], s42 offen offset:1024 sc1\n"
    "s_cbranch_vccnz Lre371_%=\n"
    "Lok372_%=:\n"
    "v_mfma_f32_32x32x16_bf16 v[64:79], a[88:91], v[240:243], v[64:79]\n"
    "v_mfma_f32_32x32x16_bf16 v[40:55], a[0:3], v[240:243], v[40:55]\n"
    "buffer_load_dwordx4 v[160:163], v0, s[24:27], s42 offen offset:2048 sc1\n"
    "s_waitcnt vmcnt(10)\n"
    "v_max3_u32 v15, v248, v249, v250\n"
    "v_mfma_f32_32x32x16_bf16 v[64:79], a[92:95], v[244:247], v[64:79]\n"
    "s_waitcnt vmcnt(9)\n"
    "v_max3_u32 v15, v15, v251, v252\n"
    "v_max3_u32 v15, v15, v253, v254\n"
    "v_max_u32 v15, v15, v255\n"
    "v_cmp_eq_u32 vcc, -1, v15\n"
    "v_mfma_f32_32x32x16_bf16 v[40:55], a[4:7], v[244:247], v[40:55]\n"
    "buffer_load_dwordx4 v[164:167], v0, s[24:27], s42 offen offset:3072 sc1\n"
    "s_cbranch_vccnz Lre373_%=\n"
    "Lok374_%=:\n"
    "v_mfma_f32_32x32x16_bf16 v[64:79], a[96:99], v[248:251], v[64:79]\n"
    "v_mfma_f32_32x32x16_bf16 v[40:55], a[8:11], v[248:251], v[40:55]\n"
    "s_add_u32 s42, s62, 0x5000\n"
    "buffer_load_dwordx4 v[168:171], v0, s[24:27], s42 offen sc1\n"
    "s_waitcnt vmcnt(10)\n"
    "v_max3_u32 v15, v128, v129, v130\n"
    "v_mfma_f32_32x32x16_bf16 v[64:79], a[100:103], v[252:255], v[64:79]\n"
    "s_waitcnt vmcnt(9)\n"
    "v_max3_u32 v15, v15, v131, v132\n"
    "v_max3_u32 v15, v15, v133, v134\n"
    "v_max_u32 v15, v15, v135\n"
    "v_cmp_eq_u32 vcc, -1, v15\n"
    "v_mfma_f32_32x32x16_bf16 v[40:55], a[12:15], v[252:255], v[40:55]\n"
    "buffer_load_dwordx4 v[172:175], v0, s[24:27], s64 offen sc1\n"
    "s_cbranch_vccnz Lre375_%=\n"
    "Lok376_%=:\n"
    "v_mfma_f32_32x32x16_bf16 v[112:127], a[120:123], v[128:131], v[112:127]\n"
    "v_mfma_f32_32x32x16_bf16 v[96:111], a[32:35], v[128:131], v[96:111]\n"
    "s_waitcnt vmcnt(9)\n"
    "v_max3_u32 v15, v136, v137, v138\n"
    "v_mfma_f32_32x32x16_bf16 v[112:127], a[124:127], v[132:135], v[112:127]\n"
    "s_waitcnt vmcnt(8)\n"
    "v_max3_u32 v15, v15, v139, v140\n"
    "v_max3_u32 v15, v15, v141, v142\n"
    "v_max_u32 v15, v15, v143\n"
    "v_cmp_eq_u32 vcc, -1, v15\n"
    "v_mfma_f32_32x32x16_bf16 v[96:111], a[36:39], v[132:135], v[96:111]\n"
    "s_cbranch_vccnz Lre377_%=\n"
    "Lok378_%=:\n"
    "v_mfma_f32_32x32x16_bf16 v[112:127], a[128:131], v[136:139], v[112:127]\n"
    "v_add_u32 v39, s81, v0\n"
    "v_add_u32 v38, s80, v0\n"
    "v_add_u32 v37, s79, v0\n"
    "v_mfma_f32_32x32x16_bf16 v[96:111], a[40:43], v[136:139], v[96:111]\n"
    "s_waitcnt vmcnt(7)\n"
    "v_max3_u32 v15, v144, v145, v146\n"
    "v_mfma_f32_32x32x16_bf16 v[112:127], a[132:135], v[140:143], v[112:127]\n"
    "s_waitcnt vmcnt(6)\n"
    "v_max3_u32 v15, v15, v147, v148\n"
    "v_max3_u32 v15, v15, v149, v150\n"
    "v_max_u32 v15, v15, v151\n"
    "v_cmp_eq_u32 vcc, -1, v15\n"
    "v_mfma_f32_32x32x16_bf16 v[96:111], a[44:47], v[140:143], v[96:111]\n"
    "s_cbranch_vccnz Lre379_%=\n"
    "Lok380_%=:\n"
    "v_mfma_f32_32x32x16_bf16 v[112:127], a[168:171], v[144:147], v[112:127]\n"
    "v_mfma_f32_32x32x16_bf16 v[96:111], a[80:83], v[144:147], v[96:111]\n"
    "s_waitcnt vmcnt(5)\n"
    "v_max3_u32 v15, v152, v153, v154\n"
    "v_mfma_f32_32x32x16_bf16 v[112:127], a[172:175], v[148:151], v[112:127]\n"
    "s_waitcnt vmcnt(4)\n"
    "v_max3_u32 v15, v15, v155, v156\n"
    "v_max3_u32 v15, v15, v157, v158\n"
    "v_max_u32 v15, v15, v159\n"
    "v_cmp_eq_u32 vcc, -1, v15\n"
    "v_mfma_f32_32x32x16_bf16 v[96:111], a[84:87], v[148:151], v[96:111]\n"
    "s_cbranch_vccnz Lre381_%=\n"
    "Lok382_%=:\n"
    "v_mfma_f32_32x32x16_bf16 v[64:79], a[120:123], v[152:155], v[64:79]\n"
    "v_mfma_f32_32x32x16_bf16 v[40:55], a[32:35], v[152:155], v[40:55]\n"
    "s_waitcnt vmcnt(3)\n"
    "v_max3_u32 v15, v160, v161, v162\n"
    "s_nop 0\n"
    "ds_write_b128 v39, v[112:115]\n"
    "v_mfma_f32_32x32x16_bf16 v[64:79], a[124:127], v[156:159], v[64:79]\n"
    "s_waitcnt vmcnt(2)\n"
    "v_max3_u32 v15, v15, v163, v164\n"
    "v_max3_u32 v15, v15, v165, v166\n"
    "v_max_u32 v15, v15, v167\n"
    "v_cmp_eq_u32 vcc, -1, v15\n"
    "ds_write_b128 v39, v[116:119] offset:1024\n"
    "v_mfma_f32_32x32x16_bf16 v[40:55], a[36:39], v[156:159], v[40:55]\n"
    "s_cbranch_vccnz Lre383_%=\n"
    "Lok384_%=:\n"
    "v_mfma_f32_32x32x16_bf16 v[64:79], a[128:131], v[160:163], v[64:79]\n"
    "ds_write_b128 v39, v[120:123] offset:2048\n"
    "v_mfma_f32_32x32x16_bf16 v[40:55], a[40:43], v[160:163], v[40:55]\n"
    "s_waitcnt vmcnt(1)\n"
    "v_max3_u32 v15, v168, v169, v170\n"
    "ds_write_b128 v39, v[124:127] offset:3072\n"
    "v_mfma_f32_32x32x16_bf16 v[64:79], a[132:135], v[164:167], v[64:79]\n"
    "s_waitcnt vmcnt(0)\n"
    "v_max3_u32 v15, v15, v171, v172\n"
    "v_max3_u32 v15, v15, v173, v174\n"
    "v_max_u32 v15, v15, v175\n"
    "v_cmp_eq_u32 vcc, -1, v15\n"
    "v_mfma_f32_32x32x16_bf16 v[40:55], a[44:47], v[164:167], v[40:55]\n"
    "s_cbranch_vccnz Lre385_%=\n"
    "Lok386_%=:\n"
    "ds_write_b128 v38, v[96:99] offset:32768\n"
    "v_mfma_f32_32x32x16_bf16 v[64:79], a[168:171], v[168:171], v[64:79]\n"
    "ds_write_b128 v38, v[100:103] offset:33792\n"
    "v_mfma_f32_32x32x16_bf16 v[40:55], a[80:83], v[168:171], v[40:55]\n"
    "v_mfma_f32_32x32x16_bf16 v[64:79], a[172:175], v[172:175], v[64:79]\n"
    "ds_write_b128 v38, v[104:107] offset:34816\n"
    "v_mfma_f32_32x32x16_bf16 v[40:55], a[84:87], v[172:175], v[40:55]\n"
    "ds_write_b128 v38, v[108:111] offset:35840\n"
    "s_lshr_b32 s42, s37, 1\n"
    "s_mul_i32 s42, s42, 0x1200\n"
    "s_and_b32 s43, s37, 1\n"
    "s_lshl_b32 s44, s43, 6\n"
    "s_add_u32 s45, s42, s44\n"
    "s_sub_u32 s22, s22, s44\n"
    "s_subb_u32 s23, s23, 0\n"
    "s_mul_i32 s44, s43, 0x900\n"
    "s_add_u32 s42, s42, s44\n"
    "s_lshl_b32 s43, s43, 14\n"
    "v_bfe_u32 v16, v0, 4, 5\n"
    "v_mul_u32_u24 v16, 144, v16\n"
    "v_lshrrev_b32 v17, 9, v0\n"
    "v_lshl_add_u32 v16, v17, 5, v16\n"
    "v_add_u32 v16, s45, v16\n"
    "v_lshrrev_b32 v15, 7, v0\n"
    "v_and_b32 v18, 0x70, v0\n"
    "v_mul_u32_u24 v17, 144, v15\n"
    "v_add3_u32 v17, v17, v18, s42\n"
    "v_lshl_add_u32 v15, v15, 10, v18\n"
    "v_add_u32 v15, s43, v15\n"
    "v_add_u32 v18, 0x2000, v15\n"
    "ds_write_b128 v37, v[64:67] offset:32768\n"
    "ds_write_b128 v37, v[68:71] offset:33792\n"
    "ds_write_b128 v37, v[72:75] offset:34816\n"
    "ds_write_b128 v37, v[76:79] offset:35840\n"
    "v_pk_add_f32 v[40:41], v[40:41], v[80:81]\n"
    "v_pk_add_f32 v[42:43], v[42:43], v[82:83]\n"
    "v_pk_add_f32 v[44:45], v[44:45], v[84:85]\n"
    "v_pk_add_f32 v[46:47], v[46:47], v[86:87]\n"
    "v_pk_add_f32 v[48:49], v[48:49], v[88:89]\n"
    "v_pk_add_f32 v[50:51], v[50:51], v[90:91]\n"
    "v_pk_add_f32 v[52:53], v[52:53], v[92:93]\n"
    "v_pk_add_f32 v[54:55], v[54:55], v[94:95]\n"
    "v_add_u32 v32, s82, v0\n"
    "v_add_u32 v33, s83, v0\n"
    "s_waitcnt lgkmcnt(0)\n"
    "s_barrier\n"
    "ds_read_b128 v[64:67], v32 offset:32768\n"
    "ds_read_b128 v[96:99], v32 offset:36864\n"
    "ds_read_b128 v[112:115], v33\n"
    "ds_read_b128 v[68:71], v32 offset:33792\n"
    "ds_read_b128 v[100:103], v32 offset:37888\n"
    "ds_read_b128 v[116:119], v33 offset:1024\n"
    "ds_read_b128 v[72:75], v32 offset:34816\n"
    "ds_read_b128 v[104:107], v32 offset:38912\n"
    "ds_read_b128 v[120:123], v33 offset:2048\n"
    "ds_read_b128 v[76:79], v32 offset:35840\n"
    "ds_read_b128 v[108:111], v32 offset:39936\n"
    "ds_read_b128 v[124:127], v33 offset:3072\n"
    "s_waitcnt lgkmcnt(9)\n"
    "v_pk_add_f32 v[40:41], v[40:41], v[64:65]\n"
    "v_pk_add_f32 v[42:43], v[42:43], v[66:67]\n"
    "v_pk_add_f32 v[40:41], v[40:41], v[96:97]\n"
    "v_pk_add_f32 v[42:43], v[42:43], v[98:99]\n"
    "v_pk_add_f32 v[40:41], v[40:41], v[112:113]\n"
    "v_pk_add_f32 v[42:43], v[42:43], v[114:115]\n"
    "v_cvt_pk_bf16_f32 v24, v40, v41\n"
    "v_cvt_pk_bf16_f32 v25, v42, v43\n"
    "s_waitcnt lgkmcnt(6)\n"
    "v_pk_add_f32 v[44:45], v[44:45], v[68:69]\n"
    "v_pk_add_f32 v[46:47], v[46:47], v[70:71]\n"
    "v_pk_add_f32 v[44:45], v[44:45], v[100:101]\n"
    "v_pk_add_f32 v[46:47], v[46:47], v[102:103]\n"
    "v_pk_add_f32 v[44:45], v[44:45], v[116:117]\n"
    "v_pk_add_f32 v[46:47], v[46:47], v[118:119]\n"
    "v_cvt_pk_bf16_f32 v26, v44, v45\n"
    "v_cvt_pk_bf16_f32 v27, v46, v47\n"
    "ds_write_b128 v16, v[24:27]\n"
    "s_waitcnt lgkmcnt(4)\n"
    "v_pk_add_f32 v[48:49], v[48:49], v[72:73]\n"
    "v_pk_add_f32 v[50:51], v[50:51], v[74:75]\n"
    "v_pk_add_f32 v[48:49], v[48:49], v[104:105]\n"
    "v_pk_add_f32 v[50:51], v[50:51], v[106:107]\n"
    "v_pk_add_f32 v[48:49], v[48:49], v[120:121]\n"
    "v_pk_add_f32 v[50:51], v[50:51], v[122:123]\n"
    "v_cvt_pk_bf16_f32 v28, v48, v49\n"
    "v_cvt_pk_bf16_f32 v29, v50, v51\n"
    "s_waitcnt lgkmcnt(1)\n"
    "v_pk_add_f32 v[52:53], v[52:53], v[76:77]\n"
    "v_pk_add_f32 v[54:55], v[54:55], v[78:79]\n"
    "v_pk_add_f32 v[52:53], v[52:53], v[108:109]\n"
    "v_pk_add_f32 v[54:55], v[54:55], v[110:111]\n"
    "v_pk_add_f32 v[52:53], v[52:53], v[124:125]\n"
    "v_pk_add_f32 v[54:55], v[54:55], v[126:127]\n"
    "v_cvt_pk_bf16_f32 v30, v52, v53\n"
    "v_cvt_pk_bf16_f32 v31, v54, v55\n"
    "ds_write_b128 v16, v[28:31] offset:16\n"
    "s_waitcnt lgkmcnt(0)\n"
    "s_barrier\n"
    "ds_read_b128 v[24:27], v17\n"
    "ds_read_b128 v[28:31], v17 offset:1152\n"
    "s_waitcnt lgkmcnt(1)\n"
    "global_store_dwordx4 v15, v[24:27], s[22:23] sc1\n"
    "s_waitcnt lgkmcnt(0)\n"
    "global_store_dwordx4 v18, v[28:31], s[22:23] sc1\n"
    "s_or_b32 s42, s36, s37\n"
    "s_cbranch_scc1 Lfail_%=\n"
    "v_mov_b32 v34, s32\n"
    "v_mov_b32 v35, s33\n"
    "s_mov_b64 exec, 1\n"
    "buffer_store_dwordx2 v[34:35], off, s[24:27], 0 sc1\n"
    "Lfail_%=:\n"
    "s_endpgm\n"
    "Lp111_%=:\n"
    "s_sleep 4\n"
    "s_cmp_eq_u32 s37, 1\n"
    "s_cselect_b32 s44, s88, s90\n"
    "s_cmp_eq_u32 s37, 0\n"
    "s_cselect_b32 s44, s89, s44\n"
    "s_cselect_b32 s49, 7, 3\n"
    "v_and_b32 v34, 0xf0, v0\n"
    "v_lshlrev_b32 v34, s49, v34\n"
    "s_add_u32 s44, s44, 0x10000\n"
    "buffer_load_dwordx2 v[34:35], v34, s[24:27], s44 offen sc1\n"
    "s_sub_u32 s50, s50, 1\n"
    "s_cbranch_scc1 Lfail_%=\n"
    "s_waitcnt vmcnt(0)\n"
    "v_cmp_ne_u64 vcc, s[32:33], v[34:35]\n"
    "s_nop 1\n"
    "s_cbranch_vccnz Lp111_%=\n"
    "s_branch Lo112_%=\n"
    "Lrd17_%=:\n"
    "v_mov_b32 v46, 0\n"
    "v_mov_b32 v47, 0\n"
    "s_mov_b64 exec, s[16:17]\n"
    "buffer_atomic_add_x2 v[46:47], off, s[24:27], s91 offset:64 sc0 sc1\n"
    "s_mov_b64 exec, -1\n"
    "s_sub_u32 s50, s50, 1\n"
    "s_cbranch_scc1 Lfail_%=\n"
    "s_add_u32 s44, s20, 12\n"
    "s_waitcnt vmcnt(0)\n"
    "v_cmp_ne_u32 vcc, s44, v46\n"
    "s_nop 0\n"
    "s_and_b64 vcc, vcc, s[16:17]\n"
    "s_cbranch_scc1 Lrd17_%=\n"
    "s_branch Lrk18_%=\n"
    "Lrep19_%=:\n"
    "s_sleep 4\n"
    "v_mov_b32 v44, 1\n"
    "v_mov_b32 v45, 0\n"
    "s_mov_b64 exec, s[14:15]\n"
    "buffer_atomic_add_x2 v[44:45], v18, s[24:27], s21 offen sc0 sc1\n"
    "s_mov_b64 exec, -1\n"
    "s_sub_u32 s50, s50, 1\n"
    "s_cbranch_scc1 Lfail_%=\n"
    "s_waitcnt vmcnt(0)\n"
    "v_and_b32 v44, 0xffffff00, v44\n"
    "v_xor_b32 v44, s20, v44\n"
    "v_xor_b32 v45, s33, v45\n"
    "v_or_b32 v44, v44, v45\n"
    "v_cmp_ne_u32 vcc, 0, v44\n"
    "s_nop 0\n"
    "s_and_b64 s[14:15], s[14:15], vcc\n"
    "s_cbranch_scc1 Lrep19_%=\n"
    "s_branch Lvok20_%=\n"
    "Lrd23_%=:\n"
    "v_mov_b32 v46, 0\n"
    "v_mov_b32 v47, 0\n"
    "s_mov_b64 exec, s[16:17]\n"
    "buffer_atomic_add_x2 v[46:47], off, s[24:27], s91 offset:64 sc0 sc1\n"
    "s_mov_b64 exec, -1\n"
    "s_sub_u32 s50, s50, 1\n"
    "s_cbranch_scc1 Lfail_%=\n"
    "s_add_u32 s44, s20, 12\n"
    "s_waitcnt vmcnt(0)\n"
    "v_cmp_ne_u32 vcc, s44, v46\n"
    "s_nop 0\n"
    "s_and_b64 vcc, vcc, s[16:17]\n"
    "s_cbranch_scc1 Lrd23_%=\n"
    "s_branch Lrk24_%=\n"
    "Lrep25_%=:\n"
    "s_sleep 4\n"
    "v_mov_b32 v32, 1\n"
    "v_mov_b32 v33, 0\n"
    "s_mov_b64 exec, s[14:15]\n"
    "buffer_atomic_add_x2 v[32:33], v18, s[24:27], s21 offen sc0 sc1\n"
    "s_mov_b64 exec, -1\n"
    "s_sub_u32 s50, s50, 1\n"
    "s_cbranch_scc1 Lfail_%=\n"
    "s_waitcnt vmcnt(0)\n"
    "v_and_b32 v32, 0xffffff00, v32\n"
    "v_xor_b32 v32, s20, v32\n"
    "v_xor_b32 v33, s33, v33\n"
    "v_or_b32 v32, v32, v33\n"
    "v_cmp_ne_u32 vcc, 0, v32\n"
    "s_nop 0\n"
    "s_and_b64 s[14:15], s[14:15], vcc\n"
    "s_cbranch_scc1 Lrep25_%=\n"
    "s_branch Lvok26_%=\n"
    "Lrd28_%=:\n"
    "v_mov_b32 v24, 0\n"
    "v_mov_b32 v25, 0\n"
    "s_mov_b64 exec, s[16:17]\n"
    "buffer_atomic_add_x2 v[24:25], off, s[24:27], s91 offset:72 sc0 sc1\n"
    "s_mov_b64 exec, -1\n"
    "s_sub_u32 s50, s50, 1\n"
    "s_cbranch_scc1 Lfail_%=\n"
    "s_add_u32 s44, s20, s48\n"
    "s_waitcnt vmcnt(0)\n"
    "v_cmp_ne_u32 vcc, s44, v24\n"
    "s_nop 0\n"
    "s_and_b64 vcc, vcc, s[16:17]\n"
    "s_cbranch_scc1 Lrd28_%=\n"
    "s_branch Lrk29_%=\n"
    "Lrd30_%=:\n"
    "v_mov_b32 v26, 0\n"
    "v_mov_b32 v27, 0\n"
    "s_mov_b64 exec, s[16:17]\n"
    "buffer_atomic_add_x2 v[26:27], off, s[24:27], s92 offset:64 sc0 sc1\n"
    "s_mov_b64 exec, -1\n"
    "s_sub_u32 s50, s50, 1\n"
    "s_cbranch_scc1 Lfail_%=\n"
    "s_add_u32 s44, s20, 30\n"
    "s_waitcnt vmcnt(0)\n"
    "v_cmp_ne_u32 vcc, s44, v26\n"
    "s_nop 0\n"
    "s_and_b64 vcc, vcc, s[16:17]\n"
    "s_cbranch_scc1 Lrd30_%=\n"
    "s_branch Lrk31_%=\n"
    "Le36_%=:\n"
    "v_max3_u32 v15, v131, v129, v130\n"
    "v_max_u32 v15, v15, v128\n"
    "v_cmp_eq_u32 vcc, -1, v15\n"
    "s_cbranch_vccz Ls46_%=\n"
    "s_add_u32 s45, s56, s74\n"
    "buffer_load_dwordx4 v[128:131], v168, s[24:27], s45 offen sc1\n"
    "s_mov_b32 s49, 1\n"
    "Ls46_%=:\n"
    "s_sub_u32 s48, s48, 1\n"
    "s_cbranch_scc1 Ld45_%=\n"
    "Le37_%=:\n"
    "v_max3_u32 v15, v135, v133, v134\n"
    "v_max_u32 v15, v15, v132\n"
    "v_cmp_eq_u32 vcc, -1, v15\n"
    "s_cbranch_vccz Ls47_%=\n"
    "s_add_u32 s45, s56, s74\n"
    "buffer_load_dwordx4 v[132:135], v168, s[24:27], s45 offen offset:256 sc1\n"
    "s_mov_b32 s49, 1\n"
    "Ls47_%=:\n"
    "s_sub_u32 s48, s48, 1\n"
    "s_cbranch_scc1 Ld45_%=\n"
    "Le38_%=:\n"
    "v_max3_u32 v15, v139, v137, v138\n"
    "v_max_u32 v15, v15, v136\n"
    "v_cmp_eq_u32 vcc, -1, v15\n"
    "s_cbranch_vccz Ls48_%=\n"
    "s_add_u32 s45, s56, s75\n"
    "buffer_load_dwordx4 v[136:139], v168, s[24:27], s45 offen sc1\n"
    "s_mov_b32 s49, 1\n"
    "Ls48_%=:\n"
    "s_sub_u32 s48, s48, 1\n"
    "s_cbranch_scc1 Ld45_%=\n"
    "Le39_%=:\n"
    "v_max3_u32 v15, v143, v141, v142\n"
    "v_max_u32 v15, v15, v140\n"
    "v_cmp_eq_u32 vcc, -1, v15\n"
    "s_cbranch_vccz Ls49_%=\n"
    "s_add_u32 s45, s56, s75\n"
    "buffer_load_dwordx4 v[140:143], v168, s[24:27], s45 offen offset:256 sc1\n"
    "s_mov_b32 s49, 1\n"
    "Ls49_%=:\n"
    "s_sub_u32 s48, s48, 1\n"
    "s_cbranch_scc1 Ld45_%=\n"
    "Le40_%=:\n"
    "v_max3_u32 v15, v147, v145, v146\n"
    "v_max_u32 v15, v15, v144\n"
    "v_cmp_eq_u32 vcc, -1, v15\n"
    "s_cbranch_vccz Ls50_%=\n"
    "s_add_u32 s45, s56, s76\n"
    "buffer_load_dwordx4 v[144:147], v168, s[24:27], s45 offen sc1\n"
    "s_mov_b32 s49, 1\n"
    "Ls50_%=:\n"
    "s_sub_u32 s48, s48, 1\n"
    "s_cbranch_scc1 Ld45_%=\n"
    "Le41_%=:\n"
    "v_max3_u32 v15, v151, v149, v150\n"
    "v_max_u32 v15, v15, v148\n"
    "v_cmp_eq_u32 vcc, -1, v15\n"
    "s_cbranch_vccz Ls51_%=\n"
    "s_add_u32 s45, s56, s76\n"
    "buffer_load_dwordx4 v[148:151], v168, s[24:27], s45 offen offset:256 sc1\n"
    "s_mov_b32 s49, 1\n"
    "Ls51_%=:\n"
    "s_sub_u32 s48, s48, 1\n"
    "s_cbranch_scc1 Ld45_%=\n"
    "Le42_%=:\n"
    "v_max3_u32 v15, v155, v153, v154\n"
    "v_max_u32 v15, v15, v152\n"
    "v_cmp_eq_u32 vcc, -1, v15\n"
    "s_cbranch_vccz Ls52_%=\n"
    "s_add_u32 s45, s56, s77\n"
    "buffer_load_dwordx4 v[152:155], v168, s[24:27], s45 offen sc1\n"
    "s_mov_b32 s49, 1\n"
    "Ls52_%=:\n"
    "s_sub_u32 s48, s48, 1\n"
    "s_cbranch_scc1 Ld45_%=\n"
    "Le43_%=:\n"
    "v_max3_u32 v15, v159, v157, v158\n"
    "v_max_u32 v15, v15, v156\n"
    "v_cmp_eq_u32 vcc, -1, v15\n"
    "s_cbranch_vccz Ls53_%=\n"
    "s_add_u32 s45, s56, s77\n"
    "buffer_load_dwordx4 v[156:159], v168, s[24:27], s45 offen offset:256 sc1\n"
    "s_mov_b32 s49, 1\n"
    "Ls53_%=:\n"
    "s_sub_u32 s48, s48, 1\n"
    "s_cbranch_scc1 Ld45_%=\n"
    "Le44_%=:\n"
    "s_cmp_lg_u32 s49, 0\n"
    "s_cbranch_scc1 Ld45_%=\n"
    "v_max3_u32 v15, v195, v193, v194\n"
    "v_max_u32 v15, v15, v192\n"
    "v_cmp_eq_u32 vcc, -1, v15\n"
    "s_cbranch_vccz Ls54_%=\n"
    "s_add_u32 s45, s56, 0x2000\n"
    "buffer_load_dwordx4 v[192:195], v32, s[24:27], s45 offen sc1\n"
    "s_mov_b32 s49, 1\n"
    "Ls54_%=:\n"
    "Ld45_%=:\n"
    "s_sub_u32 s50, s50, s49\n"
    "s_cbranch_scc1 Lfail_%=\n"
    "s_cmp_lg_u32 s49, 0\n"
    "s_setpc_b64 s[34:35]\n"
    "Lre55_%=:\n"
    "s_waitcnt vmcnt(0)\n"
    "s_mov_b32 s49, 0\n"
    "s_mov_b32 s48, 8\n"
    "s_call_b64 s[34:35], Le36_%=\n"
    "s_cbranch_scc0 Lok56_%=\n"
    "Lag57_%=:\n"
    "s_waitcnt vmcnt(0)\n"
    "s_mov_b32 s49, 0\n"
    "s_mov_b32 s48, 8\n"
    "s_call_b64 s[34:35], Le36_%=\n"
    "s_cbranch_scc1 Lag57_%=\n"
    "s_branch Lkv35_%=\n"
    "Le61_%=:\n"
    "v_max3_u32 v15, v135, v129, v130\n"
    "v_max3_u32 v15, v15, v128, v131\n"
    "v_max3_u32 v15, v15, v132, v133\n"
    "v_max_u32 v15, v15, v134\n"
    "v_cmp_eq_u32 vcc, -1, v15\n"
    "s_cbranch_vccz Ls70_%=\n"
    "buffer_load_dwordx4 v[128:131], v0, s[24:27], s59 offen sc1\n"
    "s_add_u32 s45, s59, 0x1000\n"
    "buffer_load_dwordx4 v[132:135], v0, s[24:27], s45 offen sc1\n"
    "s_mov_b32 s49, 1\n"
    "Ls70_%=:\n"
    "s_sub_u32 s48, s48, 1\n"
    "s_cbranch_scc1 Ld69_%=\n"
    "Le62_%=:\n"
    "v_max3_u32 v15, v143, v137, v138\n"
    "v_max3_u32 v15, v15, v136, v139\n"
    "v_max3_u32 v15, v15, v140, v141\n"
    "v_max_u32 v15, v15, v142\n"
    "v_cmp_eq_u32 vcc, -1, v15\n"
    "s_cbranch_vccz Ls71_%=\n"
    "s_add_u32 s45, s59, 0x2000\n"
    "buffer_load_dwordx4 v[136:139], v0, s[24:27], s45 offen sc1\n"
    "s_add_u32 s45, s59, 0x3000\n"
    "buffer_load_dwordx4 v[140:143], v0, s[24:27], s45 offen sc1\n"
    "s_mov_b32 s49, 1\n"
    "Ls71_%=:\n"
    "s_sub_u32 s48, s48, 1\n"
    "s_cbranch_scc1 Ld69_%=\n"
    "Le63_%=:\n"
    "v_max3_u32 v15, v151, v145, v146\n"
    "v_max3_u32 v15, v15, v144, v147\n"
    "v_max3_u32 v15, v15, v148, v149\n"
    "v_max_u32 v15, v15, v150\n"
    "v_cmp_eq_u32 vcc, -1, v15\n"
    "s_cbranch_vccz Ls72_%=\n"
    "s_add_u32 s45, s59, 0x4000\n"
    "buffer_load_dwordx4 v[144:147], v0, s[24:27], s45 offen sc1\n"
    "s_add_u32 s45, s59, 0x5000\n"
    "buffer_load_dwordx4 v[148:151], v0, s[24:27], s45 offen sc1\n"
    "s_mov_b32 s49, 1\n"
    "Ls72_%=:\n"
    "s_sub_u32 s48, s48, 1\n"
    "s_cbranch_scc1 Ld69_%=\n"
    "Le64_%=:\n"
    "v_max3_u32 v15, v159, v153, v154\n"
    "v_max3_u32 v15, v15, v152, v155\n"
    "v_max3_u32 v15, v15, v156, v157\n"
    "v_max_u32 v15, v15, v158\n"
    "v_cmp_eq_u32 vcc, -1, v15\n"
    "s_cbranch_vccz Ls73_%=\n"
    "s_add_u32 s45, s59, 0x6000\n"
    "buffer_load_dwordx4 v[152:155], v0, s[24:27], s45 offen sc1\n"
    "s_add_u32 s45, s59, 0x7000\n"
    "buffer_load_dwordx4 v[156:159], v0, s[24:27], s45 offen sc1\n"
    "s_mov_b32 s49, 1\n"
    "Ls73_%=:\n"
    "s_sub_u32 s48, s48, 1\n"
    "s_cbranch_scc1 Ld69_%=\n"
    "Le65_%=:\n"
    "v_max3_u32 v15, v167, v161, v162\n"
    "v_max3_u32 v15, v15, v160, v163\n"
    "v_max3_u32 v15, v15, v164, v165\n"
    "v_max_u32 v15, v15, v166\n"
    "v_cmp_eq_u32 vcc, -1, v15\n"
    "s_cbranch_vccz Ls74_%=\n"
    "buffer_load_dwordx4 v[160:163], v0, s[24:27], s58 offen sc1\n"
    "s_add_u32 s45, s58, 0x1000\n"
    "buffer_load_dwordx4 v[164:167], v0, s[24:27], s45 offen sc1\n"
    "s_mov_b32 s49, 1\n"
    "Ls74_%=:\n"
    "s_sub_u32 s48, s48, 1\n"
    "s_cbranch_scc1 Ld69_%=\n"
    "Le66_%=:\n"
    "v_max3_u32 v15, v175, v169, v170\n"
    "v_max3_u32 v15, v15, v168, v171\n"
    "v_max3_u32 v15, v15, v172, v173\n"
    "v_max_u32 v15, v15, v174\n"
    "v_cmp_eq_u32 vcc, -1, v15\n"
    "s_cbranch_vccz Ls75_%=\n"
    "s_add_u32 s45, s58, 0x2000\n"
    "buffer_load_dwordx4 v[168:171], v0, s[24:27], s45 offen sc1\n"
    "s_add_u32 s45, s58, 0x3000\n"
    "buffer_load_dwordx4 v[172:175], v0, s[24:27], s45 offen sc1\n"
    "s_mov_b32 s49, 1\n"
    "Ls75_%=:\n"
    "s_sub_u32 s48, s48, 1\n"
    "s_cbranch_scc1 Ld69_%=\n"
    "Le67_%=:\n"
    "v_max3_u32 v15, v183, v177, v178\n"
    "v_max3_u32 v15, v15, v176, v179\n"
    "v_max3_u32 v15, v15, v180, v181\n"
    "v_max_u32 v15, v15, v182\n"
    "v_cmp_eq_u32 vcc, -1, v15\n"
    "s_cbranch_vccz Ls76_%=\n"
    "s_add_u32 s45, s58, 0x4000\n"
    "buffer_load_dwordx4 v[176:179], v0, s[24:27], s45 offen sc1\n"
    "s_add_u32 s45, s58, 0x5000\n"
    "buffer_load_dwordx4 v[180:183], v0, s[24:27], s45 offen sc1\n"
    "s_mov_b32 s49, 1\n"
    "Ls76_%=:\n"
    "s_sub_u32 s48, s48, 1\n"
    "s_cbranch_scc1 Ld69_%=\n"
    "Le68_%=:\n"
    "v_max3_u32 v15, v191, v185, v186\n"
    "v_max3_u32 v15, v15, v184, v187\n"
    "v_max3_u32 v15, v15, v188, v189\n"
    "v_max_u32 v15, v15, v190\n"
    "v_cmp_eq_u32 vcc, -1, v15\n"
    "s_cbranch_vccz Ls77_%=\n"
    "s_add_u32 s45, s58, 0x6000\n"
    "buffer_load_dwordx4 v[184:187], v0, s[24:27], s45 offen sc1\n"
    "s_add_u32 s45, s58, 0x7000\n"
    "buffer_load_dwordx4 v[188:191], v0, s[24:27], s45 offen sc1\n"
    "s_mov_b32 s49, 1\n"
    "Ls77_%=:\n"
    "Ld69_%=:\n"
    "s_sub_u32 s50, s50, s49\n"
    "s_cbranch_scc1 Lfail_%=\n"
    "s_cmp_lg_u32 s49, 0\n"
    "s_setpc_b64 s[34:35]\n"
    "Lre78_%=:\n"
    "s_waitcnt vmcnt(0)\n"
    "s_mov_b32 s49, 0\n"
    "s_mov_b32 s48, 5\n"
    "s_call_b64 s[34:35], Le61_%=\n"
    "s_cbranch_scc1 Lre78_%=\n"
    "s_branch Lok79_%=\n"
    "Lre80_%=:\n"
    "s_waitcnt vmcnt(0)\n"
    "s_mov_b32 s49, 0\n"
    "s_mov_b32 s48, 5\n"
    "s_call_b64 s[34:35], Le62_%=\n"
    "s_cbranch_scc1 Lre80_%=\n"
    "s_branch Lok81_%=\n"
    "Lre82_%=:\n"
    "s_waitcnt vmcnt(0)\n"
    "s_mov_b32 s49, 0\n"
    "s_mov_b32 s48, 5\n"
    "s_call_b64 s[34:35], Le63_%=\n"
    "s_cbranch_scc1 Lre82_%=\n"
    "s_branch Lok83_%=\n"
    "Lre84_%=:\n"
    "s_waitcnt vmcnt(0)\n"
    "s_mov_b32 s49, 0\n"
    "s_mov_b32 s48, 4\n"
    "s_call_b64 s[34:35], Le64_%=\n"
    "s_cbranch_scc1 Lre84_%=\n"
    "s_branch Lok85_%=\n"
    "Lre86_%=:\n"
    "s_waitcnt vmcnt(0)\n"
    "s_mov_b32 s49, 0\n"
    "s_mov_b32 s48, 3\n"
    "s_call_b64 s[34:35], Le65_%=\n"
    "s_cbranch_scc1 Lre86_%=\n"
    "s_branch Lok87_%=\n"
    "Lre88_%=:\n"
    "s_waitcnt vmcnt(0)\n"
    "s_mov_b32 s49, 0\n"
    "s_mov_b32 s48, 2\n"
    "s_call_b64 s[34:35], Le66_%=\n"
    "s_cbranch_scc1 Lre88_%=\n"
    "s_branch Lok89_%=\n"
    "Lre90_%=:\n"
    "s_waitcnt vmcnt(0)\n"
    "s_mov_b32 s49, 0\n"
    "s_mov_b32 s48, 1\n"
    "s_call_b64 s[34:35], Le67_%=\n"
    "s_cbranch_scc1 Lre90_%=\n"
    "s_branch Lok91_%=\n"
    "Lre92_%=:\n"
    "s_waitcnt vmcnt(0)\n"
    "s_mov_b32 s49, 0\n"
    "s_mov_b32 s48, 0\n"
    "s_call_b64 s[34:35], Le68_%=\n"
    "s_cbranch_scc1 Lre92_%=\n"
    "s_branch Lok93_%=\n"
    "Le97_%=:\n"
    "v_max3_u32 v15, v135, v129, v130\n"
    "v_max3_u32 v15, v15, v128, v131\n"
    "v_max3_u32 v15, v15, v132, v133\n"
    "v_max_u32 v15, v15, v134\n"
    "v_cmp_eq_u32 vcc, -1, v15\n"
    "s_cbranch_vccz Ls114_%=\n"
    "buffer_load_dwordx4 v[128:131], v0, s[24:27], s51 offen sc1\n"
    "buffer_load_dwordx4 v[132:135], v0, s[24:27], s51 offen offset:1024 sc1\n"
    "s_mov_b32 s49, 1\n"
    "Ls114_%=:\n"
    "s_sub_u32 s48, s48, 1\n"
    "s_cbranch_scc1 Ld113_%=\n"
    "Le98_%=:\n"
    "v_max3_u32 v15, v143, v137, v138\n"
    "v_max3_u32 v15, v15, v136, v139\n"
    "v_max3_u32 v15, v15, v140, v141\n"
    "v_max_u32 v15, v15, v142\n"
    "v_cmp_eq_u32 vcc, -1, v15\n"
    "s_cbranch_vccz Ls115_%=\n"
    "buffer_load_dwordx4 v[136:139], v0, s[24:27], s51 offen offset:2048 sc1\n"
    "buffer_load_dwordx4 v[140:143], v0, s[24:27], s51 offen offset:3072 sc1\n"
    "s_mov_b32 s49, 1\n"
    "Ls115_%=:\n"
    "s_sub_u32 s48, s48, 1\n"
    "s_cbranch_scc1 Ld113_%=\n"
    "Le99_%=:\n"
    "v_max3_u32 v15, v151, v145, v146\n"
    "v_max3_u32 v15, v15, v144, v147\n"
    "v_max3_u32 v15, v15, v148, v149\n"
    "v_max_u32 v15, v15, v150\n"
    "v_cmp_eq_u32 vcc, -1, v15\n"
    "s_cbranch_vccz Ls116_%=\n"
    "s_add_u32 s45, s51, 0x1000\n"
    "buffer_load_dwordx4 v[144:147], v0, s[24:27], s45 offen sc1\n"
    "buffer_load_dwordx4 v[148:151], v0, s[24:27], s45 offen offset:1024 sc1\n"
    "s_mov_b32 s49, 1\n"
    "Ls116_%=:\n"
    "s_sub_u32 s48, s48, 1\n"
    "s_cbranch_scc1 Ld113_%=\n"
    "Le100_%=:\n"
    "v_max3_u32 v15, v159, v153, v154\n"
    "v_max3_u32 v15, v15, v152, v155\n"
    "v_max3_u32 v15, v15, v156, v157\n"
    "v_max_u32 v15, v15, v158\n"
    "v_cmp_eq_u32 vcc, -1, v15\n"
    "s_cbranch_vccz Ls117_%=\n"
    "s_add_u32 s45, s51, 0x1000\n"
    "buffer_load_dwordx4 v[152:155], v0, s[24:27], s45 offen offset:2048 sc1\n"
    "buffer_load_dwordx4 v[156:159], v0, s[24:27], s45 offen offset:3072 sc1\n"
    "s_mov_b32 s49, 1\n"
    "Ls117_%=:\n"
    "s_sub_u32 s48, s48, 1\n"
    "s_cbranch_scc1 Ld113_%=\n"
    "Le101_%=:\n"
    "v_max3_u32 v15, v167, v161, v162\n"
    "v_max3_u32 v15, v15, v160, v163\n"
    "v_max3_u32 v15, v15, v164, v165\n"
    "v_max_u32 v15, v15, v166\n"
    "v_cmp_eq_u32 vcc, -1, v15\n"
    "s_cbranch_vccz Ls118_%=\n"
    "s_add_u32 s45, s51, 0x2000\n"
    "buffer_load_dwordx4 v[160:163], v0, s[24:27], s45 offen sc1\n"
    "buffer_load_dwordx4 v[164:167], v0, s[24:27], s45 offen offset:1024 sc1\n"
    "s_mov_b32 s49, 1\n"
    "Ls118_%=:\n"
    "s_sub_u32 s48, s48, 1\n"
    "s_cbranch_scc1 Ld113_%=\n"
    "Le102_%=:\n"
    "v_max3_u32 v15, v175, v169, v170\n"
    "v_max3_u32 v15, v15, v168, v171\n"
    "v_max3_u32 v15, v15, v172, v173\n"
    "v_max_u32 v15, v15, v174\n"
    "v_cmp_eq_u32 vcc, -1, v15\n"
    "s_cbranch_vccz Ls119_%=\n"
    "s_add_u32 s45, s51, 0x2000\n"
    "buffer_load_dwordx4 v[168:171], v0, s[24:27], s45 offen offset:2048 sc1\n"
    "buffer_load_dwordx4 v[172:175], v0, s[24:27], s45 offen offset:3072 sc1\n"
    "s_mov_b32 s49, 1\n"
    "Ls119_%=:\n"
    "s_sub_u32 s48, s48, 1\n"
    "s_cbranch_scc1 Ld113_%=\n"
    "Le103_%=:\n"
    "v_max3_u32 v15, v183, v177, v178\n"
    "v_max3_u32 v15, v15, v176, v179\n"
    "v_max3_u32 v15, v15, v180, v181\n"
    "v_max_u32 v15, v15, v182\n"
    "v_cmp_eq_u32 vcc, -1, v15\n"
    "s_cbranch_vccz Ls120_%=\n"
    "s_add_u32 s45, s51, 0x3000\n"
    "buffer_load_dwordx4 v[176:179], v0, s[24:27], s45 offen sc1\n"
    "buffer_load_dwordx4 v[180:183], v0, s[24:27], s45 offen offset:1024 sc1\n"
    "s_mov_b32 s49, 1\n"
    "Ls120_%=:\n"
    "s_sub_u32 s48, s48, 1\n"
    "s_cbranch_scc1 Ld113_%=\n"
    "Le104_%=:\n"
    "v_max3_u32 v15, v191, v185, v186\n"
    "v_max3_u32 v15, v15, v184, v187\n"
    "v_max3_u32 v15, v15, v188, v189\n"
    "v_max_u32 v15, v15, v190\n"
    "v_cmp_eq_u32 vcc, -1, v15\n"
    "s_cbranch_vccz Ls121_%=\n"
    "s_add_u32 s45, s51, 0x3000\n"
    "buffer_load_dwordx4 v[184:187], v0, s[24:27], s45 offen offset:2048 sc1\n"
    "buffer_load_dwordx4 v[188:191], v0, s[24:27], s45 offen offset:3072 sc1\n"
    "s_mov_b32 s49, 1\n"
    "Ls121_%=:\n"
    "s_sub_u32 s48, s48, 1\n"
    "s_cbranch_scc1 Ld113_%=\n"
    "Le105_%=:\n"
    "v_max3_u32 v15, v199, v193, v194\n"
    "v_max3_u32 v15, v15, v192, v195\n"
    "v_max3_u32 v15, v15, v196, v197\n"
    "v_max_u32 v15, v15, v198\n"
    "v_cmp_eq_u32 vcc, -1, v15\n"
    "s_cbranch_vccz Ls122_%=\n"
    "s_add_u32 s45, s51, 0x4000\n"
    "buffer_load_dwordx4 v[192:195], v0, s[24:27], s45 offen sc1\n"
    "buffer_load_dwordx4 v[196:199], v0, s[24:27], s45 offen offset:1024 sc1\n"
    "s_mov_b32 s49, 1\n"
    "Ls122_%=:\n"
    "s_sub_u32 s48, s48, 1\n"
    "s_cbranch_scc1 Ld113_%=\n"
    "Le106_%=:\n"
    "v_max3_u32 v15, v207, v201, v202\n"
    "v_max3_u32 v15, v15, v200, v203\n"
    "v_max3_u32 v15, v15, v204, v205\n"
    "v_max_u32 v15, v15, v206\n"
    "v_cmp_eq_u32 vcc, -1, v15\n"
    "s_cbranch_vccz Ls123_%=\n"
    "s_add_u32 s45, s51, 0x4000\n"
    "buffer_load_dwordx4 v[200:203], v0, s[24:27], s45 offen offset:2048 sc1\n"
    "buffer_load_dwordx4 v[204:207], v0, s[24:27], s45 offen offset:3072 sc1\n"
    "s_mov_b32 s49, 1\n"
    "Ls123_%=:\n"
    "s_sub_u32 s48, s48, 1\n"
    "s_cbranch_scc1 Ld113_%=\n"
    "Le107_%=:\n"
    "v_max3_u32 v15, v215, v209, v210\n"
    "v_max3_u32 v15, v15, v208, v211\n"
    "v_max3_u32 v15, v15, v212, v213\n"
    "v_max_u32 v15, v15, v214\n"
    "v_cmp_eq_u32 vcc, -1, v15\n"
    "s_cbranch_vccz Ls124_%=\n"
    "s_add_u32 s45, s51, 0x5000\n"
    "buffer_load_dwordx4 v[208:211], v0, s[24:27], s45 offen sc1\n"
    "buffer_load_dwordx4 v[212:215], v0, s[24:27], s45 offen offset:1024 sc1\n"
    "s_mov_b32 s49, 1\n"
    "Ls124_%=:\n"
    "s_sub_u32 s48, s48, 1\n"
    "s_cbranch_scc1 Ld113_%=\n"
    "Le108_%=:\n"
    "v_max3_u32 v15, v223, v217, v218\n"
    "v_max3_u32 v15, v15, v216, v219\n"
    "v_max3_u32 v15, v15, v220, v221\n"
    "v_max_u32 v15, v15, v222\n"
    "v_cmp_eq_u32 vcc, -1, v15\n"
    "s_cbranch_vccz Ls125_%=\n"
    "s_add_u32 s45, s51, 0x5000\n"
    "buffer_load_dwordx4 v[216:219], v0, s[24:27], s45 offen offset:2048 sc1\n"
    "buffer_load_dwordx4 v[220:223], v0, s[24:27], s45 offen offset:3072 sc1\n"
    "s_mov_b32 s49, 1\n"
    "Ls125_%=:\n"
    "s_sub_u32 s48, s48, 1\n"
    "s_cbranch_scc1 Ld113_%=\n"
    "Le109_%=:\n"
    "v_max3_u32 v15, v231, v225, v226\n"
    "v_max3_u32 v15, v15, v224, v227\n"
    "v_max3_u32 v15, v15, v228, v229\n"
    "v_max_u32 v15, v15, v230\n"
    "v_cmp_eq_u32 vcc, -1, v15\n"
    "s_cbranch_vccz Ls126_%=\n"
    "s_add_u32 s45, s51, 0x6000\n"
    "buffer_load_dwordx4 v[224:227], v0, s[24:27], s45 offen sc1\n"
    "buffer_load_dwordx4 v[228:231], v0, s[24:27], s45 offen offset:1024 sc1\n"
    "s_mov_b32 s49, 1\n"
    "Ls126_%=:\n"
    "s_sub_u32 s48, s48, 1\n"
    "s_cbranch_scc1 Ld113_%=\n"
    "Le110_%=:\n"
    "v_max3_u32 v15, v239, v233, v234\n"
    "v_max3_u32 v15, v15, v232, v235\n"
    "v_max3_u32 v15, v15, v236, v237\n"
    "v_max_u32 v15, v15, v238\n"
    "v_cmp_eq_u32 vcc, -1, v15\n"
    "s_cbranch_vccz Ls127_%=\n"
    "s_add_u32 s45, s51, 0x6000\n"
    "buffer_load_dwordx4 v[232:235], v0, s[24:27], s45 offen offset:2048 sc1\n"
    "buffer_load_dwordx4 v[236:239], v0, s[24:27], s45 offen offset:3072 sc1\n"
    "s_mov_b32 s49, 1\n"
    "Ls127_%=:\n"
    "s_sub_u32 s48, s48, 1\n"
    "s_cbranch_scc1 Ld113_%=\n"
    "Le111_%=:\n"
    "v_max3_u32 v15, v247, v241, v242\n"
    "v_max3_u32 v15, v15, v240, v243\n"
    "v_max3_u32 v15, v15, v244, v245\n"
    "v_max_u32 v15, v15, v246\n"
    "v_cmp_eq_u32 vcc, -1, v15\n"
    "s_cbranch_vccz Ls128_%=\n"
    "s_add_u32 s45, s51, 0x7000\n"
    "buffer_load_dwordx4 v[240:243], v0, s[24:27], s45 offen sc1\n"
    "buffer_load_dwordx4 v[244:247], v0, s[24:27], s45 offen offset:1024 sc1\n"
    "s_mov_b32 s49, 1\n"
    "Ls128_%=:\n"
    "s_sub_u32 s48, s48, 1\n"
    "s_cbranch_scc1 Ld113_%=\n"
    "Le112_%=:\n"
    "v_max3_u32 v15, v255, v249, v250\n"
    "v_max3_u32 v15, v15, v248, v251\n"
    "v_max3_u32 v15, v15, v252, v253\n"
    "v_max_u32 v15, v15, v254\n"
    "v_cmp_eq_u32 vcc, -1, v15\n"
    "s_cbranch_vccz Ls129_%=\n"
    "s_add_u32 s45, s51, 0x7000\n"
    "buffer_load_dwordx4 v[248:251], v0, s[24:27], s45 offen offset:2048 sc1\n"
    "buffer_load_dwordx4 v[252:255], v0, s[24:27], s45 offen offset:3072 sc1\n"
    "s_mov_b32 s49, 1\n"
    "Ls129_%=:\n"
    "Ld113_%=:\n"
    "s_sub_u32 s50, s50, s49\n"
    "s_cbranch_scc1 Lfail_%=\n"
    "s_cmp_lg_u32 s49, 0\n"
    "s_setpc_b64 s[34:35]\n"
    "Le130_%=:\n"
    "v_max3_u32 v15, v119, v113, v114\n"
    "v_max3_u32 v15, v15, v112, v115\n"
    "v_max3_u32 v15, v15, v116, v117\n"
    "v_max_u32 v15, v15, v118\n"
    "v_cmp_eq_u32 vcc, -1, v15\n"
    "s_cbranch_vccz Ls132_%=\n"
    "s_add_u32 s45, s51, 0x8000\n"
    "buffer_load_dwordx4 v[112:115], v0, s[24:27], s45 offen sc1\n"
    "buffer_load_dwordx4 v[116:119], v0, s[24:27], s45 offen offset:1024 sc1\n"
    "s_mov_b32 s49, 1\n"
    "Ls132_%=:\n"
    "Ld131_%=:\n"
    "s_sub_u32 s50, s50, s49\n"
    "s_cbranch_scc1 Lfail_%=\n"
    "s_cmp_lg_u32 s49, 0\n"
    "s_setpc_b64 s[34:35]\n"
    "Lre133_%=:\n"
    "s_waitcnt vmcnt(0)\n"
    "s_mov_b32 s49, 0\n"
    "s_mov_b32 s48, 5\n"
    "s_call_b64 s[34:35], Le97_%=\n"
    "s_cbranch_scc1 Lre133_%=\n"
    "s_branch Lok134_%=\n"
    "Lre135_%=:\n"
    "s_waitcnt vmcnt(0)\n"
    "s_mov_b32 s49, 0\n"
    "s_mov_b32 s48, 5\n"
    "s_call_b64 s[34:35], Le98_%=\n"
    "s_cbranch_scc1 Lre135_%=\n"
    "s_branch Lok136_%=\n"
    "Lre137_%=:\n"
    "s_waitcnt vmcnt(0)\n"
    "s_mov_b32 s49, 0\n"
    "s_mov_b32 s48, 5\n"
    "s_call_b64 s[34:35], Le99_%=\n"
    "s_cbranch_scc1 Lre137_%=\n"
    "s_branch Lok138_%=\n"
    "Lre139_%=:\n"
    "s_waitcnt vmcnt(0)\n"
    "s_mov_b32 s49, 0\n"
    "s_mov_b32 s48, 5\n"
    "s_call_b64 s[34:35], Le100_%=\n"
    "s_cbranch_scc1 Lre139_%=\n"
    "s_branch Lok140_%=\n"
    "Lre141_%=:\n"
    "s_waitcnt vmcnt(0)\n"
    "s_mov_b32 s49, 0\n"
    "s_mov_b32 s48, 5\n"
    "s_call_b64 s[34:35], Le101_%=\n"
    "s_cbranch_scc1 Lre141_%=\n"
    "s_branch Lok142_%=\n"
    "Lre143_%=:\n"
    "s_waitcnt vmcnt(0)\n"
    "s_mov_b32 s49, 0\n"
    "s_mov_b32 s48, 5\n"
    "s_call_b64 s[34:35], Le102_%=\n"
    "s_cbranch_scc1 Lre143_%=\n"
    "s_branch Lok144_%=\n"
    "Lre145_%=:\n"
    "s_waitcnt vmcnt(0)\n"
    "s_mov_b32 s49, 0\n"
    "s_mov_b32 s48, 5\n"
    "s_call_b64 s[34:35], Le103_%=\n"
    "s_cbranch_scc1 Lre145_%=\n"
    "s_branch Lok146_%=\n"
    "Lre147_%=:\n"
    "s_waitcnt vmcnt(0)\n"
    "s_mov_b32 s49, 0\n"
    "s_mov_b32 s48, 5\n"
    "s_call_b64 s[34:35], Le104_%=\n"
    "s_cbranch_scc1 Lre147_%=\n"
    "s_branch Lok148_%=\n"
    "Lre149_%=:\n"
    "s_waitcnt vmcnt(0)\n"
    "s_mov_b32 s49, 0\n"
    "s_mov_b32 s48, 5\n"
    "s_call_b64 s[34:35], Le105_%=\n"
    "s_cbranch_scc1 Lre149_%=\n"
    "s_branch Lok150_%=\n"
    "Lre151_%=:\n"
    "s_waitcnt vmcnt(0)\n"
    "s_mov_b32 s49, 0\n"
    "s_mov_b32 s48, 5\n"
    "s_call_b64 s[34:35], Le106_%=\n"
    "s_cbranch_scc1 Lre151_%=\n"
    "s_branch Lok152_%=\n"
    "Lre153_%=:\n"
    "s_waitcnt vmcnt(0)\n"
    "s_mov_b32 s49, 0\n"
    "s_mov_b32 s48, 5\n"
    "s_call_b64 s[34:35], Le107_%=\n"
    "s_cbranch_scc1 Lre153_%=\n"
    "s_branch Lok154_%=\n"
    "Lre155_%=:\n"
    "s_waitcnt vmcnt(0)\n"
    "s_mov_b32 s49, 0\n"
    "s_mov_b32 s48, 4\n"
    "s_call_b64 s[34:35], Le108_%=\n"
    "s_cbranch_scc1 Lre155_%=\n"
    "s_branch Lok156_%=\n"
    "Lre157_%=:\n"
    "s_waitcnt vmcnt(0)\n"
    "s_mov_b32 s49, 0\n"
    "s_mov_b32 s48, 3\n"
    "s_call_b64 s[34:35], Le109_%=\n"
    "s_cbranch_scc1 Lre157_%=\n"
    "s_branch Lok158_%=\n"
    "Lre159_%=:\n"
    "s_waitcnt vmcnt(0)\n"
    "s_mov_b32 s49, 0\n"
    "s_mov_b32 s48, 2\n"
    "s_call_b64 s[34:35], Le110_%=\n"
    "s_cbranch_scc1 Lre159_%=\n"
    "s_branch Lok160_%=\n"
    "Lre161_%=:\n"
    "s_waitcnt vmcnt(0)\n"
    "s_mov_b32 s49, 0\n"
    "s_mov_b32 s48, 1\n"
    "s_call_b64 s[34:35], Le111_%=\n"
    "s_cbranch_scc1 Lre161_%=\n"
    "s_branch Lok162_%=\n"
    "Lre163_%=:\n"
    "s_waitcnt vmcnt(0)\n"
    "s_mov_b32 s49, 0\n"
    "s_mov_b32 s48, 0\n"
    "s_call_b64 s[34:35], Le112_%=\n"
    "s_cbranch_scc1 Lre163_%=\n"
    "s_branch Lok164_%=\n"
    "Lre165_%=:\n"
    "s_waitcnt vmcnt(0)\n"
    "s_mov_b32 s49, 0\n"
    "s_mov_b32 s48, 0\n"
    "s_call_b64 s[34:35], Le130_%=\n"
    "s_cbranch_scc1 Lre165_%=\n"
    "s_branch Lok166_%=\n"
    "Le170_%=:\n"
    "v_max3_u32 v15, v199, v193, v194\n"
    "v_max3_u32 v15, v15, v192, v195\n"
    "v_max3_u32 v15, v15, v196, v197\n"
    "v_max_u32 v15, v15, v198\n"
    "v_cmp_eq_u32 vcc, -1, v15\n"
    "s_cbranch_vccz Ls185_%=\n"
    "s_add_u32 s45, s63, 0x2000\n"
    "buffer_load_dwordx4 v[192:195], v0, s[24:27], s45 offen sc1\n"
    "buffer_load_dwordx4 v[196:199], v0, s[24:27], s45 offen offset:1024 sc1\n"
    "s_mov_b32 s49, 1\n"
    "Ls185_%=:\n"
    "s_sub_u32 s48, s48, 1\n"
    "s_cbranch_scc1 Ld184_%=\n"
    "Le171_%=:\n"
    "v_max3_u32 v15, v207, v201, v202\n"
    "v_max3_u32 v15, v15, v200, v203\n"
    "v_max3_u32 v15, v15, v204, v205\n"
    "v_max_u32 v15, v15, v206\n"
    "v_cmp_eq_u32 vcc, -1, v15\n"
    "s_cbranch_vccz Ls186_%=\n"
    "s_add_u32 s45, s63, 0x2000\n"
    "buffer_load_dwordx4 v[200:203], v0, s[24:27], s45 offen offset:2048 sc1\n"
    "buffer_load_dwordx4 v[204:207], v0, s[24:27], s45 offen offset:3072 sc1\n"
    "s_mov_b32 s49, 1\n"
    "Ls186_%=:\n"
    "s_sub_u32 s48, s48, 1\n"
    "s_cbranch_scc1 Ld184_%=\n"
    "Le172_%=:\n"
    "v_max3_u32 v15, v215, v209, v210\n"
    "v_max3_u32 v15, v15, v208, v211\n"
    "v_max3_u32 v15, v15, v212, v213\n"
    "v_max_u32 v15, v15, v214\n"
    "v_cmp_eq_u32 vcc, -1, v15\n"
    "s_cbranch_vccz Ls187_%=\n"
    "s_add_u32 s45, s62, 0x2000\n"
    "buffer_load_dwordx4 v[208:211], v0, s[24:27], s45 offen sc1\n"
    "buffer_load_dwordx4 v[212:215], v0, s[24:27], s45 offen offset:1024 sc1\n"
    "s_mov_b32 s49, 1\n"
    "Ls187_%=:\n"
    "s_sub_u32 s48, s48, 1\n"
    "s_cbranch_scc1 Ld184_%=\n"
    "Le173_%=:\n"
    "v_max3_u32 v15, v223, v217, v218\n"
    "v_max3_u32 v15, v15, v216, v219\n"
    "v_max3_u32 v15, v15, v220, v221\n"
    "v_max_u32 v15, v15, v222\n"
    "v_cmp_eq_u32 vcc, -1, v15\n"
    "s_cbranch_vccz Ls188_%=\n"
    "s_add_u32 s45, s62, 0x2000\n"
    "buffer_load_dwordx4 v[216:219], v0, s[24:27], s45 offen offset:2048 sc1\n"
    "buffer_load_dwordx4 v[220:223], v0, s[24:27], s45 offen offset:3072 sc1\n"
    "s_mov_b32 s49, 1\n"
    "Ls188_%=:\n"
    "s_sub_u32 s48, s48, 1\n"
    "s_cbranch_scc1 Ld184_%=\n"
    "Le174_%=:\n"
    "v_max3_u32 v15, v231, v225, v226\n"
    "v_max3_u32 v15, v15, v224, v227\n"
    "v_max3_u32 v15, v15, v228, v229\n"
    "v_max_u32 v15, v15, v230\n"
    "v_cmp_eq_u32 vcc, -1, v15\n"
    "s_cbranch_vccz Ls189_%=\n"
    "s_add_u32 s45, s63, 0x3000\n"
    "buffer_load_dwordx4 v[224:227], v0, s[24:27], s45 offen sc1\n"
    "buffer_load_dwordx4 v[228:231], v0, s[24:27], s45 offen offset:1024 sc1\n"
    "s_mov_b32 s49, 1\n"
    "Ls189_%=:\n"
    "s_sub_u32 s48, s48, 1\n"
    "s_cbranch_scc1 Ld184_%=\n"
    "Le175_%=:\n"
    "v_max3_u32 v15, v239, v233, v234\n"
    "v_max3_u32 v15, v15, v232, v235\n"
    "v_max3_u32 v15, v15, v236, v237\n"
    "v_max_u32 v15, v15, v238\n"
    "v_cmp_eq_u32 vcc, -1, v15\n"
    "s_cbranch_vccz Ls190_%=\n"
    "s_add_u32 s45, s63, 0x3000\n"
    "buffer_load_dwordx4 v[232:235], v0, s[24:27], s45 offen offset:2048 sc1\n"
    "buffer_load_dwordx4 v[236:239], v0, s[24:27], s45 offen offset:3072 sc1\n"
    "s_mov_b32 s49, 1\n"
    "Ls190_%=:\n"
    "s_sub_u32 s48, s48, 1\n"
    "s_cbranch_scc1 Ld184_%=\n"
    "Le176_%=:\n"
    "v_max3_u32 v15, v247, v241, v242\n"
    "v_max3_u32 v15, v15, v240, v243\n"
    "v_max3_u32 v15, v15, v244, v245\n"
    "v_max_u32 v15, v15, v246\n"
    "v_cmp_eq_u32 vcc, -1, v15\n"
    "s_cbranch_vccz Ls191_%=\n"
    "s_add_u32 s45, s62, 0x3000\n"
    "buffer_load_dwordx4 v[240:243], v0, s[24:27], s45 offen sc1\n"
    "buffer_load_dwordx4 v[244:247], v0, s[24:27], s45 offen offset:1024 sc1\n"
    "s_mov_b32 s49, 1\n"
    "Ls191_%=:\n"
    "s_sub_u32 s48, s48, 1\n"
    "s_cbranch_scc1 Ld184_%=\n"
    "Le177_%=:\n"
    "v_max3_u32 v15, v255, v249, v250\n"
    "v_max3_u32 v15, v15, v248, v251\n"
    "v_max3_u32 v15, v15, v252, v253\n"
    "v_max_u32 v15, v15, v254\n"
    "v_cmp_eq_u32 vcc, -1, v15\n"
    "s_cbranch_vccz Ls192_%=\n"
    "s_add_u32 s45, s62, 0x3000\n"
    "buffer_load_dwordx4 v[248:251], v0, s[24:27], s45 offen offset:2048 sc1\n"
    "buffer_load_dwordx4 v[252:255], v0, s[24:27], s45 offen offset:3072 sc1\n"
    "s_mov_b32 s49, 1\n"
    "Ls192_%=:\n"
    "s_sub_u32 s48, s48, 1\n"
    "s_cbranch_scc1 Ld184_%=\n"
    "Le178_%=:\n"
    "v_max3_u32 v15, v135, v129, v130\n"
    "v_max3_u32 v15, v15, v128, v131\n"
    "v_max3_u32 v15, v15, v132, v133\n"
    "v_max_u32 v15, v15, v134\n"
    "v_cmp_eq_u32 vcc, -1, v15\n"
    "s_cbranch_vccz Ls193_%=\n"
    "s_add_u32 s45, s63, 0x4000\n"
    "buffer_load_dwordx4 v[128:131], v0, s[24:27], s45 offen sc1\n"
    "buffer_load_dwordx4 v[132:135], v0, s[24:27], s45 offen offset:1024 sc1\n"
    "s_mov_b32 s49, 1\n"
    "Ls193_%=:\n"
    "s_sub_u32 s48, s48, 1\n"
    "s_cbranch_scc1 Ld184_%=\n"
    "Le179_%=:\n"
    "v_max3_u32 v15, v143, v137, v138\n"
    "v_max3_u32 v15, v15, v136, v139\n"
    "v_max3_u32 v15, v15, v140, v141\n"
    "v_max_u32 v15, v15, v142\n"
    "v_cmp_eq_u32 vcc, -1, v15\n"
    "s_cbranch_vccz Ls194_%=\n"
    "s_add_u32 s45, s63, 0x4000\n"
    "buffer_load_dwordx4 v[136:139], v0, s[24:27], s45 offen offset:2048 sc1\n"
    "buffer_load_dwordx4 v[140:143], v0, s[24:27], s45 offen offset:3072 sc1\n"
    "s_mov_b32 s49, 1\n"
    "Ls194_%=:\n"
    "s_sub_u32 s48, s48, 1\n"
    "s_cbranch_scc1 Ld184_%=\n"
    "Le180_%=:\n"
    "v_max3_u32 v15, v151, v145, v146\n"
    "v_max3_u32 v15, v15, v144, v147\n"
    "v_max3_u32 v15, v15, v148, v149\n"
    "v_max_u32 v15, v15, v150\n"
    "v_cmp_eq_u32 vcc, -1, v15\n"
    "s_cbranch_vccz Ls195_%=\n"
    "s_add_u32 s45, s63, 0x5000\n"
    "buffer_load_dwordx4 v[144:147], v0, s[24:27], s45 offen sc1\n"
    "buffer_load_dwordx4 v[148:151], v0, s[24:27], s65 offen sc1\n"
    "s_mov_b32 s49, 1\n"
    "Ls195_%=:\n"
    "s_sub_u32 s48, s48, 1\n"
    "s_cbranch_scc1 Ld184_%=\n"
    "Le181_%=:\n"
    "v_max3_u32 v15, v159, v153, v154\n"
    "v_max3_u32 v15, v15, v152, v155\n"
    "v_max3_u32 v15, v15, v156, v157\n"
    "v_max_u32 v15, v15, v158\n"
    "v_cmp_eq_u32 vcc, -1, v15\n"
    "s_cbranch_vccz Ls196_%=\n"
    "s_add_u32 s45, s62, 0x4000\n"
    "buffer_load_dwordx4 v[152:155], v0, s[24:27], s45 offen sc1\n"
    "buffer_load_dwordx4 v[156:159], v0, s[24:27], s45 offen offset:1024 sc1\n"
    "s_mov_b32 s49, 1\n"
    "Ls196_%=:\n"
    "s_sub_u32 s48, s48, 1\n"
    "s_cbranch_scc1 Ld184_%=\n"
    "Le182_%=:\n"
    "v_max3_u32 v15, v167, v161, v162\n"
    "v_max3_u32 v15, v15, v160, v163\n"
    "v_max3_u32 v15, v15, v164, v165\n"
    "v_max_u32 v15, v15, v166\n"
    "v_cmp_eq_u32 vcc, -1, v15\n"
    "s_cbranch_vccz Ls197_%=\n"
    "s_add_u32 s45, s62, 0x4000\n"
    "buffer_load_dwordx4 v[160:163], v0, s[24:27], s45 offen offset:2048 sc1\n"
    "buffer_load_dwordx4 v[164:167], v0, s[24:27], s45 offen offset:3072 sc1\n"
    "s_mov_b32 s49, 1\n"
    "Ls197_%=:\n"
    "s_sub_u32 s48, s48, 1\n"
    "s_cbranch_scc1 Ld184_%=\n"
    "Le183_%=:\n"
    "v_max3_u32 v15, v175, v169, v170\n"
    "v_max3_u32 v15, v15, v168, v171\n"
    "v_max3_u32 v15, v15, v172, v173\n"
    "v_max_u32 v15, v15, v174\n"
    "v_cmp_eq_u32 vcc, -1, v15\n"
    "s_cbranch_vccz Ls198_%=\n"
    "s_add_u32 s45, s62, 0x5000\n"
    "buffer_load_dwordx4 v[168:171], v0, s[24:27], s45 offen sc1\n"
    "buffer_load_dwordx4 v[172:175], v0, s[24:27], s64 offen sc1\n"
    "s_mov_b32 s49, 1\n"
    "Ls198_%=:\n"
    "Ld184_%=:\n"
    "s_sub_u32 s50, s50, s49\n"
    "s_cbranch_scc1 Lfail_%=\n"
    "s_cmp_lg_u32 s49, 0\n"
    "s_setpc_b64 s[34:35]\n"
    "Lre199_%=:\n"
    "s_waitcnt vmcnt(0)\n"
    "s_mov_b32 s49, 0\n"
    "s_mov_b32 s48, 6\n"
    "s_call_b64 s[34:35], Le170_%=\n"
    "s_cbranch_scc1 Lre199_%=\n"
    "s_branch Lok200_%=\n"
    "Lre201_%=:\n"
    "s_waitcnt vmcnt(0)\n"
    "s_mov_b32 s49, 0\n"
    "s_mov_b32 s48, 6\n"
    "s_call_b64 s[34:35], Le171_%=\n"
    "s_cbranch_scc1 Lre201_%=\n"
    "s_branch Lok202_%=\n"
    "Lre203_%=:\n"
    "s_waitcnt vmcnt(0)\n"
    "s_mov_b32 s49, 0\n"
    "s_mov_b32 s48, 6\n"
    "s_call_b64 s[34:35], Le172_%=\n"
    "s_cbranch_scc1 Lre203_%=\n"
    "s_branch Lok204_%=\n"
    "Lre205_%=:\n"
    "s_waitcnt vmcnt(0)\n"
    "s_mov_b32 s49, 0\n"
    "s_mov_b32 s48, 6\n"
    "s_call_b64 s[34:35], Le173_%=\n"
    "s_cbranch_scc1 Lre205_%=\n"
    "s_branch Lok206_%=\n"
    "Lre207_%=:\n"
    "s_waitcnt vmcnt(0)\n"
    "s_mov_b32 s49, 0\n"
    "s_mov_b32 s48, 5\n"
    "s_call_b64 s[34:35], Le174_%=\n"
    "s_cbranch_scc1 Lre207_%=\n"
    "s_branch Lok208_%=\n"
    "Lre209_%=:\n"
    "s_waitcnt vmcnt(0)\n"
    "s_mov_b32 s49, 0\n"
    "s_mov_b32 s48, 4\n"
    "s_call_b64 s[34:35], Le175_%=\n"
    "s_cbranch_scc1 Lre209_%=\n"
    "s_branch Lok210_%=\n"
    "Lre211_%=:\n"
    "s_waitcnt vmcnt(0)\n"
    "s_mov_b32 s49, 0\n"
    "s_mov_b32 s48, 4\n"
    "s_call_b64 s[34:35], Le176_%=\n"
    "s_cbranch_scc1 Lre211_%=\n"
    "s_branch Lok212_%=\n"
    "Lre213_%=:\n"
    "s_waitcnt vmcnt(0)\n"
    "s_mov_b32 s49, 0\n"
    "s_mov_b32 s48, 4\n"
    "s_call_b64 s[34:35], Le177_%=\n"
    "s_cbranch_scc1 Lre213_%=\n"
    "s_branch Lok214_%=\n"
    "Lre215_%=:\n"
    "s_waitcnt vmcnt(0)\n"
    "s_mov_b32 s49, 0\n"
    "s_mov_b32 s48, 4\n"
    "s_call_b64 s[34:35], Le178_%=\n"
    "s_cbranch_scc1 Lre215_%=\n"
    "s_branch Lok216_%=\n"
    "Lre217_%=:\n"
    "s_waitcnt vmcnt(0)\n"
    "s_mov_b32 s49, 0\n"
    "s_mov_b32 s48, 4\n"
    "s_call_b64 s[34:35], Le179_%=\n"
    "s_cbranch_scc1 Lre217_%=\n"
    "s_branch Lok218_%=\n"
    "Lre219_%=:\n"
    "s_waitcnt vmcnt(0)\n"
    "s_mov_b32 s49, 0\n"
    "s_mov_b32 s48, 3\n"
    "s_call_b64 s[34:35], Le180_%=\n"
    "s_cbranch_scc1 Lre219_%=\n"
    "s_branch Lok220_%=\n"
    "Lre221_%=:\n"
    "s_waitcnt vmcnt(0)\n"
    "s_mov_b32 s49, 0\n"
    "s_mov_b32 s48, 2\n"
    "s_call_b64 s[34:35], Le181_%=\n"
    "s_cbranch_scc1 Lre221_%=\n"
    "s_branch Lok222_%=\n"
    "Lre223_%=:\n"
    "s_waitcnt vmcnt(0)\n"
    "s_mov_b32 s49, 0\n"
    "s_mov_b32 s48, 1\n"
    "s_call_b64 s[34:35], Le182_%=\n"
    "s_cbranch_scc1 Lre223_%=\n"
    "s_branch Lok224_%=\n"
    "Lre225_%=:\n"
    "s_waitcnt vmcnt(0)\n"
    "s_mov_b32 s49, 0\n"
    "s_mov_b32 s48, 0\n"
    "s_call_b64 s[34:35], Le183_%=\n"
    "s_cbranch_scc1 Lre225_%=\n"
    "s_branch Lok226_%=\n"
    "Lre230_%=:\n"
    "s_waitcnt vmcnt(0)\n"
    "s_mov_b32 s49, 0\n"
    "s_mov_b32 s48, 5\n"
    "s_call_b64 s[34:35], Le97_%=\n"
    "s_cbranch_scc1 Lre230_%=\n"
    "s_branch Lok231_%=\n"
    "Lre232_%=:\n"
    "s_waitcnt vmcnt(0)\n"
    "s_mov_b32 s49, 0\n"
    "s_mov_b32 s48, 5\n"
    "s_call_b64 s[34:35], Le98_%=\n"
    "s_cbranch_scc1 Lre232_%=\n"
    "s_branch Lok233_%=\n"
    "Lre234_%=:\n"
    "s_waitcnt vmcnt(0)\n"
    "s_mov_b32 s49, 0\n"
    "s_mov_b32 s48, 5\n"
    "s_call_b64 s[34:35], Le99_%=\n"
    "s_cbranch_scc1 Lre234_%=\n"
    "s_branch Lok235_%=\n"
    "Lre236_%=:\n"
    "s_waitcnt vmcnt(0)\n"
    "s_mov_b32 s49, 0\n"
    "s_mov_b32 s48, 5\n"
    "s_call_b64 s[34:35], Le100_%=\n"
    "s_cbranch_scc1 Lre236_%=\n"
    "s_branch Lok237_%=\n"
    "Lre238_%=:\n"
    "s_waitcnt vmcnt(0)\n"
    "s_mov_b32 s49, 0\n"
    "s_mov_b32 s48, 5\n"
    "s_call_b64 s[34:35], Le101_%=\n"
    "s_cbranch_scc1 Lre238_%=\n"
    "s_branch Lok239_%=\n"
    "Lre240_%=:\n"
    "s_waitcnt vmcnt(0)\n"
    "s_mov_b32 s49, 0\n"
    "s_mov_b32 s48, 5\n"
    "s_call_b64 s[34:35], Le102_%=\n"
    "s_cbranch_scc1 Lre240_%=\n"
    "s_branch Lok241_%=\n"
    "Lre242_%=:\n"
    "s_waitcnt vmcnt(0)\n"
    "s_mov_b32 s49, 0\n"
    "s_mov_b32 s48, 5\n"
    "s_call_b64 s[34:35], Le103_%=\n"
    "s_cbranch_scc1 Lre242_%=\n"
    "s_branch Lok243_%=\n"
    "Lre244_%=:\n"
    "s_waitcnt vmcnt(0)\n"
    "s_mov_b32 s49, 0\n"
    "s_mov_b32 s48, 5\n"
    "s_call_b64 s[34:35], Le104_%=\n"
    "s_cbranch_scc1 Lre244_%=\n"
    "s_branch Lok245_%=\n"
    "Lre246_%=:\n"
    "s_waitcnt vmcnt(0)\n"
    "s_mov_b32 s49, 0\n"
    "s_mov_b32 s48, 5\n"
    "s_call_b64 s[34:35], Le105_%=\n"
    "s_cbranch_scc1 Lre246_%=\n"
    "s_branch Lok247_%=\n"
    "Lre248_%=:\n"
    "s_waitcnt vmcnt(0)\n"
    "s_mov_b32 s49, 0\n"
    "s_mov_b32 s48, 5\n"
    "s_call_b64 s[34:35], Le106_%=\n"
    "s_cbranch_scc1 Lre248_%=\n"
    "s_branch Lok249_%=\n"
    "Lre250_%=:\n"
    "s_waitcnt vmcnt(0)\n"
    "s_mov_b32 s49, 0\n"
    "s_mov_b32 s48, 5\n"
    "s_call_b64 s[34:35], Le107_%=\n"
    "s_cbranch_scc1 Lre250_%=\n"
    "s_branch Lok251_%=\n"
    "Lre252_%=:\n"
    "s_waitcnt vmcnt(0)\n"
    "s_mov_b32 s49, 0\n"
    "s_mov_b32 s48, 4\n"
    "s_call_b64 s[34:35], Le108_%=\n"
    "s_cbranch_scc1 Lre252_%=\n"
    "s_branch Lok253_%=\n"
    "Lre254_%=:\n"
    "s_waitcnt vmcnt(0)\n"
    "s_mov_b32 s49, 0\n"
    "s_mov_b32 s48, 3\n"
    "s_call_b64 s[34:35], Le109_%=\n"
    "s_cbranch_scc1 Lre254_%=\n"
    "s_branch Lok255_%=\n"
    "Lre256_%=:\n"
    "s_waitcnt vmcnt(0)\n"
    "s_mov_b32 s49, 0\n"
    "s_mov_b32 s48, 2\n"
    "s_call_b64 s[34:35], Le110_%=\n"
    "s_cbranch_scc1 Lre256_%=\n"
    "s_branch Lok257_%=\n"
    "Lre258_%=:\n"
    "s_waitcnt vmcnt(0)\n"
    "s_mov_b32 s49, 0\n"
    "s_mov_b32 s48, 1\n"
    "s_call_b64 s[34:35], Le111_%=\n"
    "s_cbranch_scc1 Lre258_%=\n"
    "s_branch Lok259_%=\n"
    "Lre260_%=:\n"
    "s_waitcnt vmcnt(0)\n"
    "s_mov_b32 s49, 0\n"
    "s_mov_b32 s48, 0\n"
    "s_call_b64 s[34:35], Le112_%=\n"
    "s_cbranch_scc1 Lre260_%=\n"
    "s_branch Lok261_%=\n"
    "Lre262_%=:\n"
    "s_waitcnt vmcnt(0)\n"
    "s_mov_b32 s49, 0\n"
    "s_mov_b32 s48, 0\n"
    "s_call_b64 s[34:35], Le130_%=\n"
    "s_cbranch_scc1 Lre262_%=\n"
    "s_branch Lok263_%=\n"
    "Lre268_%=:\n"
    "s_waitcnt vmcnt(0)\n"
    "s_mov_b32 s49, 0\n"
    "s_mov_b32 s48, 8\n"
    "s_call_b64 s[34:35], Le36_%=\n"
    "s_cbranch_scc0 Lok269_%=\n"
    "Lag270_%=:\n"
    "s_waitcnt vmcnt(0)\n"
    "s_mov_b32 s49, 0\n"
    "s_mov_b32 s48, 8\n"
    "s_call_b64 s[34:35], Le36_%=\n"
    "s_cbranch_scc1 Lag270_%=\n"
    "s_branch Lkv267_%=\n"
    "Lre274_%=:\n"
    "s_waitcnt vmcnt(0)\n"
    "s_mov_b32 s49, 0\n"
    "s_mov_b32 s48, 5\n"
    "s_call_b64 s[34:35], Le61_%=\n"
    "s_cbranch_scc1 Lre274_%=\n"
    "s_branch Lok275_%=\n"
    "Lre276_%=:\n"
    "s_waitcnt vmcnt(0)\n"
    "s_mov_b32 s49, 0\n"
    "s_mov_b32 s48, 5\n"
    "s_call_b64 s[34:35], Le62_%=\n"
    "s_cbranch_scc1 Lre276_%=\n"
    "s_branch Lok277_%=\n"
    "Lre278_%=:\n"
    "s_waitcnt vmcnt(0)\n"
    "s_mov_b32 s49, 0\n"
    "s_mov_b32 s48, 5\n"
    "s_call_b64 s[34:35], Le63_%=\n"
    "s_cbranch_scc1 Lre278_%=\n"
    "s_branch Lok279_%=\n"
    "Lre280_%=:\n"
    "s_waitcnt vmcnt(0)\n"
    "s_mov_b32 s49, 0\n"
    "s_mov_b32 s48, 4\n"
    "s_call_b64 s[34:35], Le64_%=\n"
    "s_cbranch_scc1 Lre280_%=\n"
    "s_branch Lok281_%=\n"
    "Lre282_%=:\n"
    "s_waitcnt vmcnt(0)\n"
    "s_mov_b32 s49, 0\n"
    "s_mov_b32 s48, 3\n"
    "s_call_b64 s[34:35], Le65_%=\n"
    "s_cbranch_scc1 Lre282_%=\n"
    "s_branch Lok283_%=\n"
    "Lre284_%=:\n"
    "s_waitcnt vmcnt(0)\n"
    "s_mov_b32 s49, 0\n"
    "s_mov_b32 s48, 2\n"
    "s_call_b64 s[34:35], Le66_%=\n"
    "s_cbranch_scc1 Lre284_%=\n"
    "s_branch Lok285_%=\n"
    "Lre286_%=:\n"
    "s_waitcnt vmcnt(0)\n"
    "s_mov_b32 s49, 0\n"
    "s_mov_b32 s48, 1\n"
    "s_call_b64 s[34:35], Le67_%=\n"
    "s_cbranch_scc1 Lre286_%=\n"
    "s_branch Lok287_%=\n"
    "Lre288_%=:\n"
    "s_waitcnt vmcnt(0)\n"
    "s_mov_b32 s49, 0\n"
    "s_mov_b32 s48, 0\n"
    "s_call_b64 s[34:35], Le68_%=\n"
    "s_cbranch_scc1 Lre288_%=\n"
    "s_branch Lok289_%=\n"
    "Lre293_%=:\n"
    "s_waitcnt vmcnt(0)\n"
    "s_mov_b32 s49, 0\n"
    "s_mov_b32 s48, 5\n"
    "s_call_b64 s[34:35], Le97_%=\n"
    "s_cbranch_scc1 Lre293_%=\n"
    "s_branch Lok294_%=\n"
    "Lre295_%=:\n"
    "s_waitcnt vmcnt(0)\n"
    "s_mov_b32 s49, 0\n"
    "s_mov_b32 s48, 5\n"
    "s_call_b64 s[34:35], Le98_%=\n"
    "s_cbranch_scc1 Lre295_%=\n"
    "s_branch Lok296_%=\n"
    "Lre297_%=:\n"
    "s_waitcnt vmcnt(0)\n"
    "s_mov_b32 s49, 0\n"
    "s_mov_b32 s48, 5\n"
    "s_call_b64 s[34:35], Le99_%=\n"
    "s_cbranch_scc1 Lre297_%=\n"
    "s_branch Lok298_%=\n"
    "Lre299_%=:\n"
    "s_waitcnt vmcnt(0)\n"
    "s_mov_b32 s49, 0\n"
    "s_mov_b32 s48, 5\n"
    "s_call_b64 s[34:35], Le100_%=\n"
    "s_cbranch_scc1 Lre299_%=\n"
    "s_branch Lok300_%=\n"
    "Lre301_%=:\n"
    "s_waitcnt vmcnt(0)\n"
    "s_mov_b32 s49, 0\n"
    "s_mov_b32 s48, 5\n"
    "s_call_b64 s[34:35], Le101_%=\n"
    "s_cbranch_scc1 Lre301_%=\n"
    "s_branch Lok302_%=\n"
    "Lre303_%=:\n"
    "s_waitcnt vmcnt(0)\n"
    "s_mov_b32 s49, 0\n"
    "s_mov_b32 s48, 5\n"
    "s_call_b64 s[34:35], Le102_%=\n"
    "s_cbranch_scc1 Lre303_%=\n"
    "s_branch Lok304_%=\n"
    "Lre305_%=:\n"
    "s_waitcnt vmcnt(0)\n"
    "s_mov_b32 s49, 0\n"
    "s_mov_b32 s48, 5\n"
    "s_call_b64 s[34:35], Le103_%=\n"
    "s_cbranch_scc1 Lre305_%=\n"
    "s_branch Lok306_%=\n"
    "Lre307_%=:\n"
    "s_waitcnt vmcnt(0)\n"
    "s_mov_b32 s49, 0\n"
    "s_mov_b32 s48, 5\n"
    "s_call_b64 s[34:35], Le104_%=\n"
    "s_cbranch_scc1 Lre307_%=\n"
    "s_branch Lok308_%=\n"
    "Lre309_%=:\n"
    "s_waitcnt vmcnt(0)\n"
    "s_mov_b32 s49, 0\n"
    "s_mov_b32 s48, 5\n"
    "s_call_b64 s[34:35], Le105_%=\n"
    "s_cbranch_scc1 Lre309_%=\n"
    "s_branch Lok310_%=\n"
    "Lre311_%=:\n"
    "s_waitcnt vmcnt(0)\n"
    "s_mov_b32 s49, 0\n"
    "s_mov_b32 s48, 5\n"
    "s_call_b64 s[34:35], Le106_%=\n"
    "s_cbranch_scc1 Lre311_%=\n"
    "s_branch Lok312_%=\n"
    "Lre313_%=:\n"
    "s_waitcnt vmcnt(0)\n"
    "s_mov_b32 s49, 0\n"
    "s_mov_b32 s48, 5\n"
    "s_call_b64 s[34:35], Le107_%=\n"
    "s_cbranch_scc1 Lre313_%=\n"
    "s_branch Lok314_%=\n"
    "Lre315_%=:\n"
    "s_waitcnt vmcnt(0)\n"
    "s_mov_b32 s49, 0\n"
    "s_mov_b32 s48, 4\n"
    "s_call_b64 s[34:35], Le108_%=\n"
    "s_cbranch_scc1 Lre315_%=\n"
    "s_branch Lok316_%=\n"
    "Lre317_%=:\n"
    "s_waitcnt vmcnt(0)\n"
    "s_mov_b32 s49, 0\n"
    "s_mov_b32 s48, 3\n"
    "s_call_b64 s[34:35], Le109_%=\n"
    "s_cbranch_scc1 Lre317_%=\n"
    "s_branch Lok318_%=\n"
    "Lre319_%=:\n"
    "s_waitcnt vmcnt(0)\n"
    "s_mov_b32 s49, 0\n"
    "s_mov_b32 s48, 2\n"
    "s_call_b64 s[34:35], Le110_%=\n"
    "s_cbranch_scc1 Lre319_%=\n"
    "s_branch Lok320_%=\n"
    "Lre321_%=:\n"
    "s_waitcnt vmcnt(0)\n"
    "s_mov_b32 s49, 0\n"
    "s_mov_b32 s48, 1\n"
    "s_call_b64 s[34:35], Le111_%=\n"
    "s_cbranch_scc1 Lre321_%=\n"
    "s_branch Lok322_%=\n"
    "Lre323_%=:\n"
    "s_waitcnt vmcnt(0)\n"
    "s_mov_b32 s49, 0\n"
    "s_mov_b32 s48, 0\n"
    "s_call_b64 s[34:35], Le112_%=\n"
    "s_cbranch_scc1 Lre323_%=\n"
    "s_branch Lok324_%=\n"
    "Lre325_%=:\n"
    "s_waitcnt vmcnt(0)\n"
    "s_mov_b32 s49, 0\n"
    "s_mov_b32 s48, 0\n"
    "s_call_b64 s[34:35], Le130_%=\n"
    "s_cbranch_scc1 Lre325_%=\n"
    "s_branch Lok326_%=\n"
    "Le330_%=:\n"
    "v_max3_u32 v15, v199, v193, v194\n"
    "v_max3_u32 v15, v15, v192, v195\n"
    "v_max3_u32 v15, v15, v196, v197\n"
    "v_max_u32 v15, v15, v198\n"
    "v_cmp_eq_u32 vcc, -1, v15\n"
    "s_cbranch_vccz Ls345_%=\n"
    "s_add_u32 s45, s63, 0x4000\n"
    "buffer_load_dwordx4 v[192:195], v0, s[24:27], s45 offen sc1\n"
    "buffer_load_dwordx4 v[196:199], v0, s[24:27], s45 offen offset:1024 sc1\n"
    "s_mov_b32 s49, 1\n"
    "Ls345_%=:\n"
    "s_sub_u32 s48, s48, 1\n"
    "s_cbranch_scc1 Ld344_%=\n"
    "Le331_%=:\n"
    "v_max3_u32 v15, v207, v201, v202\n"
    "v_max3_u32 v15, v15, v200, v203\n"
    "v_max3_u32 v15, v15, v204, v205\n"
    "v_max_u32 v15, v15, v206\n"
    "v_cmp_eq_u32 vcc, -1, v15\n"
    "s_cbranch_vccz Ls346_%=\n"
    "s_add_u32 s45, s63, 0x4000\n"
    "buffer_load_dwordx4 v[200:203], v0, s[24:27], s45 offen offset:2048 sc1\n"
    "buffer_load_dwordx4 v[204:207], v0, s[24:27], s45 offen offset:3072 sc1\n"
    "s_mov_b32 s49, 1\n"
    "Ls346_%=:\n"
    "s_sub_u32 s48, s48, 1\n"
    "s_cbranch_scc1 Ld344_%=\n"
    "Le332_%=:\n"
    "v_max3_u32 v15, v215, v209, v210\n"
    "v_max3_u32 v15, v15, v208, v211\n"
    "v_max3_u32 v15, v15, v212, v213\n"
    "v_max_u32 v15, v15, v214\n"
    "v_cmp_eq_u32 vcc, -1, v15\n"
    "s_cbranch_vccz Ls347_%=\n"
    "s_add_u32 s45, s62, 0x4000\n"
    "buffer_load_dwordx4 v[208:211], v0, s[24:27], s45 offen sc1\n"
    "buffer_load_dwordx4 v[212:215], v0, s[24:27], s45 offen offset:1024 sc1\n"
    "s_mov_b32 s49, 1\n"
    "Ls347_%=:\n"
    "s_sub_u32 s48, s48, 1\n"
    "s_cbranch_scc1 Ld344_%=\n"
    "Le333_%=:\n"
    "v_max3_u32 v15, v223, v217, v218\n"
    "v_max3_u32 v15, v15, v216, v219\n"
    "v_max3_u32 v15, v15, v220, v221\n"
    "v_max_u32 v15, v15, v222\n"
    "v_cmp_eq_u32 vcc, -1, v15\n"
    "s_cbranch_vccz Ls348_%=\n"
    "s_add_u32 s45, s62, 0x4000\n"
    "buffer_load_dwordx4 v[216:219], v0, s[24:27], s45 offen offset:2048 sc1\n"
    "buffer_load_dwordx4 v[220:223], v0, s[24:27], s45 offen offset:3072 sc1\n"
    "s_mov_b32 s49, 1\n"
    "Ls348_%=:\n"
    "s_sub_u32 s48, s48, 1\n"
    "s_cbranch_scc1 Ld344_%=\n"
    "Le334_%=:\n"
    "v_max3_u32 v15, v231, v225, v226\n"
    "v_max3_u32 v15, v15, v224, v227\n"
    "v_max3_u32 v15, v15, v228, v229\n"
    "v_max_u32 v15, v15, v230\n"
    "v_cmp_eq_u32 vcc, -1, v15\n"
    "s_cbranch_vccz Ls349_%=\n"
    "buffer_load_dwordx4 v[224:227], v0, s[24:27], s63 offen sc1\n"
    "buffer_load_dwordx4 v[228:231], v0, s[24:27], s63 offen offset:1024 sc1\n"
    "s_mov_b32 s49, 1\n"
    "Ls349_%=:\n"
    "s_sub_u32 s48, s48, 1\n"
    "s_cbranch_scc1 Ld344_%=\n"
    "Le335_%=:\n"
    "v_max3_u32 v15, v239, v233, v234\n"
    "v_max3_u32 v15, v15, v232, v235\n"
    "v_max3_u32 v15, v15, v236, v237\n"
    "v_max_u32 v15, v15, v238\n"
    "v_cmp_eq_u32 vcc, -1, v15\n"
    "s_cbranch_vccz Ls350_%=\n"
    "buffer_load_dwordx4 v[232:235], v0, s[24:27], s63 offen offset:2048 sc1\n"
    "buffer_load_dwordx4 v[236:239], v0, s[24:27], s63 offen offset:3072 sc1\n"
    "s_mov_b32 s49, 1\n"
    "Ls350_%=:\n"
    "s_sub_u32 s48, s48, 1\n"
    "s_cbranch_scc1 Ld344_%=\n"
    "Le336_%=:\n"
    "v_max3_u32 v15, v247, v241, v242\n"
    "v_max3_u32 v15, v15, v240, v243\n"
    "v_max3_u32 v15, v15, v244, v245\n"
    "v_max_u32 v15, v15, v246\n"
    "v_cmp_eq_u32 vcc, -1, v15\n"
    "s_cbranch_vccz Ls351_%=\n"
    "buffer_load_dwordx4 v[240:243], v0, s[24:27], s62 offen sc1\n"
    "buffer_load_dwordx4 v[244:247], v0, s[24:27], s62 offen offset:1024 sc1\n"
    "s_mov_b32 s49, 1\n"
    "Ls351_%=:\n"
    "s_sub_u32 s48, s48, 1\n"
    "s_cbranch_scc1 Ld344_%=\n"
    "Le337_%=:\n"
    "v_max3_u32 v15, v255, v249, v250\n"
    "v_max3_u32 v15, v15, v248, v251\n"
    "v_max3_u32 v15, v15, v252, v253\n"
    "v_max_u32 v15, v15, v254\n"
    "v_cmp_eq_u32 vcc, -1, v15\n"
    "s_cbranch_vccz Ls352_%=\n"
    "buffer_load_dwordx4 v[248:251], v0, s[24:27], s62 offen offset:2048 sc1\n"
    "buffer_load_dwordx4 v[252:255], v0, s[24:27], s62 offen offset:3072 sc1\n"
    "s_mov_b32 s49, 1\n"
    "Ls352_%=:\n"
    "s_sub_u32 s48, s48, 1\n"
    "s_cbranch_scc1 Ld344_%=\n"
    "Le338_%=:\n"
    "v_max3_u32 v15, v135, v129, v130\n"
    "v_max3_u32 v15, v15, v128, v131\n"
    "v_max3_u32 v15, v15, v132, v133\n"
    "v_max_u32 v15, v15, v134\n"
    "v_cmp_eq_u32 vcc, -1, v15\n"
    "s_cbranch_vccz Ls353_%=\n"
    "s_add_u32 s45, s63, 0x2000\n"
    "buffer_load_dwordx4 v[128:131], v0, s[24:27], s45 offen sc1\n"
    "buffer_load_dwordx4 v[132:135], v0, s[24:27], s45 offen offset:1024 sc1\n"
    "s_mov_b32 s49, 1\n"
    "Ls353_%=:\n"
    "s_sub_u32 s48, s48, 1\n"
    "s_cbranch_scc1 Ld344_%=\n"
    "Le339_%=:\n"
    "v_max3_u32 v15, v143, v137, v138\n"
    "v_max3_u32 v15, v15, v136, v139\n"
    "v_max3_u32 v15, v15, v140, v141\n"
    "v_max_u32 v15, v15, v142\n"
    "v_cmp_eq_u32 vcc, -1, v15\n"
    "s_cbranch_vccz Ls354_%=\n"
    "s_add_u32 s45, s63, 0x2000\n"
    "buffer_load_dwordx4 v[136:139], v0, s[24:27], s45 offen offset:2048 sc1\n"
    "buffer_load_dwordx4 v[140:143], v0, s[24:27], s45 offen offset:3072 sc1\n"
    "s_mov_b32 s49, 1\n"
    "Ls354_%=:\n"
    "s_sub_u32 s48, s48, 1\n"
    "s_cbranch_scc1 Ld344_%=\n"
    "Le340_%=:\n"
    "v_max3_u32 v15, v151, v145, v146\n"
    "v_max3_u32 v15, v15, v144, v147\n"
    "v_max3_u32 v15, v15, v148, v149\n"
    "v_max_u32 v15, v15, v150\n"
    "v_cmp_eq_u32 vcc, -1, v15\n"
    "s_cbranch_vccz Ls355_%=\n"
    "s_add_u32 s45, s63, 0x5000\n"
    "buffer_load_dwordx4 v[144:147], v0, s[24:27], s45 offen sc1\n"
    "buffer_load_dwordx4 v[148:151], v0, s[24:27], s65 offen sc1\n"
    "s_mov_b32 s49, 1\n"
    "Ls355_%=:\n"
    "s_sub_u32 s48, s48, 1\n"
    "s_cbranch_scc1 Ld344_%=\n"
    "Le341_%=:\n"
    "v_max3_u32 v15, v159, v153, v154\n"
    "v_max3_u32 v15, v15, v152, v155\n"
    "v_max3_u32 v15, v15, v156, v157\n"
    "v_max_u32 v15, v15, v158\n"
    "v_cmp_eq_u32 vcc, -1, v15\n"
    "s_cbranch_vccz Ls356_%=\n"
    "s_add_u32 s45, s62, 0x2000\n"
    "buffer_load_dwordx4 v[152:155], v0, s[24:27], s45 offen sc1\n"
    "buffer_load_dwordx4 v[156:159], v0, s[24:27], s45 offen offset:1024 sc1\n"
    "s_mov_b32 s49, 1\n"
    "Ls356_%=:\n"
    "s_sub_u32 s48, s48, 1\n"
    "s_cbranch_scc1 Ld344_%=\n"
    "Le342_%=:\n"
    "v_max3_u32 v15, v167, v161, v162\n"
    "v_max3_u32 v15, v15, v160, v163\n"
    "v_max3_u32 v15, v15, v164, v165\n"
    "v_max_u32 v15, v15, v166\n"
    "v_cmp_eq_u32 vcc, -1, v15\n"
    "s_cbranch_vccz Ls357_%=\n"
    "s_add_u32 s45, s62, 0x2000\n"
    "buffer_load_dwordx4 v[160:163], v0, s[24:27], s45 offen offset:2048 sc1\n"
    "buffer_load_dwordx4 v[164:167], v0, s[24:27], s45 offen offset:3072 sc1\n"
    "s_mov_b32 s49, 1\n"
    "Ls357_%=:\n"
    "s_sub_u32 s48, s48, 1\n"
    "s_cbranch_scc1 Ld344_%=\n"
    "Le343_%=:\n"
    "v_max3_u32 v15, v175, v169, v170\n"
    "v_max3_u32 v15, v15, v168, v171\n"
    "v_max3_u32 v15, v15, v172, v173\n"
    "v_max_u32 v15, v15, v174\n"
    "v_cmp_eq_u32 vcc, -1, v15\n"
    "s_cbranch_vccz Ls358_%=\n"
    "s_add_u32 s45, s62, 0x5000\n"
    "buffer_load_dwordx4 v[168:171], v0, s[24:27], s45 offen sc1\n"
    "buffer_load_dwordx4 v[172:175], v0, s[24:27], s64 offen sc1\n"
    "s_mov_b32 s49, 1\n"
    "Ls358_%=:\n"
    "Ld344_%=:\n"
    "s_sub_u32 s50, s50, s49\n"
    "s_cbranch_scc1 Lfail_%=\n"
    "s_cmp_lg_u32 s49, 0\n"
    "s_setpc_b64 s[34:35]\n"
    "Lre359_%=:\n"
    "s_waitcnt vmcnt(0)\n"
    "s_mov_b32 s49, 0\n"
    "s_mov_b32 s48, 6\n"
    "s_call_b64 s[34:35], Le330_%=\n"
    "s_cbranch_scc1 Lre359_%=\n"
    "s_branch Lok360_%=\n"
    "Lre361_%=:\n"
    "s_waitcnt vmcnt(0)\n"
    "s_mov_b32 s49, 0\n"
    "s_mov_b32 s48, 6\n"
    "s_call_b64 s[34:35], Le331_%=\n"
    "s_cbranch_scc1 Lre361_%=\n"
    "s_branch Lok362_%=\n"
    "Lre363_%=:\n"
    "s_waitcnt vmcnt(0)\n"
    "s_mov_b32 s49, 0\n"
    "s_mov_b32 s48, 6\n"
    "s_call_b64 s[34:35], Le332_%=\n"
    "s_cbranch_scc1 Lre363_%=\n"
    "s_branch Lok364_%=\n"
    "Lre365_%=:\n"
    "s_waitcnt vmcnt(0)\n"
    "s_mov_b32 s49, 0\n"
    "s_mov_b32 s48, 6\n"
    "s_call_b64 s[34:35], Le333_%=\n"
    "s_cbranch_scc1 Lre365_%=\n"
    "s_branch Lok366_%=\n"
    "Lre367_%=:\n"
    "s_waitcnt vmcnt(0)\n"
    "s_mov_b32 s49, 0\n"
    "s_mov_b32 s48, 5\n"
    "s_call_b64 s[34:35], Le334_%=\n"
    "s_cbranch_scc1 Lre367_%=\n"
    "s_branch Lok368_%=\n"
    "Lre369_%=:\n"
    "s_waitcnt vmcnt(0)\n"
    "s_mov_b32 s49, 0\n"
    "s_mov_b32 s48, 5\n"
    "s_call_b64 s[34:35], Le335_%=\n"
    "s_cbranch_scc1 Lre369_%=\n"
    "s_branch Lok370_%=\n"
    "Lre371_%=:\n"
    "s_waitcnt vmcnt(0)\n"
    "s_mov_b32 s49, 0\n"
    "s_mov_b32 s48, 5\n"
    "s_call_b64 s[34:35], Le336_%=\n"
    "s_cbranch_scc1 Lre371_%=\n"
    "s_branch Lok372_%=\n"
    "Lre373_%=:\n"
    "s_waitcnt vmcnt(0)\n"
    "s_mov_b32 s49, 0\n"
    "s_mov_b32 s48, 5\n"
    "s_call_b64 s[34:35], Le337_%=\n"
    "s_cbranch_scc1 Lre373_%=\n"
    "s_branch Lok374_%=\n"
    "Lre375_%=:\n"
    "s_waitcnt vmcnt(0)\n"
    "s_mov_b32 s49, 0\n"
    "s_mov_b32 s48, 5\n"
    "s_call_b64 s[34:35], Le338_%=\n"
    "s_cbranch_scc1 Lre375_%=\n"
    "s_branch Lok376_%=\n"
    "Lre377_%=:\n"
    "s_waitcnt vmcnt(0)\n"
    "s_mov_b32 s49, 0\n"
    "s_mov_b32 s48, 4\n"
    "s_call_b64 s[34:35], Le339_%=\n"
    "s_cbranch_scc1 Lre377_%=\n"
    "s_branch Lok378_%=\n"
    "Lre379_%=:\n"
    "s_waitcnt vmcnt(0)\n"
    "s_mov_b32 s49, 0\n"
    "s_mov_b32 s48, 3\n"
    "s_call_b64 s[34:35], Le340_%=\n"
    "s_cbranch_scc1 Lre379_%=\n"
    "s_branch Lok380_%=\n"
    "Lre381_%=:\n"
    "s_waitcnt vmcnt(0)\n"
    "s_mov_b32 s49, 0\n"
    "s_mov_b32 s48, 2\n"
    "s_call_b64 s[34:35], Le341_%=\n"
    "s_cbranch_scc1 Lre381_%=\n"
    "s_branch Lok382_%=\n"
    "Lre383_%=:\n"
    "s_waitcnt vmcnt(0)\n"
    "s_mov_b32 s49, 0\n"
    "s_mov_b32 s48, 1\n"
    "s_call_b64 s[34:35], Le342_%=\n"
    "s_cbranch_scc1 Lre383_%=\n"
    "s_branch Lok384_%=\n"
    "Lre385_%=:\n"
    "s_waitcnt vmcnt(0)\n"
    "s_mov_b32 s49, 0\n"
    "s_mov_b32 s48, 0\n"
    "s_call_b64 s[34:35], Le343_%=\n"
    "s_cbranch_scc1 Lre385_%=\n"
    "s_branch Lok386_%=\n"
    "Lend_%=:\n"
    :
    : "s"(__builtin_amdgcn_kernarg_segment_ptr()), "s"(blockIdx.x), "v"(tid), "s"((unsigned)(size_t)(__attribute__((address_space(3))) u8*)smem)
    : "v1", "v2", "v3", "v4", "v5", "v6", "v7", "v8", "v9", "v10", "v11", "v12", "v13", "v14", "v15", "v16", "v17", "v18", "v19", "v20", "v21", "v22", "v23", "v24", "v25", "v26", "v27", "v28", "v29", "v30", "v31", "v32", "v33", "v34", "v35", "v36", "v37", "v38", "v39", "v40", "v41", "v42", "v43", "v44", "v45", "v46", "v47", "v48", "v49", "v50", "v51", "v52", "v53", "v54", "v55", "v56", "v57", "v58", "v59", "v60", "v61", "v62", "v63", "v64", "v65", "v66", "v67", "v68", "v69", "v70", "v71", "v72", "v73", "v74", "v75", "v76", "v77", "v78", "v79", "v80", "v81", "v82", "v83", "v84", "v85", "v86", "v87", "v88", "v89", "v90", "v91", "v92", "v93", "v94", "v95", "v96", "v97", "v98", "v99", "v100", "v101", "v102", "v103", "v104", "v105", "v106", "v107", "v108", "v109", "v110", "v111", "v112", "v113", "v114", "v115", "v116", "v117", "v118", "v119", "v120", "v121", "v122", "v123", "v124", "v125", "v126", "v127", "v128", "v129", "v130", "v131", "v132", "v133", "v134", "v135", "v136", "v137", "v138", "v139", "v140", "v141", "v142", "v143", "v144", "v145", "v146", "v147", "v148", "v149", "v150", "v151", "v152", "v153", "v154", "v155", "v156", "v157", "v158", "v159", "v160", "v161", "v162", "v163", "v164", "v165", "v166", "v167", "v168", "v169", "v170", "v171", "v172", "v173", "v174", "v175", "v176", "v177", "v178", "v179", "v180", "v181", "v182", "v183", "v184", "v185", "v186", "v187", "v188", "v189", "v190", "v191", "v192", "v193", "v194", "v195", "v196", "v197", "v198", "v199", "v200", "v201", "v202", "v203", "v204", "v205", "v206", "v207", "v208", "v209", "v210", "v211", "v212", "v213", "v214", "v215", "v216", "v217", "v218", "v219", "v220", "v221", "v222", "v223", "v224", "v225", "v226", "v227", "v228", "v229", "v230", "v231", "v232", "v233", "v234", "v235", "v236", "v237", "v238", "v239", "v240", "v241", "v242", "v243", "v244", "v245", "v246", "v247", "v248", "v249", "v250", "v251", "v252", "v253", "v254", "v255", "a0", "a1", "a2", "a3", "a4", "a5", "a6", "a7", "a8", "a9", "a10", "a11", "a12", "a13", "a14", "a15", "a16", "a17", "a18", "a19", "a20", "a21", "a22", "a23", "a24", "a25", "a26", "a27", "a28", "a29", "a30", "a31", "a32", "a33", "a34", "a35", "a36", "a37", "a38", "a39", "a40", "a41", "a42", "a43", "a44", "a45", "a46", "a47", "a48", "a49", "a50", "a51", "a52", "a53", "a54", "a55", "a56", "a57", "a58", "a59", "a60", "a61", "a62", "a63", "a64", "a65", "a66", "a67", "a68", "a69", "a70", "a71", "a72", "a73", "a74", "a75", "a76", "a77", "a78", "a79", "a80", "a81", "a82", "a83", "a84", "a85", "a86", "a87", "a88", "a89", "a90", "a91", "a92", "a93", "a94", "a95", "a96", "a97", "a98", "a99", "a100", "a101", "a102", "a103", "a104", "a105", "a106", "a107", "a108", "a109", "a110", "a111", "a112", "a113", "a114", "a115", "a116", "a117", "a118", "a119", "a120", "a121", "a122", "a123", "a124", "a125", "a126", "a127", "a128", "a129", "a130", "a131", "a132", "a133", "a134", "a135", "a136", "a137", "a138", "a139", "a140", "a141", "a142", "a143", "a144", "a145", "a146", "a147", "a148", "a149", "a150", "a151", "a152", "a153", "a154", "a155", "a156", "a157", "a158", "a159", "a160", "a161", "a162", "a163", "a164", "a165", "a166", "a167", "a168", "a169", "a170", "a171", "a172", "a173", "a174", "a175", "a176", "a177", "a178", "a179", "a180", "a181", "a182", "a183", "a184", "a185", "a186", "a187", "a188", "a189", "a190", "a191", "a192", "a193", "a194", "a195", "a196", "a197", "a198", "a199", "a200", "a201", "a202", "a203", "a204", "a205", "a206", "a207", "a208", "a209", "a210", "a211", "a212", "a213", "a214", "a215", "a216", "a217", "a218", "a219", "a220", "a221", "a222", "a223", "a224", "a225", "a226", "a227", "a228", "a229", "a230", "a231", "a232", "a233", "a234", "a235", "a236", "a237", "a238", "a239", "a240", "a241", "a242", "a243", "a244", "a245", "a246", "a247", "a248", "a249", "a250", "a251", "a252", "a253", "a254", "a255", "s8", "s9", "s10", "s11", "s12", "s13", "s14", "s15", "s16", "s17", "s18", "s19", "s20", "s21", "s22", "s23", "s24", "s25", "s26", "s27", "s28", "s29", "s30", "s31", "s33", "s34", "s35", "s36", "s37", "s38", "s39", "s40", "s41", "s42", "s43", "s44", "s45", "s46", "s47", "s48", "s49", "s50", "s51", "s52", "s53", "s54", "s55", "s56", "s57", "s58", "s59", "s60", "s61", "s62", "s63", "s64", "s65", "s66", "s67", "s68", "s69", "s70", "s71", "s72", "s73", "s74", "s75", "s76", "s77", "s78", "s79", "s80", "s81", "s82", "s83", "s84", "s85", "s86", "s87", "s88", "s89", "s90", "s91", "s92", "s93", "s94", "s95", "s96", "s97", "s98", "s99", "vcc", "scc", "memory");
}

extern "C" void kernel_launch(void* const* d_in, const int* in_sizes, int n_in, void* d_out, int out_size, void* d_ws, size_t ws_size,
                              hipStream_t stream) {
  fused_block<<<256, 256, 0, stream>>>((const u16*)d_in[0], (const u16*)d_in[1], (const u16*)d_in[2], (const u16*)d_in[3], (const u16*)d_in[5],
                                        (const u16*)d_in[6], (const u16*)d_in[7], (u16*)d_out, (u8*)d_ws);
}
```
